```python
import torch

def _softmax(x, axis=-1):
    return torch.softmax(x, dim=axis)
def _gelu(x):
    return torch.nn.functional.gelu(x, approximate="tanh")
def _silu(x):
    return torch.nn.functional.silu(x)
def _sigmoid(x):
    return torch.sigmoid(x)
def _layernorm(x, axis=-1, eps=1e-5):
    m = x.mean(dim=axis, keepdim=True)
    v = x.var(dim=axis, keepdim=True, unbiased=False)
    return (x - m) / torch.sqrt(v + eps)
def _scatter_add(tgt, idx, src, axis=0):
    return tgt.clone().index_add_(axis, idx, src)
def _segment_sum(src, seg, n):
    out = torch.zeros((n,) + src.shape[1:], dtype=src.dtype)
    return out.index_add_(0, seg, src)
def _pad(x, pw):
    flat = tuple(v for pair in reversed(pw) for v in pair)
    return torch.nn.functional.pad(x, flat)

kernel_name = 'gated_mix__linear_attn_block__spectral_gfnet__prompt'

def setup_inputs():

    _w = (lambda a: (lambda t: t.to(torch.bfloat16) if t.is_floating_point() else t)(torch.as_tensor(a)))
    def _A_setup():
        B, S, D = 1, 2048, 512
        return {
            "x": _w((lambda *s: torch.randn(*s, dtype=torch.bfloat16) * 0.05)(B, S, D)),
            "wqkv": _w((lambda *s: torch.randn(*s, dtype=torch.bfloat16) * 0.4)(D, 3 * D)),
            "wo": _w((lambda *s: torch.randn(*s, dtype=torch.bfloat16) * 0.05)(D, D)),
        }
    _A_in = _A_setup()

    def _B_setup():
        B, S, D = 1, 2048, 512
        Df = D // 2 + 1
        return {
            "x": _w((lambda *s: torch.randn(*s, dtype=torch.bfloat16) * 0.05)(B, S, D)),
            "w_r": _w((lambda *s: torch.randn(*s, dtype=torch.bfloat16) * 1.6)(S, Df) * 0.02),
            "w_i": _w((lambda *s: torch.randn(*s, dtype=torch.bfloat16) * 1.6)(S, Df) * 0.02),
        }
    _B_in = _B_setup()

    return {
        "A_x": _A_in["x"],
        "A_wqkv": _A_in["wqkv"],
        "A_wo": _A_in["wo"],
        "B_x": _B_in["x"],
        "B_w_r": _B_in["w_r"],
        "B_w_i": _B_in["w_i"],
        "gate_w": _w((lambda *s: torch.randn(*s, dtype=torch.bfloat16) * 0.05)(512) * 16.0),
    }

def reference(A_x, A_wqkv, A_wo, B_x, B_w_r, B_w_i, gate_w):
    def _A_ref(x, wqkv, wo):
        B, S, D = x.shape
        H = 32; Dh = D // H
        qkv = (lambda x, *d: x.permute(*d))((x @ wqkv).reshape(B, S, 3, H, Dh), 2, 0, 3, 1, 4)
        q, k, v = qkv[0], qkv[1], qkv[2]
        phi = lambda t: torch.where(t > 0, t + 1, torch.exp(t)) + 1e-6
        pq, pk = phi(q), phi(k)
        kv = torch.einsum("bhsd,bhse->bhde", pk, v)
        z = torch.einsum("bhsd->bhd", pk)
        num = torch.einsum("bhqd,bhde->bhqe", pq, kv)
        den = torch.einsum("bhqd,bhd->bhq", pq, z)[..., None] + 1e-6
        o = num / den
        return (lambda x, *d: x.permute(*d))(o, 0, 2, 1, 3).reshape(B, S, D) @ wo

    def _B_ref(x, w_r, w_i):
        B, S, D = x.shape
        X = (lambda x, axes=(-2,-1): torch.fft.rfft2(x.float(), dim=axes))(x, axes=(-2, -1))
        W = w_r + 1j * w_i
        X = X * W
        y = (lambda x, s, axes=(-2,-1): torch.fft.irfft2(x, s=s, dim=axes).to(torch.bfloat16))(X, (S, D), axes=(-2, -1))
        return y

    x = A_x
    g = _sigmoid((x * gate_w).sum(-1, keepdims=True))
    a = _A_ref(x, A_wqkv, A_wo)
    b = _B_ref(x, B_w_r, B_w_i)
    return g * a + (1 - g) * b
```

```cpp
#include <hip/hip_runtime.h>
#include <hip/hip_bf16.h>
typedef __hip_bfloat16 bf16;
static constexpr int S=2048, D=512, H=32, DH=16, DF=257;
__device__ __forceinline__ float b2f(bf16 v){return __bfloat162float(v);}
__device__ __forceinline__ float rbf(float v){return __bfloat162float(__float2bfloat16(v));}

__global__ void k_qkv(const bf16* x,const bf16* w,float* qkv){
  int n=blockIdx.x*256+threadIdx.x; int s=blockIdx.y;
  float acc=0; for(int k=0;k<D;k++) acc+=b2f(x[s*D+k])*b2f(w[k*1536+n]);
  float v=rbf(acc);
  if(n<1024){ float p = v>0? rbf(v+1.f): rbf(expf(v)); v=rbf(p+1e-6f);}
  qkv[s*1536+n]=v;
}
__global__ void k_kv(const float* qkv,float* kv,float* z){
  int h=blockIdx.x; int t=threadIdx.x; int d=t>>4,e=t&15;
  float acc=0,zz=0;
  for(int s=0;s<S;s++){ float pk=qkv[s*1536+512+h*16+d]; acc+=pk*qkv[s*1536+1024+h*16+e]; zz+=pk;}
  kv[h*256+t]=rbf(acc); if(e==0) z[h*16+d]=rbf(zz);
}
__global__ void k_o(const float* qkv,const float* kv,const float* z,float* o){
  int s=blockIdx.x; int c=threadIdx.x; int h=c>>4,e=c&15;
  float num=0,den=0;
  for(int d=0;d<16;d++){float pq=qkv[s*1536+h*16+d]; num+=pq*kv[h*256+d*16+e]; den+=pq*z[h*16+d];}
  num=rbf(num); den=rbf(rbf(den)+1e-6f);
  o[s*512+c]=rbf(num/den);
}
__global__ void k_tab(float2* t2048){ int i=blockIdx.x*256+threadIdx.x; if(i<2048){ double a=-2.0*M_PI*i/2048.0; t2048[i]=make_float2((float)cos(a),(float)sin(a));}}
__global__ void k_rowdft(const bf16* x,const float2* tab,float2* X1){
  int s=blockIdx.x; int k=threadIdx.x; if(k>=DF) return;
  float re=0,im=0; for(int n=0;n<D;n++){ float2 w=tab[((n*k)&511)*4]; float v=b2f(x[s*D+n]); re+=v*w.x; im+=v*w.y;}
  X1[k*S+s]=make_float2(re,im);
}
__global__ void k_coldft(const float2* X1,const float2* tab,const bf16* wr,const bf16* wi,float2* X2){
  int k=blockIdx.y; int ks=blockIdx.x*256+threadIdx.x;
  float re=0,im=0; for(int s=0;s<S;s++){ float2 w=tab[(s*ks)&2047]; float2 v=X1[k*S+s]; re+=v.x*w.x-v.y*w.y; im+=v.x*w.y+v.y*w.x;}
  float a=b2f(wr[ks*DF+k]), b=b2f(wi[ks*DF+k]);
  X2[k*S+ks]=make_float2(re*a-im*b, re*b+im*a);
}
__global__ void k_colidft(const float2* X2,const float2* tab,float2* Z){
  int k=blockIdx.y; int s=blockIdx.x*256+threadIdx.x;
  float re=0,im=0; for(int ks=0;ks<S;ks++){ float2 w=tab[(s*ks)&2047]; w.y=-w.y; float2 v=X2[k*S+ks]; re+=v.x*w.x-v.y*w.y; im+=v.x*w.y+v.y*w.x;}
  Z[k*S+s]=make_float2(re,im);
}
__global__ void k_final(const bf16* x,const bf16* gw,const float* o,const bf16* wo,const float2* Z,const float2* tab,bf16* out){
  int s=blockIdx.x; int n=threadIdx.x;
  __shared__ float red[512];
  red[n]=rbf(b2f(x[s*D+n])*b2f(gw[n])); __syncthreads();
  float gs=0; for(int i=0;i<512;i++) gs+=red[i];
  gs=rbf(gs); float g=rbf(1.f/(1.f+expf(-gs)));
  float a=0; for(int k=0;k<D;k++) a+=o[s*512+k]*b2f(wo[k*512+n]); a=rbf(a);
  float y=Z[0*S+s].x + ((n&1)?-1.f:1.f)*Z[256*S+s].x;
  for(int k=1;k<256;k++){ float2 w=tab[((n*k)&511)*4]; float2 v=Z[k*S+s]; y+=2.f*(v.x*w.x+v.y*w.y);}
  y*=1.f/(2048.f*512.f); y=rbf(y);
  out[s*D+n]=__float2bfloat16(rbf(g*a)+rbf(rbf(1.f-g)*y));
}
extern "C" void kernel_launch(void* const* d_in, const int* in_sizes, int n_in, void* d_out, int out_size, void* d_ws, size_t ws_size, hipStream_t stream) {
  const bf16* x=(const bf16*)d_in[0]; const bf16* wqkv=(const bf16*)d_in[1]; const bf16* wo=(const bf16*)d_in[2];
  const bf16* wr=(const bf16*)d_in[4]; const bf16* wi=(const bf16*)d_in[5]; const bf16* gw=(const bf16*)d_in[6];
  char* ws=(char*)d_ws;
  float* qkv=(float*)ws; ws+= (size_t)S*1536*4;
  float* kv=(float*)ws; ws+= 32*256*4;
  float* z=(float*)ws; ws+= 512*4;
  float* o=(float*)ws; ws+= (size_t)S*512*4;
  float2* tab=(float2*)ws; ws+=2048*8;
  float2* X1=(float2*)ws; ws+=(size_t)DF*S*8;
  float2* X2=(float2*)ws; ws+=(size_t)DF*S*8;
  float2* Z=(float2*)ws; ws+=(size_t)DF*S*8;
  k_tab<<<8,256,0,stream>>>(tab);
  k_qkv<<<dim3(6,S),256,0,stream>>>(x,wqkv,qkv);
  k_kv<<<32,256,0,stream>>>(qkv,kv,z);
  k_o<<<S,512,0,stream>>>(qkv,kv,z,o);
  k_rowdft<<<S,320,0,stream>>>(x,tab,X1);
  k_coldft<<<dim3(8,DF),256,0,stream>>>(X1,tab,wr,wi,X2);
  k_colidft<<<dim3(8,DF),256,0,stream>>>(X2,tab,Z);
  k_final<<<S,512,0,stream>>>(x,gw,o,wo,Z,tab,(bf16*)d_out);
}
```

```cpp
#include <hip/hip_runtime.h>
#include <stdint.h>

typedef short s16x4 __attribute__((ext_vector_type(4)));
typedef short s16x8 __attribute__((ext_vector_type(8)));
typedef float f32x4 __attribute__((ext_vector_type(4)));
typedef float f32x2 __attribute__((ext_vector_type(2)));
typedef unsigned u32x4 __attribute__((ext_vector_type(4)));
typedef unsigned u32x2 __attribute__((ext_vector_type(2)));
typedef __bf16 bfx2 __attribute__((ext_vector_type(2)));
#define LAS __attribute__((address_space(3)))
#define GAS __attribute__((address_space(1)))
#define DI __device__ __forceinline__

static constexpr size_t WS_X1T = 0;
static constexpr size_t WS_WT  = 2u << 20;
static constexpr size_t WS_PQ  = WS_WT + (9u << 18);
static constexpr size_t WS_KVP = WS_PQ + (2u << 20);
static constexpr size_t WS_ZP  = WS_KVP + (512u << 10);
static constexpr size_t WS_GT  = WS_ZP + (32u << 10);
static constexpr size_t WS_GF  = WS_GT + (16u << 10);
static constexpr size_t WS_OF  = WS_GF + (512u << 10);
static constexpr size_t WS_YT  = WS_OF + (2u << 20);

DI unsigned pk(float lo, float hi) { f32x2 p = {lo, hi}; return __builtin_bit_cast(unsigned, __builtin_convertvector(p, bfx2)); }
DI float bflo(unsigned u) { return __builtin_bit_cast(float, u << 16); }
DI float bfhi(unsigned u) { return __builtin_bit_cast(float, u & 0xffff0000u); }
DI float rbf(float v) { return bflo(pk(v, 0.f)); }
DI s16x8 as8(u32x4 v) { return __builtin_bit_cast(s16x8, v); }
DI s16x4 as4(u32x2 v) { return __builtin_bit_cast(s16x4, v); }
DI f32x4 mma(s16x8 a, s16x8 b, f32x4 c) { return __builtin_amdgcn_mfma_f32_16x16x32_bf16(a, b, c, 0, 0, 0); }
DI f32x4 mma16(s16x4 a, s16x4 b, f32x4 c) { return __builtin_amdgcn_mfma_f32_16x16x16bf16_1k(a, b, c, 0, 0, 0); }
DI float fsin(float rev) { return __builtin_amdgcn_sinf(rev); }
DI float fcos(float rev) { return __builtin_amdgcn_cosf(rev); }
DI void glds16(const char* g, char* l) { __builtin_amdgcn_global_load_lds((const GAS unsigned*)g, (LAS unsigned*)l, 16, 0, 0); }
DI void glds4(unsigned laddr, const char* sb, unsigned v0, unsigned v1, unsigned v2, unsigned v3) {
  asm volatile("s_mov_b32 m0, %0\n\ts_nop 0\n\t"
               "global_load_lds_dwordx4 %2, %1\n\t"
               "global_load_lds_dwordx4 %3, %1 offset:1024\n\t"
               "global_load_lds_dwordx4 %4, %1 offset:2048\n\t"
               "global_load_lds_dwordx4 %5, %1 offset:3072"
               :: "s"(laddr), "s"(sb), "v"(v0), "v"(v1), "v"(v2), "v"(v3) : "memory");
}
DI unsigned laddr_of(const char* l) { return (unsigned)(size_t)(const LAS char*)l; }
DI s16x8 trd2(const char* l0, const char* l1) {
  s16x4 a = __builtin_amdgcn_ds_read_tr16_b64_v4i16((LAS s16x4*)l0);
  s16x4 b = __builtin_amdgcn_ds_read_tr16_b64_v4i16((LAS s16x4*)l1);
  return __builtin_shufflevector(a, b, 0, 1, 2, 3, 4, 5, 6, 7);
}
DI s16x8 lrd(const char* l) { return *(const s16x8*)l; }
DI int perm32(int t, int i) { return 8 * (i >> 2) + 4 * t + (i & 3); }
DI float phi(float t) {
  float e = __builtin_amdgcn_exp2f(t * 1.44269504088896341f);
  e = __builtin_amdgcn_fmed3f(e, 0.f, 1.f);
  return (fmaxf(t, 0.f) + 1e-6f) + e;
}
#define WAITV(n) asm volatile("s_waitcnt vmcnt(" #n ")" ::: "memory")
#define BAR() asm volatile("s_waitcnt lgkmcnt(0)\n\ts_barrier" ::: "memory")

template <bool QT>
DI void k1_gemm(char* lds, const char* x, const char* wqkv, char* ws, int m, int nt, int rot, int w, int lane) {
  const int i = lane & 15, g = lane >> 4, q = i >> 2, p = lane & 3;
  const int col0 = QT ? (nt - 8) * 128 : 512 + nt * 64;
  const int col1 = QT ? col0 + 64 : col0 + 512;
  unsigned voffA[4], voffB[4];
#pragma unroll
  for (int j = 0; j < 4; j++) {
    const int par = j & 1;
    voffA[j] = (lane >> 3) * 1024 + (((lane & 7) ^ ((4 * par + (lane >> 4)) & 7)) * 16) + j * (8192 - 1024);
    voffB[j] = (lane >> 3) * 3072 + ((((lane & 7) >> 1) ^ (((lane >> 4) & 1) | (par << 1))) * 32) + (lane & 1) * 16 + j * (24576 - 1024);
  }
  const unsigned lbase = laddr_of(lds) + w * 4096;
  const char* xa = x + (size_t)(m * 128 + w * 32) * 1024;
  const char* wb = wqkv + (size_t)((w & 1) * 32) * 3072 + ((w >> 1) ? col1 : col0) * 2;
  auto issue = [&](int idx) {
    const int ks = (idx + rot) & 7;
    const unsigned slot = lbase + (idx & 3) * 32768;
    glds4(slot, xa + ks * 128, voffA[0], voffA[1], voffA[2], voffA[3]);
    glds4(slot + 16384, wb + (size_t)(ks * 64) * 3072, voffB[0], voffB[1], voffB[2], voffB[3]);
  };
  issue(0); issue(1); issue(2);
  f32x4 acc[8][2];
#pragma unroll
  for (int a = 0; a < 8; a++) { acc[a][0] = f32x4{0, 0, 0, 0}; acc[a][1] = f32x4{0, 0, 0, 0}; }
  const int aoff = i * 128 + ((g ^ (i >> 1)) * 16);
  const int boff = 16384 + (8 * g + q) * 128 + ((w ^ (((q >> 1) & 1) | ((g & 1) << 1))) * 32) + p * 8;
#pragma unroll 1
  for (int idx = 0; idx < 8; idx++) {
    if (idx <= 5) WAITV(16); else if (idx == 6) WAITV(8); else WAITV(0);
    BAR();
    const char* slot = lds + (idx & 3) * 32768;
#pragma unroll
    for (int ks = 0; ks < 2; ks++) {
      s16x8 bf[2];
#pragma unroll
      for (int hh = 0; hh < 2; hh++) bf[hh] = trd2(slot + boff + hh * 8192 + ks * 4096, slot + boff + hh * 8192 + ks * 4096 + 512);
#pragma unroll
      for (int mt = 0; mt < 8; mt++) {
        s16x8 af = lrd(slot + (aoff ^ (ks * 64)) + mt * 2048);
#pragma unroll
        for (int hh = 0; hh < 2; hh++) acc[mt][hh] = QT ? mma(bf[hh], af, acc[mt][hh]) : mma(af, bf[hh], acc[mt][hh]);
      }
    }
    if (idx < 5) issue(idx + 3);
  }
  if (QT) {
#pragma unroll
    for (int hh = 0; hh < 2; hh++) {
      const int head = (nt - 8) * 8 + hh * 4 + w;
#pragma unroll
      for (int u = 0; u < 4; u++) {
        u32x4 v = {pk(acc[2 * u][hh][0], acc[2 * u][hh][1]), pk(acc[2 * u][hh][2], acc[2 * u][hh][3]),
                   pk(acc[2 * u + 1][hh][0], acc[2 * u + 1][hh][1]), pk(acc[2 * u + 1][hh][2], acc[2 * u + 1][hh][3])};
        *(u32x4*)(ws + WS_PQ + ((size_t)(head * 64 + m * 4 + u) * 64 + lane) * 16) = v;
      }
    }
  } else {
    f32x4 kv = {0, 0, 0, 0}, z = {0, 0, 0, 0};
    const u32x4 ones = {0x3f803f80u, 0x3f803f80u, 0x3f803f80u, 0x3f803f80u};
#pragma unroll
    for (int u = 0; u < 4; u++) {
      u32x4 a = {pk(phi(acc[2 * u][0][0]), phi(acc[2 * u][0][1])), pk(phi(acc[2 * u][0][2]), phi(acc[2 * u][0][3])),
                 pk(phi(acc[2 * u + 1][0][0]), phi(acc[2 * u + 1][0][1])), pk(phi(acc[2 * u + 1][0][2]), phi(acc[2 * u + 1][0][3]))};
      u32x4 b = {pk(acc[2 * u][1][0], acc[2 * u][1][1]), pk(acc[2 * u][1][2], acc[2 * u][1][3]),
                 pk(acc[2 * u + 1][1][0], acc[2 * u + 1][1][1]), pk(acc[2 * u + 1][1][2], acc[2 * u + 1][1][3])};
      kv = mma(as8(a), as8(b), kv);
      z = mma(as8(a), as8(ones), z);
    }
    const int head = nt * 4 + w;
    *(f32x4*)(ws + WS_KVP + ((size_t)(m * 32 + head) * 64 + lane) * 16) = kv;
    if (i == 0) *(f32x4*)(ws + WS_ZP + ((size_t)(m * 32 + head) * 4 + g) * 16) = z;
  }
}

DI void k1_aux(char* lds, const char* x, const unsigned short* wr, const unsigned short* wi, const char* gw, char* ws, int a, int w, int lane) {
  const int i = lane & 15, g = lane >> 4;
  char* LX = lds;
  char* LS = lds + 32768;
  char* LW = lds + 65536;
  const int amt = (a >> 1) & 1;
#pragma unroll
  for (int b = 0; b < 2; b++) {
    const int n2 = perm32(amt, 8 * (a & 1) + 2 * w + b);
    const unsigned v = lane * 16;
    glds4(laddr_of(LX) + (w * 8 + b * 4) * 1024, x + (size_t)(128 * (a >> 2) + n2) * 1024, v, v + 31 * 1024, v + 62 * 1024, v + 93 * 1024);
  }
  unsigned wv[8][4];
  {
    const int c = 64 * w + lane;
#pragma unroll
    for (int o8 = 0; o8 < 8; o8++)
#pragma unroll
      for (int r = 0; r < 4; r++) {
        const int k = 8 * (a & 7) + o8 + 64 * (4 * (a >> 3) + r);
        wv[o8][r] = (unsigned)wr[k * 257 + c] | ((unsigned)wi[k * 257 + c] << 16);
      }
  }
  const int wline = ((((a & 7) >> 1) * 2 + ((a >> 3) & 1)) * 64 + (a >> 4) * 16 + (a & 1) * 8) * 16;
  if (w == 0 && lane < 32) {
    const int k = 8 * (a & 7) + (lane >> 2) + 64 * (4 * (a >> 3) + (lane & 3));
    unsigned v = (unsigned)wr[k * 257 + 256] | ((unsigned)wi[k * 257 + 256] << 16);
    *(unsigned*)(ws + WS_WT + (size_t)256 * 8192 + wline + lane * 4) = v;
  }
  u32x4 gwv = *(const u32x4*)(gw + lane * 16);
  u32x4 b1re, b1im, a3re, a3im, a4re, a4im;
  float twc[4], tws[4];
#pragma unroll
  for (int d = 0; d < 4; d++) {
    float c[2], s[2];
#pragma unroll
    for (int e = 0; e < 2; e++) {
      const int j = 2 * d + e;
      const int n1 = 16 * (j >> 2) + 4 * g + (j & 3);
      const float ang = (float)((n1 * i) & 31) * (1.f / 32.f);
      c[e] = fcos(ang);
      s[e] = (i == 0) ? ((n1 & 1) ? -1.f : 1.f) : -fsin(ang);
    }
    b1re[d] = pk(c[0], c[1]); b1im[d] = pk(s[0], s[1]);
    const int n2 = 4 * g + d;
    {
      const float ang = (float)((n2 * i) & 15) * (1.f / 16.f);
      const float cs = fcos(ang), sn = fsin(ang);
      a3re[d] = pk(cs, sn);
      a3im[d] = (i >= 8) ? pk(sn, -cs) : pk(-sn, cs);
    }
    {
      const float ang = (float)((n2 * i) & 31) * (1.f / 32.f);
      const float cs = fcos(ang);
      const float sn = (i == 0) ? ((n2 & 1) ? -1.f : 1.f) : -fsin(ang);
      a4re[d] = (i & 1) ? pk(0.f, cs) : pk(cs, 0.f);
      a4im[d] = (i & 1) ? pk(0.f, sn) : pk(sn, 0.f);
    }
    {
      const float ang = (float)(n2 * i) * (1.f / 512.f);
      twc[d] = fcos(ang); tws[d] = fsin(ang);
    }
  }
#pragma unroll
  for (int t = 0; t < 2; t++) {
    const int fr = a * 8 + w * 2 + t;
    const int tile = fr >> 4, ks = fr & 15;
    const int n = 32 * (tile >> 1) + perm32(tile & 1, i);
    u32x4 v;
#pragma unroll
    for (int d = 0; d < 4; d++) {
      const int k = 16 * ks + 8 * (d >> 1) + 2 * g + (d & 1);
      const float ang = (float)((k * n) & 511) * (1.f / 512.f);
      float cs = fcos(ang) * 0x1p-19f, sn = fsin(ang) * 0x1p-19f;
      if (k == 0) { cs = 0x1p-20f; sn = (n & 1) ? -0x1p-20f : 0x1p-20f; }
      v[d] = pk(cs, sn);
    }
    *(u32x4*)(ws + WS_GF + ((size_t)fr * 64 + lane) * 16) = v;
  }
  WAITV(0);
  {
    const int c = 64 * w + lane;
#pragma unroll
    for (int o8 = 0; o8 < 8; o8++) {
      u32x4 v = {wv[o8][0], wv[o8][1], wv[o8][2], wv[o8][3]};
      *(u32x4*)(LW + c * 128 + ((o8 ^ (c & 7)) * 16)) = v;
    }
  }
  int colr[4];
#pragma unroll
  for (int r = 0; r < 4; r++) {
    const int k2 = 4 * g + r;
    const int k = i + 32 * k2;
    colr[r] = (i == 0) ? 16 * k2 : (k2 < 8 ? k : 512 - k);
  }
#pragma unroll 1
  for (int b = 0; b < 2; b++) {
    unsigned o[4][4];
#pragma unroll
    for (int a4 = 0; a4 < 4; a4++) {
      const char* row = LX + (w * 8 + b * 4 + a4) * 1024;
      s16x8 xf = trd2(row + lane * 8, row + 512 + lane * 8);
      f32x4 z4 = {0, 0, 0, 0};
      f32x4 are = mma(xf, as8(b1re), z4), aim = mma(xf, as8(b1im), z4);
      u32x4 bb;
#pragma unroll
      for (int r = 0; r < 4; r++) bb[r] = pk(are[r] * twc[r] + aim[r] * tws[r], aim[r] * twc[r] - are[r] * tws[r]);
      f32x4 xre = mma(as8(a3re), as8(bb), z4), xim = mma(as8(a3im), as8(bb), z4);
      f32x4 sre = mma(as8(a4re), as8(bb), z4), sim = mma(as8(a4im), as8(bb), z4);
#pragma unroll
      for (int r = 0; r < 4; r++) o[r][a4] = (i == 0) ? pk(sre[r], sim[r]) : pk(xre[r], xim[r]);
      u32x4 xv = *(const u32x4*)(row + lane * 16);
      float gs = 0.f;
#pragma unroll
      for (int d = 0; d < 4; d++) {
        unsigned pr = pk(bflo(xv[d]) * bflo(gwv[d]), bfhi(xv[d]) * bfhi(gwv[d]));
        gs += bflo(pr) + bfhi(pr);
      }
#pragma unroll
      for (int sh = 32; sh >= 1; sh >>= 1) gs += __shfl_xor(gs, sh);
      if (lane == 0) {
        gs = rbf(gs);
        const float gg = rbf(1.f / (1.f + __expf(-gs)));
        const int n1 = 4 * (a >> 2) + a4;
        const int n2 = perm32(amt, 8 * (a & 1) + 2 * w + b);
        f32x2 gv = {gg, rbf(1.f - gg)};
        *(f32x2*)(ws + WS_GT + (size_t)(32 * n1 + n2) * 8) = gv;
      }
    }
#pragma unroll
    for (int r = 0; r < 4; r++) {
      u32x4 v = {o[r][0], o[r][1], o[r][2], o[r][3]};
      *(u32x4*)(LS + colr[r] * 128 + (((2 * w + b) ^ (colr[r] & 7)) * 16)) = v;
    }
  }
  __syncthreads();
  const int xline = (amt * 4 + (a >> 4)) * 1024 + ((a >> 2) & 3) * 256 + (a & 1) * 128;
#pragma unroll
  for (int j = 0; j < 8; j++) {
    const int c = (j * 4 + w) * 8 + (lane >> 3);
    const int sl = lane & 7;
    u32x4 v0 = *(const u32x4*)(LS + c * 128 + ((sl ^ (c & 7)) * 16));
    u32x4 v1 = *(const u32x4*)(LW + c * 128 + ((sl ^ (c & 7)) * 16));
    *(u32x4*)(ws + WS_X1T + (size_t)c * 8192 + xline + sl * 16) = v0;
    *(u32x4*)(ws + WS_WT + (size_t)c * 8192 + wline + sl * 16) = v1;
  }
}

__global__ __launch_bounds__(256, 1) void k1(const char* x, const char* wqkv, const unsigned short* wr, const unsigned short* wi, const char* gw, char* ws) {
  __shared__ __attribute__((aligned(16))) char lds[131072];
  const int b = blockIdx.x, xcd = b & 7, ii = b >> 3;
  const int w = __builtin_amdgcn_readfirstlane(threadIdx.x >> 6), lane = threadIdx.x & 63;
  if (ii >= 24) { k1_aux(lds, x, wr, wi, gw, ws, xcd * 8 + (ii - 24), w, lane); return; }
  const int ml = ii & 3, nl = ii >> 2;
  const int m = (xcd >> 1) * 4 + ml;
  const int rot = (2 * ml + nl) & 7;
  if (nl < 4) k1_gemm<false>(lds, x, wqkv, ws, m, (xcd & 1) * 4 + nl, rot, w, lane);
  else k1_gemm<true>(lds, x, wqkv, ws, m, 8 + (xcd & 1) * 2 + (nl - 4), rot, w, lane);
}

DI void k2_fft(char* lds, char* ws, int c, int w, int lane) {
  const int i = lane & 15, g = lane >> 4;
  u32x4 xa[2][4], wf[2];
  const char* strip = ws + WS_X1T + (size_t)(c & 255) * 8192 + lane * 16;
#pragma unroll
  for (int mt = 0; mt < 2; mt++)
#pragma unroll
    for (int ks = 0; ks < 4; ks++) xa[mt][ks] = *(const u32x4*)(strip + (mt * 4 + ks) * 1024);
#pragma unroll
  for (int mt = 0; mt < 2; mt++) wf[mt] = *(const u32x4*)(ws + WS_WT + (size_t)c * 8192 + ((w * 2 + mt) * 64 + lane) * 16);
  const int k1 = 16 * w + i;
  u32x4 fre[4], fim[4], fc[2], fs[2], fn[2];
#pragma unroll
  for (int ks = 0; ks < 4; ks++)
#pragma unroll
    for (int d = 0; d < 4; d++) {
      const int n1 = 16 * ks + 4 * g + d;
      const float ang = (float)((n1 * k1) & 63) * (1.f / 64.f);
      const float cs = fcos(ang), sn = fsin(ang);
      fre[ks][d] = pk(cs, sn); fim[ks][d] = pk(-sn, cs);
    }
  float c1[2][4], s1[2][4], c2[2][4], s2[2][4];
#pragma unroll
  for (int mt = 0; mt < 2; mt++) {
    const int a = perm32(mt, i);
#pragma unroll
    for (int d = 0; d < 4; d++) {
      const float a0 = (float)((a * (8 * g + 2 * d)) & 31) * (1.f / 32.f), a1 = (float)((a * (8 * g + 2 * d + 1)) & 31) * (1.f / 32.f);
      fc[mt][d] = pk(fcos(a0), fcos(a1));
      fs[mt][d] = pk(fsin(a0), fsin(a1));
      fn[mt][d] = fs[mt][d] ^ 0x80008000u;
      const float t1 = (float)((8 * g + 4 * mt + d) * k1) * (1.f / 2048.f);
      c1[mt][d] = fcos(t1); s1[mt][d] = fsin(t1);
      const float t2 = (float)(a * (16 * w + 4 * g + d)) * (1.f / 2048.f);
      c2[mt][d] = fcos(t2); s2[mt][d] = fsin(t2);
    }
  }
  if ((c & 255) == 0) {
#pragma unroll
    for (int mt = 0; mt < 2; mt++)
#pragma unroll
      for (int ks = 0; ks < 4; ks++) xa[mt][ks] = c ? (xa[mt][ks] >> 16) : (xa[mt][ks] & 0xffffu);
  }
  const f32x4 z4 = {0, 0, 0, 0};
  f32x4 re[2], im[2];
#pragma unroll
  for (int mt = 0; mt < 2; mt++) { re[mt] = z4; im[mt] = z4; }
#pragma unroll
  for (int ks = 0; ks < 4; ks++)
#pragma unroll
    for (int mt = 0; mt < 2; mt++) { re[mt] = mma(as8(xa[mt][ks]), as8(fre[ks]), re[mt]); im[mt] = mma(as8(xa[mt][ks]), as8(fim[ks]), im[mt]); }
  u32x4 pr, pi;
#pragma unroll
  for (int mt = 0; mt < 2; mt++) {
    float tr[4], ti[4];
#pragma unroll
    for (int r = 0; r < 4; r++) { tr[r] = re[mt][r] * c1[mt][r] + im[mt][r] * s1[mt][r]; ti[r] = im[mt][r] * c1[mt][r] - re[mt][r] * s1[mt][r]; }
    pr[2 * mt] = pk(tr[0], tr[1]); pr[2 * mt + 1] = pk(tr[2], tr[3]);
    pi[2 * mt] = pk(ti[0], ti[1]); pi[2 * mt + 1] = pk(ti[2], ti[3]);
  }
#pragma unroll
  for (int mt = 0; mt < 2; mt++) {
    re[mt] = mma(as8(fs[mt]), as8(pi), mma(as8(fc[mt]), as8(pr), z4));
    im[mt] = mma(as8(fn[mt]), as8(pr), mma(as8(fc[mt]), as8(pi), z4));
  }
#pragma unroll
  for (int mt = 0; mt < 2; mt++) {
    float tr[4], ti[4];
#pragma unroll
    for (int r = 0; r < 4; r++) {
      const float a = bflo(wf[mt][r]), b = bfhi(wf[mt][r]);
      tr[r] = re[mt][r] * a - im[mt][r] * b; ti[r] = re[mt][r] * b + im[mt][r] * a;
    }
    pr[2 * mt] = pk(tr[0], tr[1]); pr[2 * mt + 1] = pk(tr[2], tr[3]);
    pi[2 * mt] = pk(ti[0], ti[1]); pi[2 * mt + 1] = pk(ti[2], ti[3]);
  }
#pragma unroll
  for (int nt = 0; nt < 2; nt++) {
    re[nt] = mma(as8(pi), as8(fn[nt]), mma(as8(pr), as8(fc[nt]), z4));
    im[nt] = mma(as8(pi), as8(fc[nt]), mma(as8(pr), as8(fs[nt]), z4));
  }
#pragma unroll
  for (int nt = 0; nt < 2; nt++) {
    u32x4 v;
#pragma unroll
    for (int r = 0; r < 4; r++) v[r] = pk(re[nt][r] * c2[nt][r] - im[nt][r] * s2[nt][r], -(re[nt][r] * s2[nt][r] + im[nt][r] * c2[nt][r]));
    *(u32x4*)(lds + ((nt * 4 + w) * 64 + lane) * 16) = v;
  }
  __syncthreads();
#pragma unroll
  for (int mt = 0; mt < 2; mt++) { re[mt] = z4; im[mt] = z4; }
#pragma unroll
  for (int ks = 0; ks < 4; ks++)
#pragma unroll
    for (int mt = 0; mt < 2; mt++) {
      s16x8 ya = lrd(lds + ((mt * 4 + ks) * 64 + lane) * 16);
      re[mt] = mma(ya, as8(fre[ks]), re[mt]); im[mt] = mma(ya, as8(fim[ks]), im[mt]);
    }
  u32x4 vr = {pk(re[0][0], re[0][1]), pk(re[0][2], re[0][3]), pk(re[1][0], re[1][1]), pk(re[1][2], re[1][3])};
  u32x4 vi = {pk(im[0][0], im[0][1]), pk(im[0][2], im[0][3]), pk(im[1][0], im[1][1]), pk(im[1][2], im[1][3])};
  char* yt = ws + WS_YT + (32 * k1 + 8 * g) * 2;
  const int rr = (c == 256) ? 1 : 2 * c;
  *(u32x4*)(yt + (size_t)rr * 4096) = vr;
  if (c & 255) *(u32x4*)(yt + (size_t)(rr + 1) * 4096) = vi;
}

DI void k2_att(char* lds, char* ws, int t, int mb, int w, int lane) {
  const int g = lane >> 4;
  u32x4 pq[2];
#pragma unroll
  for (int h = 0; h < 2; h++) pq[h] = *(const u32x4*)(ws + WS_PQ + ((size_t)((2 * t + h) * 64 + mb * 4 + w) * 64 + lane) * 16);
  f32x4 kv[2], z[2];
#pragma unroll
  for (int h = 0; h < 2; h++) {
    kv[h] = f32x4{0, 0, 0, 0}; z[h] = f32x4{0, 0, 0, 0};
#pragma unroll
    for (int pp = 0; pp < 4; pp++) {
      const int id = (4 * w + pp) * 32 + 2 * t + h;
      kv[h] += *(const f32x4*)(ws + WS_KVP + ((size_t)id * 64 + lane) * 16);
      z[h] += *(const f32x4*)(ws + WS_ZP + ((size_t)id * 4 + g) * 16);
    }
    *(f32x4*)(lds + (((w * 2 + h) * 2 + 0) * 64 + lane) * 16) = kv[h];
    *(f32x4*)(lds + (((w * 2 + h) * 2 + 1) * 64 + lane) * 16) = z[h];
  }
#pragma unroll
  for (int h = 0; h < 2; h++)
#pragma unroll
    for (int d = 0; d < 4; d++) pq[h][d] = pk(phi(bflo(pq[h][d])), phi(bfhi(pq[h][d])));
  __syncthreads();
  u32x2 kvf[2], zf[2];
#pragma unroll
  for (int h = 0; h < 2; h++) {
    f32x4 a = {0, 0, 0, 0}, b = {0, 0, 0, 0};
#pragma unroll
    for (int ww = 0; ww < 4; ww++) {
      a += *(const f32x4*)(lds + (((ww * 2 + h) * 2 + 0) * 64 + lane) * 16);
      b += *(const f32x4*)(lds + (((ww * 2 + h) * 2 + 1) * 64 + lane) * 16);
    }
    kvf[h] = u32x2{pk(a[0], a[1]), pk(a[2], a[3])};
    zf[h] = u32x2{pk(b[0], b[1]), pk(b[2], b[3])};
  }
  const f32x4 z4 = {0, 0, 0, 0};
#pragma unroll
  for (int st = 0; st < 2; st++) {
    u32x4 o;
#pragma unroll
    for (int h = 0; h < 2; h++) {
      u32x2 b = {pq[h][2 * st], pq[h][2 * st + 1]};
      f32x4 num = mma16(as4(kvf[h]), as4(b), z4);
      f32x4 den = mma16(as4(zf[h]), as4(b), z4);
      const float rd = 1.f / (den[0] + 1e-6f);
      o[2 * h] = pk(num[0] * rd, num[1] * rd);
      o[2 * h + 1] = pk(num[2] * rd, num[3] * rd);
    }
    *(u32x4*)(ws + WS_OF + ((size_t)((mb * 8 + w * 2 + st) * 16 + t) * 64 + lane) * 16) = o;
  }
}

__global__ __launch_bounds__(256, 2) void k2(char* ws) {
  __shared__ __attribute__((aligned(16))) char lds[16384];
  const int b = blockIdx.x;
  const int w = __builtin_amdgcn_readfirstlane(threadIdx.x >> 6), lane = threadIdx.x & 63;
  if (b < 257) k2_fft(lds, ws, b, w, lane);
  else k2_att(lds, ws, (b - 257) & 15, (b - 257) >> 4, w, lane);
}

__global__ __launch_bounds__(256, 1) void k3(const char* wo, char* ws, char* out) {
  __shared__ __attribute__((aligned(16))) char lds[131072];
  const int b = blockIdx.x, xcd = b & 7, ii = b >> 3;
  const int w = __builtin_amdgcn_readfirstlane(threadIdx.x >> 6), lane = threadIdx.x & 63;
  const int i = lane & 15, g = lane >> 4, q = i >> 2, p = lane & 3;
  const int ml = ii & 3, ntile = ii >> 2;
  const int mtile = xcd * 4 + ml;
  const int rot = (2 * ml + ntile) & 7;
  const int swz = (((lane & 7) >> 1) ^ ((lane >> 4) & 3)) * 32 + (lane & 1) * 16;
  const unsigned voffW = (lane >> 3) * 1024 + swz, voffY = (lane >> 3) * 4096 + swz, voffR = lane * 16;
  const char* ra = ws + WS_OF + (size_t)(4 * mtile + w) * 16384;
  const char* rb = ws + WS_GF + (size_t)(4 * ntile + w) * 16384;
  const char* ta = wo + (size_t)(w * 32) * 1024 + ntile * 128;
  const char* tb = ws + WS_YT + (size_t)(w * 32) * 4096 + mtile * 128;
  const unsigned lbase = laddr_of(lds) + w * 4096;
  auto issue = [&](int idx) {
    const int st = (idx + rot) & 7;
    const unsigned slot = lbase + (idx & 3) * 32768;
    if (st < 4) {
      glds4(slot, ra + st * 4096, voffR, voffR, voffR, voffR);
      glds4(slot + 16384, ta + (size_t)(st * 128) * 1024, voffW, voffW + 7168, voffW + 2 * 7168, voffW + 3 * 7168);
    } else {
      glds4(slot, rb + (st - 4) * 4096, voffR, voffR, voffR, voffR);
      glds4(slot + 16384, tb + (size_t)((st - 4) * 128) * 4096, voffY, voffY + 31744, voffY + 2 * 31744, voffY + 3 * 31744);
    }
  };
  issue(0); issue(1); issue(2);
  const int wn = w & 1, wsn = w >> 1;
  f32x4 accA[2][2], accB[2][2];
#pragma unroll
  for (int a = 0; a < 2; a++)
#pragma unroll
    for (int c = 0; c < 2; c++) { accA[a][c] = f32x4{0, 0, 0, 0}; accB[a][c] = f32x4{0, 0, 0, 0}; }
  const int fsw = (2 * g + (q >> 1)) & 3;
  const int trow = 16384 + (4 * g + q) * 128;
  const int toffA = trow + (((2 * wn + (p >> 1)) ^ fsw) * 32) + (p & 1) * 16;
  const int toffB0 = trow + (((2 * wsn) ^ fsw) * 32) + p * 8;
#pragma unroll 1
  for (int idx = 0; idx < 8; idx++) {
    if (idx <= 5) WAITV(16); else if (idx == 6) WAITV(8); else WAITV(0);
    BAR();
    const char* slot = lds + (idx & 3) * 32768;
    const int st = (idx + rot) & 7;
    if (st < 4) {
#pragma unroll
      for (int ks = 0; ks < 4; ks++) {
        s16x8 tf[2], rf[2];
#pragma unroll
        for (int a = 0; a < 2; a++) {
          tf[a] = trd2(slot + toffA + 8 * a + ks * 4096, slot + toffA + 8 * a + ks * 4096 + 2048);
          rf[a] = lrd(slot + ((2 * wsn + a) * 4 + ks) * 1024 + lane * 16);
        }
#pragma unroll
        for (int a = 0; a < 2; a++)
#pragma unroll
          for (int c = 0; c < 2; c++) accA[a][c] = mma(tf[a], rf[c], accA[a][c]);
      }
    } else {
#pragma unroll
      for (int ks = 0; ks < 4; ks++) {
        s16x8 tf[2], rf[2];
#pragma unroll
        for (int a = 0; a < 2; a++) {
          tf[a] = trd2(slot + (toffB0 ^ (32 * a)) + ks * 4096, slot + (toffB0 ^ (32 * a)) + ks * 4096 + 2048);
          rf[a] = lrd(slot + ((2 * wn + a) * 4 + ks) * 1024 + lane * 16);
        }
#pragma unroll
        for (int a = 0; a < 2; a++)
#pragma unroll
          for (int c = 0; c < 2; c++) accB[a][c] = mma(rf[a], tf[c], accB[a][c]);
      }
    }
    if (idx < 5) issue(idx + 3);
  }
#pragma unroll
  for (int c = 0; c < 2; c++) {
    const int s = 64 * mtile + 32 * wsn + 16 * c + i;
    const f32x2 gv = *(const f32x2*)(ws + WS_GT + (size_t)s * 8);
    u32x4 v;
#pragma unroll
    for (int a = 0; a < 2; a++) {
      v[2 * a] = pk(gv[0] * accA[a][c][0] + gv[1] * accB[a][c][0], gv[0] * accA[a][c][1] + gv[1] * accB[a][c][1]);
      v[2 * a + 1] = pk(gv[0] * accA[a][c][2] + gv[1] * accB[a][c][2], gv[0] * accA[a][c][3] + gv[1] * accB[a][c][3]);
    }
    *(u32x4*)(out + (size_t)s * 1024 + (64 * ntile + 32 * wn + 8 * g) * 2) = v;
  }
}

extern "C" void kernel_launch(void* const* d_in, const int* in_sizes, int n_in, void* d_out, int out_size, void* d_ws, size_t ws_size, hipStream_t stream) {
  const char* x = (const char*)d_in[0];
  const char* wqkv = (const char*)d_in[1];
  const char* wo = (const char*)d_in[2];
  const unsigned short* wr = (const unsigned short*)d_in[4];
  const unsigned short* wi = (const unsigned short*)d_in[5];
  const char* gw = (const char*)d_in[6];
  char* ws = (char*)d_ws;
  k1<<<256, 256, 0, stream>>>(x, wqkv, wr, wi, gw, ws);
  k2<<<513, 256, 0, stream>>>(ws);
  k3<<<256, 256, 0, stream>>>(wo, ws, (char*)d_out);
}
```

```cpp
#include <hip/hip_runtime.h>
#include <stdint.h>

typedef short s16x4 __attribute__((ext_vector_type(4)));
typedef short s16x8 __attribute__((ext_vector_type(8)));
typedef float f32x4 __attribute__((ext_vector_type(4)));
typedef float f32x2 __attribute__((ext_vector_type(2)));
typedef unsigned u32x4 __attribute__((ext_vector_type(4)));
typedef unsigned u32x2 __attribute__((ext_vector_type(2)));
typedef __bf16 bfx2 __attribute__((ext_vector_type(2)));
#define LAS __attribute__((address_space(3)))
#define DI __device__ __forceinline__

static constexpr size_t WS_X1T = 0;
static constexpr size_t WS_WT  = WS_X1T + (9u << 18);
static constexpr size_t WS_PQ  = WS_WT + (9u << 18);
static constexpr size_t WS_KVP = WS_PQ + (2u << 20);
static constexpr size_t WS_ZP  = WS_KVP + (512u << 10);
static constexpr size_t WS_GT  = WS_ZP + (32u << 10);
static constexpr size_t WS_GF  = WS_GT + (16u << 10);
static constexpr size_t WS_OF  = WS_GF + (512u << 10);
static constexpr size_t WS_YT  = WS_OF + (2u << 20);

DI unsigned pk(float lo, float hi) { f32x2 p = {lo, hi}; return __builtin_bit_cast(unsigned, __builtin_convertvector(p, bfx2)); }
DI float bflo(unsigned u) { return __builtin_bit_cast(float, u << 16); }
DI float bfhi(unsigned u) { return __builtin_bit_cast(float, u & 0xffff0000u); }
DI float rbf(float v) { return bflo(pk(v, 0.f)); }
DI s16x8 as8(u32x4 v) { return __builtin_bit_cast(s16x8, v); }
DI s16x4 as4(u32x2 v) { return __builtin_bit_cast(s16x4, v); }
DI f32x4 mma(s16x8 a, s16x8 b, f32x4 c) { return __builtin_amdgcn_mfma_f32_16x16x32_bf16(a, b, c, 0, 0, 0); }
DI f32x4 mma16(s16x4 a, s16x4 b, f32x4 c) { return __builtin_amdgcn_mfma_f32_16x16x16bf16_1k(a, b, c, 0, 0, 0); }
DI float fsin(float rev) { return __builtin_amdgcn_sinf(rev); }
DI float fcos(float rev) { return __builtin_amdgcn_cosf(rev); }
DI void glds4(unsigned laddr, const char* sb, unsigned v0, unsigned v1, unsigned v2, unsigned v3) {
  asm volatile("s_mov_b32 m0, %0\n\ts_nop 0\n\t"
               "global_load_lds_dwordx4 %2, %1\n\t"
               "global_load_lds_dwordx4 %3, %1 offset:1024\n\t"
               "global_load_lds_dwordx4 %4, %1 offset:2048\n\t"
               "global_load_lds_dwordx4 %5, %1 offset:3072"
               :: "s"(laddr), "s"(sb), "v"(v0), "v"(v1), "v"(v2), "v"(v3) : "memory");
}
DI unsigned laddr_of(const char* l) { return (unsigned)(size_t)(const LAS char*)l; }
DI s16x8 trd2(const char* l0, const char* l1) {
  s16x4 a = __builtin_amdgcn_ds_read_tr16_b64_v4i16((LAS s16x4*)l0);
  s16x4 b = __builtin_amdgcn_ds_read_tr16_b64_v4i16((LAS s16x4*)l1);
  return __builtin_shufflevector(a, b, 0, 1, 2, 3, 4, 5, 6, 7);
}
DI s16x8 lrd(const char* l) { return *(const s16x8*)l; }
DI int perm32(int t, int i) { return 8 * (i >> 2) + 4 * t + (i & 3); }
DI float phi(float t) {
  float e = __builtin_amdgcn_exp2f(t * 1.44269504088896341f);
  e = __builtin_amdgcn_fmed3f(e, 0.f, 1.f);
  return fmaxf(t + 1e-6f, 1e-6f) + e;
}
#define TS(kern, slot)
DI void st_wt(char* p, u32x4 v) { asm volatile("global_store_dwordx4 %0, %1, off sc1\n\ts_nop 1" :: "v"(p), "v"(v) : "memory"); }
#define SCHED() __builtin_amdgcn_sched_barrier(0)
#define SGB(mask, n) __builtin_amdgcn_sched_group_barrier(mask, n, 0)
#define PIN(x) asm volatile("" : "+v"(x))
#define WAITV(n) asm volatile("s_waitcnt vmcnt(" #n ")" ::: "memory")
#define BAR() asm volatile("s_waitcnt lgkmcnt(0)\n\ts_barrier" ::: "memory")

template <class F> DI void produce(F issue) {
  issue(0); issue(1);
  WAITV(8); BAR();
  issue(2); issue(3);
#pragma unroll 1
  for (int j = 1; j < 5; j++) { WAITV(16); BAR(); issue(j + 3); }
  WAITV(16); BAR();
  WAITV(8); BAR();
  WAITV(0); BAR();
}

DI void k1_prod(char* lds, const char* x, const char* wqkv, int m, int col0, int col1, int rot, int w, int lane) {
  unsigned voffA[4], voffB[4];
#pragma unroll
  for (int j = 0; j < 4; j++) {
    const int par = j & 1;
    voffA[j] = (lane >> 3) * 1024 + (((lane & 7) ^ ((4 * par + (lane >> 4)) & 7)) * 16) + j * (8192 - 1024);
    voffB[j] = (lane >> 3) * 3072 + ((((lane & 7) >> 1) ^ (((lane >> 4) & 1) | (par << 1))) * 32) + (lane & 1) * 16 + j * (24576 - 1024);
  }
  const unsigned lbase = laddr_of(lds) + w * 4096;
  const char* xa = x + (size_t)(m * 128 + w * 32) * 1024;
  const char* wb = wqkv + (size_t)((w & 1) * 32) * 3072 + ((w >> 1) ? col1 : col0) * 2;
  produce([&](int idx) {
    const int ks = (idx + rot) & 7;
    const unsigned slot = lbase + (idx & 3) * 32768;
    glds4(slot, xa + ks * 128, voffA[0], voffA[1], voffA[2], voffA[3]);
    glds4(slot + 16384, wb + (size_t)(ks * 64) * 3072, voffB[0], voffB[1], voffB[2], voffB[3]);
  });
}

template <bool QT>
DI void k1_gemm(char* lds, char* ws, int m, int nt, int w, int lane) {
  const int i = lane & 15, g = lane >> 4, q = i >> 2, p = lane & 3;
  f32x4 acc[8][2];
#pragma unroll
  for (int a = 0; a < 8; a++) { acc[a][0] = f32x4{0, 0, 0, 0}; acc[a][1] = f32x4{0, 0, 0, 0}; }
  const int aoff = i * 128 + ((g ^ (i >> 1)) * 16);
  const int boff = 16384 + (8 * g + q) * 128 + ((w ^ (((q >> 1) & 1) | ((g & 1) << 1))) * 32) + p * 8;
  s16x8 a0[2][8], b0[2][2], a1[2][8], b1[2][2];
  auto rd = [&](s16x8 (*af)[8], s16x8 (*bf)[2], const char* slot) {
#pragma unroll
    for (int ks = 0; ks < 2; ks++) {
#pragma unroll
      for (int hh = 0; hh < 2; hh++) bf[ks][hh] = trd2(slot + boff + hh * 8192 + ks * 4096, slot + boff + hh * 8192 + ks * 4096 + 512);
#pragma unroll
      for (int mt = 0; mt < 8; mt++) af[ks][mt] = lrd(slot + (aoff ^ (ks * 64)) + mt * 2048);
    }
  };
  auto mm = [&](s16x8 (*af)[8], s16x8 (*bf)[2]) {
#pragma unroll
    for (int ks = 0; ks < 2; ks++)
#pragma unroll
      for (int mt = 0; mt < 8; mt++)
#pragma unroll
        for (int hh = 0; hh < 2; hh++) acc[mt][hh] = QT ? mma(bf[ks][hh], af[ks][mt], acc[mt][hh]) : mma(af[ks][mt], bf[ks][hh], acc[mt][hh]);
  };
  auto ilv = [&]() {
#pragma unroll
    for (int t = 0; t < 24; t++) { SGB(0x008, 1); SGB(0x100, 1); }
    SGB(0x008, 8);
  };
  __builtin_amdgcn_s_waitcnt(0xC07F);
  BAR();
  TS(0, 1);
  rd(a0, b0, lds);
  SCHED();
#pragma unroll 1
  for (int it = 0; it < 3; it++) {
    BAR(); SCHED();
    rd(a1, b1, lds + ((2 * it + 1) & 3) * 32768); mm(a0, b0); ilv(); SCHED();
    BAR(); SCHED();
    rd(a0, b0, lds + ((2 * it + 2) & 3) * 32768); mm(a1, b1); ilv(); SCHED();
  }
  BAR(); SCHED();
  rd(a1, b1, lds + 3 * 32768); mm(a0, b0); ilv(); SCHED();
  mm(a1, b1);
  TS(0, 2);
  if (QT) {
#pragma unroll
    for (int hh = 0; hh < 2; hh++) {
      const int head = (nt - 8) * 8 + hh * 4 + w;
#pragma unroll
      for (int u = 0; u < 4; u++) {
        u32x4 v = {pk(acc[2 * u][hh][0], acc[2 * u][hh][1]), pk(acc[2 * u][hh][2], acc[2 * u][hh][3]),
                   pk(acc[2 * u + 1][hh][0], acc[2 * u + 1][hh][1]), pk(acc[2 * u + 1][hh][2], acc[2 * u + 1][hh][3])};
        *(u32x4*)(ws + WS_PQ + ((size_t)(head * 64 + m * 4 + u) * 64 + lane) * 16) = v;
      }
    }
  } else {
    f32x4 kv = {0, 0, 0, 0}, z = {0, 0, 0, 0};
    const u32x4 ones = {0x3f803f80u, 0x3f803f80u, 0x3f803f80u, 0x3f803f80u};
#pragma unroll
    for (int u = 0; u < 4; u++) {
      u32x4 a = {pk(phi(acc[2 * u][0][0]), phi(acc[2 * u][0][1])), pk(phi(acc[2 * u][0][2]), phi(acc[2 * u][0][3])),
                 pk(phi(acc[2 * u + 1][0][0]), phi(acc[2 * u + 1][0][1])), pk(phi(acc[2 * u + 1][0][2]), phi(acc[2 * u + 1][0][3]))};
      u32x4 b = {pk(acc[2 * u][1][0], acc[2 * u][1][1]), pk(acc[2 * u][1][2], acc[2 * u][1][3]),
                 pk(acc[2 * u + 1][1][0], acc[2 * u + 1][1][1]), pk(acc[2 * u + 1][1][2], acc[2 * u + 1][1][3])};
      kv = mma(as8(a), as8(b), kv);
      z = mma(as8(a), as8(ones), z);
    }
    const int head = nt * 4 + w;
    *(f32x4*)(ws + WS_KVP + ((size_t)(m * 32 + head) * 64 + lane) * 16) = kv;
    if (i == 0) *(f32x4*)(ws + WS_ZP + ((size_t)(m * 32 + head) * 4 + g) * 16) = z;
  }
}

DI float dpp_sum16(float v) {
  v += __builtin_bit_cast(float, __builtin_amdgcn_update_dpp(0, __builtin_bit_cast(int, v), 0x128, 0xf, 0xf, false));
  v += __builtin_bit_cast(float, __builtin_amdgcn_update_dpp(0, __builtin_bit_cast(int, v), 0x124, 0xf, 0xf, false));
  v += __builtin_bit_cast(float, __builtin_amdgcn_update_dpp(0, __builtin_bit_cast(int, v), 0x122, 0xf, 0xf, false));
  v += __builtin_bit_cast(float, __builtin_amdgcn_update_dpp(0, __builtin_bit_cast(int, v), 0x121, 0xf, 0xf, false));
  return v;
}

DI void k1_aux(char* lds, const char* x, const unsigned short* wr, const unsigned short* wi, const char* gw, char* ws, int a, int w, int lane) {
  const int i = lane & 15, g = lane >> 4;
  char* LX = lds;
  char* LS = lds + 32768;
  char* LW = lds + 69632;
  const int amt = (a >> 1) & 1;
  const int n2s = perm32(amt, 8 * (a & 1) + w);
  {
    const unsigned v = lane * 16;
    glds4(laddr_of(LX) + w * 4096, x + (size_t)(128 * (a >> 2) + n2s) * 1024, v, v + 31 * 1024, v + 62 * 1024, v + 93 * 1024);
  }
  u32x4 gwv = *(const u32x4*)(gw + lane * 16);
  unsigned short wvr[4][4], wvi[4][4];
  const int wc = 64 * (w & 3) + lane;
  {
    const int k0 = 8 * (a & 7) + 4 * (w >> 2) + 256 * (a >> 3);
    const unsigned short* pr = wr + k0 * 257 + wc;
    const unsigned short* pi = wi + k0 * 257 + wc;
#pragma unroll
    for (int o4 = 0; o4 < 4; o4++)
#pragma unroll
      for (int r = 0; r < 4; r++) { wvr[o4][r] = pr[(o4 + 64 * r) * 257]; wvi[o4][r] = pi[(o4 + 64 * r) * 257]; }
  }
  const int wline = ((((a & 7) >> 1) * 2 + ((a >> 3) & 1)) * 64 + (a >> 4) * 16 + (a & 1) * 8) * 16;
  unsigned short wl0 = 0, wl1 = 0;
  if (w == 7) {
    const int k = 8 * (a & 7) + ((lane >> 2) & 7) + 64 * (4 * (a >> 3) + (lane & 3));
    wl0 = wr[k * 257 + 256]; wl1 = wi[k * 257 + 256];
  }
  u32x4 b1re, b1im, a3re, a3im, a4re, a4im;
  float twc[4], tws[4];
  const float fi = (float)i;
  const float g4 = (float)(4 * g);
#pragma unroll
  for (int d = 0; d < 4; d++) {
    float c[2], s[2];
#pragma unroll
    for (int e = 0; e < 2; e++) {
      const int j = 2 * d + e;
      const float ang = (g4 + (float)(16 * (j >> 2) + (j & 3))) * (fi * (1.f / 32.f));
      c[e] = fcos(ang);
      s[e] = (i == 0) ? (e ? -1.f : 1.f) : fsin(-ang);
    }
    b1re[d] = pk(c[0], c[1]); b1im[d] = pk(s[0], s[1]);
    const float n2i = (g4 + (float)d) * fi;
    {
      const float ang = n2i * (1.f / 16.f);
      const float cs = fcos(ang), sn = fsin(ang);
      a3re[d] = pk(cs, sn);
      a3im[d] = pk(sn, cs) ^ ((i >= 8) ? 0x80000000u : 0x00008000u);
    }
    {
      const float ang = n2i * (1.f / 32.f);
      const float cs = fcos(ang);
      const float sn = (i == 0) ? ((d & 1) ? -1.f : 1.f) : fsin(-ang);
      const unsigned u = pk(cs, sn);
      const int sh = (i & 1) * 16;
      a4re[d] = (u & 0xffffu) << sh;
      a4im[d] = (u >> 16) << sh;
    }
    {
      const float ang = n2i * (1.f / 512.f);
      twc[d] = fcos(ang); tws[d] = fsin(ang);
      PIN(twc[d]); PIN(tws[d]);
    }
  }
  PIN(b1re); PIN(b1im); PIN(a3re); PIN(a3im); PIN(a4re); PIN(a4im);
  {
    const int fr = a * 8 + w;
    const int tile = fr >> 4, ks = fr & 15;
    const int n = 32 * (tile >> 1) + perm32(tile & 1, i);
    u32x4 v;
#pragma unroll
    for (int d = 0; d < 4; d++) {
      const int k = 16 * ks + 8 * (d >> 1) + 2 * g + (d & 1);
      const float ang = (float)(k * n) * (1.f / 512.f);
      float cs = fcos(ang) * 0x1p-19f, sn = fsin(ang) * 0x1p-19f;
      if (k == 0) { cs = 0x1p-20f; sn = (n & 1) ? -0x1p-20f : 0x1p-20f; }
      v[d] = pk(cs, sn);
    }
    st_wt(ws + WS_GF + ((size_t)fr * 64 + lane) * 16, v);
  }
  int soff[4];
#pragma unroll
  for (int r = 0; r < 4; r++) {
    const int k2 = 4 * g + r;
    const int k = i + 32 * k2;
    const int col = (i == 0) ? 16 * k2 : (k2 < 8 ? k : 512 - k);
    soff[r] = col * 128 + ((w ^ (col & 7)) * 16);
    PIN(soff[r]);
  }
  TS(0, 1);
  PIN(gwv);
  TS(0, 2);
  unsigned o[4][4];
  float gsum[4];
  const u32x4 ones = {0x3f803f80u, 0x3f803f80u, 0x3f803f80u, 0x3f803f80u};
  asm volatile("" ::: "memory");
#pragma unroll
  for (int a4 = 0; a4 < 4; a4++) {
    const char* row = LX + (w * 4 + a4) * 1024;
    s16x8 xf = trd2(row + lane * 8, row + 512 + lane * 8);
    const f32x4 z4 = {0, 0, 0, 0};
    f32x4 are = mma(xf, as8(b1re), z4), aim = mma(xf, as8(b1im), z4);
    u32x4 bb;
#pragma unroll
    for (int r = 0; r < 4; r++) bb[r] = pk(are[r] * twc[r] + aim[r] * tws[r], aim[r] * twc[r] - are[r] * tws[r]);
    f32x4 xre = mma(as8(a3re), as8(bb), z4), xim = mma(as8(a3im), as8(bb), z4);
    f32x4 sre = mma(as8(a4re), as8(bb), z4), sim = mma(as8(a4im), as8(bb), z4);
#pragma unroll
    for (int r = 0; r < 4; r++) o[r][a4] = pk((i == 0) ? sre[r] : xre[r], (i == 0) ? sim[r] : xim[r]);
    u32x4 xv = *(const u32x4*)(row + lane * 16);
    u32x4 pr;
#pragma unroll
    for (int d = 0; d < 4; d++) pr[d] = pk(bflo(xv[d]) * bflo(gwv[d]), bfhi(xv[d]) * bfhi(gwv[d]));
    gsum[a4] = mma(as8(ones), as8(pr), z4)[0];
  }
  if (lane == 0) {
    u32x4 v = {o[0][0] >> 16, o[0][1] >> 16, o[0][2] >> 16, o[0][3] >> 16};
    *(u32x4*)(LS + 256 * 128 + w * 16) = v;
#pragma unroll
    for (int a4 = 0; a4 < 4; a4++) o[0][a4] &= 0xffffu;
  }
#pragma unroll
  for (int r = 0; r < 4; r++) {
    u32x4 v = {o[r][0], o[r][1], o[r][2], o[r][3]};
    *(u32x4*)(LS + soff[r]) = v;
  }
#pragma unroll
  for (int o4 = 0; o4 < 4; o4++) {
    u32x4 v;
#pragma unroll
    for (int r = 0; r < 4; r++) v[r] = (unsigned)wvr[o4][r] | ((unsigned)wvi[o4][r] << 16);
    *(u32x4*)(LW + wc * 128 + (((4 * (w >> 2) + o4) ^ (wc & 7)) * 16)) = v;
  }
  BAR();
  TS(0, 4);
  const int sc = w * 8 + (lane >> 3), sl = lane & 7;
  const int xline = (amt * 4 + (a >> 4)) * 1024 + ((a >> 2) & 3) * 256 + (a & 1) * 128;
  u32x4 v0[4], v1[4];
#pragma unroll
  for (int j = 0; j < 4; j++) {
    const int c = j * 64 + sc;
    v0[j] = *(const u32x4*)(LS + c * 128 + ((sl ^ (c & 7)) * 16));
    v1[j] = *(const u32x4*)(LW + c * 128 + ((sl ^ (c & 7)) * 16));
  }
#pragma unroll
  for (int j = 0; j < 4; j++) {
    const int c = j * 64 + sc;
    st_wt(ws + WS_X1T + (size_t)c * 8192 + xline + sl * 16, v0[j]);
    st_wt(ws + WS_WT + (size_t)c * 8192 + wline + sl * 16, v1[j]);
  }
  if (w == 6 && lane < 8) st_wt(ws + WS_X1T + (size_t)256 * 8192 + xline + lane * 16, *(const u32x4*)(LS + 256 * 128 + lane * 16));
  if (w == 7 && lane < 32) __hip_atomic_store((unsigned*)(ws + WS_WT + (size_t)256 * 8192 + wline + lane * 4), (unsigned)wl0 | ((unsigned)wl1 << 16), __ATOMIC_RELAXED, __HIP_MEMORY_SCOPE_AGENT);
  {
    float gs = (g == 0) ? gsum[0] : (g == 1) ? gsum[1] : (g == 2) ? gsum[2] : gsum[3];
    gs = rbf(dpp_sum16(gs));
    const float gg = rbf(__builtin_amdgcn_rcpf(1.f + __builtin_amdgcn_exp2f(gs * -1.44269504088896341f)));
    f32x2 gv = {gg, rbf(1.f - gg)};
    if (i == 0) __hip_atomic_store((unsigned long long*)(ws + WS_GT + (size_t)(32 * (4 * (a >> 2) + g) + n2s) * 8), __builtin_bit_cast(unsigned long long, gv), __ATOMIC_RELAXED, __HIP_MEMORY_SCOPE_AGENT);
  }
}

__global__ __launch_bounds__(512, 2) void k1(const char* x, const char* wqkv, const unsigned short* wr, const unsigned short* wi, const char* gw, char* ws) {
  __shared__ __attribute__((aligned(16))) char lds[131072];
  const int b = blockIdx.x, xcd = b & 7, ii = b >> 3;
  const int w = __builtin_amdgcn_readfirstlane(threadIdx.x >> 6), lane = threadIdx.x & 63;
  TS(0, 0);
  if (ii >= 24) { k1_aux(lds, x, wr, wi, gw, ws, xcd * 8 + (ii - 24), w, lane); TS(0, 3); return; }
  const int ml = ii & 3, nl = ii >> 2;
  const int m = (xcd >> 1) * 4 + ml;
  const int nt = nl < 4 ? (xcd & 1) * 4 + nl : 8 + (xcd & 1) * 2 + (nl - 4);
  if (w >= 4) {
    const int col0 = nl >= 4 ? (nt - 8) * 128 : 512 + nt * 64;
    const int col1 = nl >= 4 ? col0 + 64 : col0 + 512;
    k1_prod(lds, x, wqkv, m, col0, col1, (2 * ml + nl) & 7, w - 4, lane);
    return;
  }
  if (nl < 4) k1_gemm<false>(lds, ws, m, nt, w, lane);
  else k1_gemm<true>(lds, ws, m, nt, w, lane);
  TS(0, 3);
}

DI void k2_fft(char* lds, char* ws, int c, int w, int lane) {
  const int i = lane & 15, g = lane >> 4;
  u32x4 xa[2][4], wf[2];
  const char* strip = ws + WS_X1T + (size_t)c * 8192 + lane * 16;
#pragma unroll
  for (int ks = 0; ks < 4; ks++)
#pragma unroll
    for (int mt = 0; mt < 2; mt++) xa[mt][ks] = *(const u32x4*)(strip + (mt * 4 + ks) * 1024);
#pragma unroll
  for (int mt = 0; mt < 2; mt++) wf[mt] = *(const u32x4*)(ws + WS_WT + (size_t)c * 8192 + ((w * 2 + mt) * 64 + lane) * 16);
  const int k1 = 16 * w + i;
  const float fk1 = (float)k1, fg = (float)g;
  u32x4 fre[4], fim[4], fc[2], fs[2], fn[2];
  {
    const float st = fk1 * (1.f / 64.f);
    const float a0 = fg * 4.f * st;
#pragma unroll
    for (int ks = 0; ks < 4; ks++) {
#pragma unroll
      for (int d = 0; d < 4; d++) {
        const float ang = __builtin_fmaf((float)(16 * ks + d), st, a0);
        const float cs = fcos(ang), sn = fsin(ang);
        fre[ks][d] = pk(cs, sn); fim[ks][d] = pk(sn, cs) ^ 0x8000u;
      }
      PIN(fre[ks]); PIN(fim[ks]);
    }
  }
  float c1[2][4], s1[2][4], c2[2][4], s2[2][4];
#pragma unroll
  for (int mt = 0; mt < 2; mt++) {
    const float fa = (float)perm32(mt, i);
    const float st = fa * (1.f / 32.f), a0 = fg * 8.f * st;
    const float st1 = fk1 * (1.f / 2048.f), b0 = (fg * 8.f + (float)(4 * mt)) * st1;
    const float st2 = fa * (1.f / 2048.f), d0 = (fg * 4.f + (float)(16 * w)) * st2;
#pragma unroll
    for (int d = 0; d < 4; d++) {
      const float x0 = __builtin_fmaf((float)(2 * d), st, a0), x1 = __builtin_fmaf((float)(2 * d + 1), st, a0);
      fc[mt][d] = pk(fcos(x0), fcos(x1));
      fs[mt][d] = pk(fsin(x0), fsin(x1));
      fn[mt][d] = fs[mt][d] ^ 0x80008000u;
      const float t1 = __builtin_fmaf((float)d, st1, b0);
      c1[mt][d] = fcos(t1); s1[mt][d] = fsin(t1);
      const float t2 = __builtin_fmaf((float)d, st2, d0);
      c2[mt][d] = fcos(t2); s2[mt][d] = fsin(t2);
      PIN(c1[mt][d]); PIN(s1[mt][d]); PIN(c2[mt][d]); PIN(s2[mt][d]);
    }
    PIN(fc[mt]); PIN(fs[mt]); PIN(fn[mt]);
  }
  SCHED();
  TS(1, 1);
  TS(1, 2);
  SCHED();
  const f32x4 z4 = {0, 0, 0, 0};
  f32x4 re[2], im[2];
#pragma unroll
  for (int mt = 0; mt < 2; mt++) { re[mt] = z4; im[mt] = z4; }
#pragma unroll
  for (int ks = 0; ks < 4; ks++)
#pragma unroll
    for (int mt = 0; mt < 2; mt++) { re[mt] = mma(as8(xa[mt][ks]), as8(fre[ks]), re[mt]); im[mt] = mma(as8(xa[mt][ks]), as8(fim[ks]), im[mt]); }
  u32x4 pr, pi;
#pragma unroll
  for (int mt = 0; mt < 2; mt++) {
    float tr[4], ti[4];
#pragma unroll
    for (int r = 0; r < 4; r++) { tr[r] = re[mt][r] * c1[mt][r] + im[mt][r] * s1[mt][r]; ti[r] = im[mt][r] * c1[mt][r] - re[mt][r] * s1[mt][r]; }
    pr[2 * mt] = pk(tr[0], tr[1]); pr[2 * mt + 1] = pk(tr[2], tr[3]);
    pi[2 * mt] = pk(ti[0], ti[1]); pi[2 * mt + 1] = pk(ti[2], ti[3]);
  }
#pragma unroll
  for (int mt = 0; mt < 2; mt++) {
    re[mt] = mma(as8(fs[mt]), as8(pi), mma(as8(fc[mt]), as8(pr), z4));
    im[mt] = mma(as8(fn[mt]), as8(pr), mma(as8(fc[mt]), as8(pi), z4));
  }
#pragma unroll
  for (int mt = 0; mt < 2; mt++) {
    float tr[4], ti[4];
#pragma unroll
    for (int r = 0; r < 4; r++) {
      const float a = bflo(wf[mt][r]), b = bfhi(wf[mt][r]);
      tr[r] = re[mt][r] * a - im[mt][r] * b; ti[r] = re[mt][r] * b + im[mt][r] * a;
    }
    pr[2 * mt] = pk(tr[0], tr[1]); pr[2 * mt + 1] = pk(tr[2], tr[3]);
    pi[2 * mt] = pk(ti[0], ti[1]); pi[2 * mt + 1] = pk(ti[2], ti[3]);
  }
#pragma unroll
  for (int nt = 0; nt < 2; nt++) {
    re[nt] = mma(as8(pi), as8(fn[nt]), mma(as8(pr), as8(fc[nt]), z4));
    im[nt] = mma(as8(pi), as8(fc[nt]), mma(as8(pr), as8(fs[nt]), z4));
  }
#pragma unroll
  for (int nt = 0; nt < 2; nt++) {
    u32x4 v;
#pragma unroll
    for (int r = 0; r < 4; r++) v[r] = pk(re[nt][r] * c2[nt][r] - im[nt][r] * s2[nt][r], -re[nt][r] * s2[nt][r] - im[nt][r] * c2[nt][r]);
    *(u32x4*)(lds + ((nt * 4 + w) * 64 + lane) * 16) = v;
  }
  BAR();
#pragma unroll
  for (int mt = 0; mt < 2; mt++) { re[mt] = z4; im[mt] = z4; }
#pragma unroll
  for (int ks = 0; ks < 4; ks++)
#pragma unroll
    for (int mt = 0; mt < 2; mt++) {
      s16x8 ya = lrd(lds + ((mt * 4 + ks) * 64 + lane) * 16);
      re[mt] = mma(ya, as8(fre[ks]), re[mt]); im[mt] = mma(ya, as8(fim[ks]), im[mt]);
    }
  u32x4 vr = {pk(re[0][0], re[0][1]), pk(re[0][2], re[0][3]), pk(re[1][0], re[1][1]), pk(re[1][2], re[1][3])};
  u32x4 vi = {pk(im[0][0], im[0][1]), pk(im[0][2], im[0][3]), pk(im[1][0], im[1][1]), pk(im[1][2], im[1][3])};
  char* yt = ws + WS_YT + (32 * k1 + 8 * g) * 2;
  const int rr = (c == 256) ? 1 : 2 * c;
  *(u32x4*)(yt + (size_t)rr * 4096) = vr;
  if (c & 255) *(u32x4*)(yt + (size_t)(rr + 1) * 4096) = vi;
}

DI void k2_att(char* lds, char* ws, int t, int mb, int w, int lane) {
  const int g = lane >> 4;
  u32x4 pq[2];
  f32x4 kvl[2][4], zl[2][4];
#pragma unroll
  for (int h = 0; h < 2; h++)
#pragma unroll
    for (int pp = 0; pp < 4; pp++) {
      const int id = (4 * w + pp) * 32 + 2 * t + h;
      kvl[h][pp] = *(const f32x4*)(ws + WS_KVP + ((size_t)id * 64 + lane) * 16);
      zl[h][pp] = *(const f32x4*)(ws + WS_ZP + ((size_t)id * 4 + g) * 16);
    }
#pragma unroll
  for (int h = 0; h < 2; h++) pq[h] = *(const u32x4*)(ws + WS_PQ + ((size_t)((2 * t + h) * 64 + mb * 4 + w) * 64 + lane) * 16);
#pragma unroll
  for (int h = 0; h < 2; h++) {
    *(f32x4*)(lds + (((w * 2 + h) * 2 + 0) * 64 + lane) * 16) = (kvl[h][0] + kvl[h][1]) + (kvl[h][2] + kvl[h][3]);
    *(f32x4*)(lds + (((w * 2 + h) * 2 + 1) * 64 + lane) * 16) = (zl[h][0] + zl[h][1]) + (zl[h][2] + zl[h][3]);
  }
  TS(1, 1);
  BAR();
  TS(1, 2);
  f32x4 ra[2][4], rb[2][4];
#pragma unroll
  for (int h = 0; h < 2; h++)
#pragma unroll
    for (int ww = 0; ww < 4; ww++) {
      ra[h][ww] = *(const f32x4*)(lds + (((ww * 2 + h) * 2 + 0) * 64 + lane) * 16);
      rb[h][ww] = *(const f32x4*)(lds + (((ww * 2 + h) * 2 + 1) * 64 + lane) * 16);
    }
#pragma unroll
  for (int h = 0; h < 2; h++)
#pragma unroll
    for (int d = 0; d < 4; d++) pq[h][d] = pk(phi(bflo(pq[h][d])), phi(bfhi(pq[h][d])));
  u32x2 kvf[2], zf[2];
#pragma unroll
  for (int h = 0; h < 2; h++) {
    f32x4 a = (ra[h][0] + ra[h][1]) + (ra[h][2] + ra[h][3]), b = (rb[h][0] + rb[h][1]) + (rb[h][2] + rb[h][3]);
    kvf[h] = u32x2{pk(a[0], a[1]), pk(a[2], a[3])};
    zf[h] = u32x2{pk(b[0], b[1]), pk(b[2], b[3])};
  }
  const f32x4 z4 = {0, 0, 0, 0};
#pragma unroll
  for (int st = 0; st < 2; st++) {
    u32x4 o;
#pragma unroll
    for (int h = 0; h < 2; h++) {
      u32x2 b = {pq[h][2 * st], pq[h][2 * st + 1]};
      f32x4 num = mma16(as4(kvf[h]), as4(b), z4);
      f32x4 den = mma16(as4(zf[h]), as4(b), z4);
      const float rd = __builtin_amdgcn_rcpf(den[0] + 1e-6f);
      o[2 * h] = pk(num[0] * rd, num[1] * rd);
      o[2 * h + 1] = pk(num[2] * rd, num[3] * rd);
    }
    *(u32x4*)(ws + WS_OF + ((size_t)((mb * 8 + w * 2 + st) * 16 + t) * 64 + lane) * 16) = o;
  }
}

__global__ __launch_bounds__(256, 2) void k2(char* ws) {
  __shared__ __attribute__((aligned(16))) char lds[16384];
  const int b = blockIdx.x;
  const int w = __builtin_amdgcn_readfirstlane(threadIdx.x >> 6), lane = threadIdx.x & 63;
  TS(1, 0);
  if (b < 257) k2_fft(lds, ws, b, w, lane);
  else k2_att(lds, ws, (b - 257) & 15, (b - 257) >> 4, w, lane);
  TS(1, 3);
}

__global__ __launch_bounds__(512, 2) void k3(const char* wo, char* ws, char* out) {
  __shared__ __attribute__((aligned(16))) char lds[131072];
  const int b = blockIdx.x, xcd = b & 7, ii = b >> 3;
  const int w8 = __builtin_amdgcn_readfirstlane(threadIdx.x >> 6), lane = threadIdx.x & 63;
  const int w = w8 & 3;
  TS(2, 0);
  const int ml = ii & 3, ntile = ii >> 2;
  const int mtile = xcd * 4 + ml;
  const int rot = (ml + ntile) & 3;
  if (w8 >= 4) {
    const int swz = (((lane & 7) >> 1) ^ ((lane >> 4) & 3)) * 32 + (lane & 1) * 16;
    const unsigned voffW = (lane >> 3) * 1024 + swz, voffY = (lane >> 3) * 4096 + swz, voffR = lane * 16;
    const char* ra = ws + WS_OF + (size_t)(4 * mtile + w) * 16384;
    const char* rb = ws + WS_GF + (size_t)(4 * ntile + w) * 16384;
    const char* ta = wo + (size_t)(w * 32) * 1024 + ntile * 128;
    const char* tb = ws + WS_YT + (size_t)(w * 32) * 4096 + mtile * 128;
    const unsigned lbase = laddr_of(lds) + w * 4096;
    produce([&](int idx) {
      const int st = ((idx >> 1) + rot) & 3;
      const unsigned slot = lbase + (idx & 3) * 32768;
      if ((idx & 1) == 0) {
        glds4(slot, ra + st * 4096, voffR, voffR, voffR, voffR);
        glds4(slot + 16384, ta + (size_t)(st * 128) * 1024, voffW, voffW + 7168, voffW + 2 * 7168, voffW + 3 * 7168);
      } else {
        glds4(slot, rb + st * 4096, voffR, voffR, voffR, voffR);
        glds4(slot + 16384, tb + (size_t)(st * 128) * 4096, voffY, voffY + 31744, voffY + 2 * 31744, voffY + 3 * 31744);
      }
    });
    return;
  }
  const int i = lane & 15, g = lane >> 4, q = i >> 2, p = lane & 3;
  const int wn = w & 1, wsn = w >> 1;
  f32x2 gv[2];
#pragma unroll
  for (int c = 0; c < 2; c++) gv[c] = *(const f32x2*)(ws + WS_GT + (size_t)(64 * mtile + 32 * wsn + 16 * c + i) * 8);
  f32x4 accA[2][2], accB[2][2];
#pragma unroll
  for (int a = 0; a < 2; a++)
#pragma unroll
    for (int c = 0; c < 2; c++) { accA[a][c] = f32x4{0, 0, 0, 0}; accB[a][c] = f32x4{0, 0, 0, 0}; }
  const int fsw = (2 * g + (q >> 1)) & 3;
  const int trow = 16384 + (4 * g + q) * 128;
  const int toffA = trow + (((2 * wn + (p >> 1)) ^ fsw) * 32) + (p & 1) * 16;
  const int toffB = trow + (((2 * wsn) ^ fsw) * 32) + p * 8;
  s16x8 ta_[4][2], ra_[4][2], tb_[4][2], rb_[4][2];
  auto rdA = [&](const char* slot) {
#pragma unroll
    for (int ks = 0; ks < 4; ks++)
#pragma unroll
      for (int a = 0; a < 2; a++) {
        ta_[ks][a] = trd2(slot + toffA + 8 * a + ks * 4096, slot + toffA + 8 * a + ks * 4096 + 2048);
        ra_[ks][a] = lrd(slot + wsn * 8192 + a * 4096 + ks * 1024 + lane * 16);
      }
  };
  auto rdB = [&](const char* slot) {
#pragma unroll
    for (int ks = 0; ks < 4; ks++)
#pragma unroll
      for (int a = 0; a < 2; a++) {
        tb_[ks][a] = trd2(slot + (toffB ^ (32 * a)) + ks * 4096, slot + (toffB ^ (32 * a)) + ks * 4096 + 2048);
        rb_[ks][a] = lrd(slot + wn * 8192 + a * 4096 + ks * 1024 + lane * 16);
      }
  };
  auto mmA = [&]() {
#pragma unroll
    for (int ks = 0; ks < 4; ks++)
#pragma unroll
      for (int a = 0; a < 2; a++)
#pragma unroll
        for (int c = 0; c < 2; c++) accA[a][c] = mma(ta_[ks][a], ra_[ks][c], accA[a][c]);
  };
  auto mmB = [&]() {
#pragma unroll
    for (int ks = 0; ks < 4; ks++)
#pragma unroll
      for (int a = 0; a < 2; a++)
#pragma unroll
        for (int c = 0; c < 2; c++) accB[a][c] = mma(rb_[ks][a], tb_[ks][c], accB[a][c]);
  };
  auto ilv = [&]() {
#pragma unroll
    for (int t = 0; t < 12; t++) { SGB(0x100, 2); SGB(0x008, 1); }
    SGB(0x008, 4);
  };
  __builtin_amdgcn_s_waitcnt(0xC07F);
  BAR();
  TS(2, 1);
  rdA(lds);
  SCHED();
#pragma unroll 1
  for (int it = 0; it < 3; it++) {
    BAR(); SCHED();
    rdB(lds + ((2 * it + 1) & 3) * 32768); mmA(); ilv(); SCHED();
    BAR(); SCHED();
    rdA(lds + ((2 * it + 2) & 3) * 32768); mmB(); ilv(); SCHED();
  }
  BAR(); SCHED();
  rdB(lds + 3 * 32768); mmA(); ilv(); SCHED();
  mmB();
  TS(2, 2);
#pragma unroll
  for (int c = 0; c < 2; c++) {
    const int s = 64 * mtile + 32 * wsn + 16 * c + i;
    u32x4 v;
#pragma unroll
    for (int a = 0; a < 2; a++) {
      v[2 * a] = pk(gv[c][0] * accA[a][c][0] + gv[c][1] * accB[a][c][0], gv[c][0] * accA[a][c][1] + gv[c][1] * accB[a][c][1]);
      v[2 * a + 1] = pk(gv[c][0] * accA[a][c][2] + gv[c][1] * accB[a][c][2], gv[c][0] * accA[a][c][3] + gv[c][1] * accB[a][c][3]);
    }
    *(u32x4*)(out + (size_t)s * 1024 + (64 * ntile + 32 * wn + 8 * g) * 2) = v;
  }
  TS(2, 3);
}

extern "C" void kernel_launch(void* const* d_in, const int* in_sizes, int n_in, void* d_out, int out_size, void* d_ws, size_t ws_size, hipStream_t stream) {
  const char* x = (const char*)d_in[0];
  const char* wqkv = (const char*)d_in[1];
  const char* wo = (const char*)d_in[2];
  const unsigned short* wr = (const unsigned short*)d_in[4];
  const unsigned short* wi = (const unsigned short*)d_in[5];
  const char* gw = (const char*)d_in[6];
  char* ws = (char*)d_ws;
  k1<<<256, 512, 0, stream>>>(x, wqkv, wr, wi, gw, ws);
  k2<<<513, 256, 0, stream>>>(ws);
  k3<<<256, 512, 0, stream>>>(wo, ws, (char*)d_out);
}
```

```cpp
#include <hip/hip_runtime.h>
#include <stdint.h>

typedef short s16x4 __attribute__((ext_vector_type(4)));
typedef short s16x8 __attribute__((ext_vector_type(8)));
typedef float f32x4 __attribute__((ext_vector_type(4)));
typedef float f32x2 __attribute__((ext_vector_type(2)));
typedef unsigned u32x4 __attribute__((ext_vector_type(4)));
typedef unsigned u32x2 __attribute__((ext_vector_type(2)));
typedef __bf16 bfx2 __attribute__((ext_vector_type(2)));
#define LAS __attribute__((address_space(3)))
#define DI __device__ __forceinline__
#define ROT1 6
#define ROT3 3

static constexpr size_t WS_X1T = 0;
static constexpr size_t WS_WT  = WS_X1T + (9u << 18);
static constexpr size_t WS_PQ  = WS_WT + (9u << 18);
static constexpr size_t WS_KVP = WS_PQ + (2u << 20);
static constexpr size_t WS_ZP  = WS_KVP + (512u << 10);
static constexpr size_t WS_GT  = WS_ZP + (32u << 10);
static constexpr size_t WS_GF  = WS_GT + (16u << 10);
static constexpr size_t WS_OF  = WS_GF + (512u << 10);
static constexpr size_t WS_YT  = WS_OF + (2u << 20);

DI unsigned pk(float lo, float hi) { f32x2 p = {lo, hi}; return __builtin_bit_cast(unsigned, __builtin_convertvector(p, bfx2)); }
DI float bflo(unsigned u) { return __builtin_bit_cast(float, u << 16); }
DI float bfhi(unsigned u) { return __builtin_bit_cast(float, u & 0xffff0000u); }
DI float rbf(float v) { return bflo(pk(v, 0.f)); }
DI s16x8 as8(u32x4 v) { return __builtin_bit_cast(s16x8, v); }
DI s16x4 as4(u32x2 v) { return __builtin_bit_cast(s16x4, v); }
DI f32x4 mma(s16x8 a, s16x8 b, f32x4 c) { return __builtin_amdgcn_mfma_f32_16x16x32_bf16(a, b, c, 0, 0, 0); }
DI f32x4 mma16(s16x4 a, s16x4 b, f32x4 c) { return __builtin_amdgcn_mfma_f32_16x16x16bf16_1k(a, b, c, 0, 0, 0); }
DI void mfa(f32x4& c, s16x8 a, s16x8 b) { asm volatile("v_mfma_f32_16x16x32_bf16 %0, %1, %2, %0" : "+v"(c) : "v"(a), "v"(b)); }
DI s16x4 trd(const char* l) { return __builtin_amdgcn_ds_read_tr16_b64_v4i16((LAS s16x4*)l); }
DI s16x8 cat(s16x4 a, s16x4 b) { return __builtin_shufflevector(a, b, 0, 1, 2, 3, 4, 5, 6, 7); }
DI float fsin(float rev) { return __builtin_amdgcn_sinf(rev); }
DI float fcos(float rev) { return __builtin_amdgcn_cosf(rev); }
DI void glds4(unsigned laddr, const char* sb, unsigned v0, unsigned v1, unsigned v2, unsigned v3) {
  asm volatile("s_mov_b32 m0, %0\n\ts_nop 0\n\t"
               "global_load_lds_dwordx4 %2, %1\n\t"
               "global_load_lds_dwordx4 %3, %1 offset:1024\n\t"
               "global_load_lds_dwordx4 %4, %1 offset:2048\n\t"
               "global_load_lds_dwordx4 %5, %1 offset:3072"
               :: "s"(laddr), "s"(sb), "v"(v0), "v"(v1), "v"(v2), "v"(v3) : "memory");
}
DI unsigned laddr_of(const char* l) { return (unsigned)(size_t)(const LAS char*)l; }
DI s16x8 trd2(const char* l0, const char* l1) {
  s16x4 a = __builtin_amdgcn_ds_read_tr16_b64_v4i16((LAS s16x4*)l0);
  s16x4 b = __builtin_amdgcn_ds_read_tr16_b64_v4i16((LAS s16x4*)l1);
  return __builtin_shufflevector(a, b, 0, 1, 2, 3, 4, 5, 6, 7);
}
DI s16x8 lrd(const char* l) { return *(const s16x8*)l; }
DI int perm32(int t, int i) { return 8 * (i >> 2) + 4 * t + (i & 3); }
DI float phi(float t) {
  float e = __builtin_amdgcn_exp2f(t * 1.44269504088896341f);
  e = __builtin_amdgcn_fmed3f(e, 0.f, 1.f);
  return fmaxf(t + 1e-6f, 1e-6f) + e;
}
#define TSP
#define TSA
#define TSDECL
#define TS(kern, slot)
#define TSFLUSH(kern)
DI void st_wt(char* p, u32x4 v) { asm volatile("global_store_dwordx4 %0, %1, off sc1\n\ts_nop 1" :: "v"(p), "v"(v) : "memory"); }
#define SCHED() __builtin_amdgcn_sched_barrier(0)
#define SGB(mask, n) __builtin_amdgcn_sched_group_barrier(mask, n, 0)
#define PIN(x) asm volatile("" : "+v"(x))
#define WAITV(n) asm volatile("s_waitcnt vmcnt(" #n ")" ::: "memory")
#define BAR() asm volatile("s_waitcnt lgkmcnt(0)\n\ts_barrier" ::: "memory")

template <class F> DI void produce(F issue) {
  issue(0); issue(1);
  WAITV(8); BAR();
  issue(2); issue(3);
#pragma unroll 1
  for (int j = 1; j < 5; j++) { WAITV(16); BAR(); issue(j + 3); }
  WAITV(16); BAR();
  WAITV(8); BAR();
  WAITV(0); BAR();
}

DI void k1_prod(char* lds, const char* x, const char* wqkv, int m, int col0, int col1, int rot, int w, int lane) {
  unsigned voffA[4], voffB[4];
#pragma unroll
  for (int j = 0; j < 4; j++) {
    const int par = j & 1;
    voffA[j] = (lane >> 3) * 1024 + (((lane & 7) ^ ((4 * par + (lane >> 4)) & 7)) * 16) + j * (8192 - 1024);
    voffB[j] = (lane >> 3) * 3072 + ((((lane & 7) >> 1) ^ (((lane >> 4) & 1) | (par << 1))) * 32) + (lane & 1) * 16 + j * (24576 - 1024);
  }
  const unsigned lbase = laddr_of(lds) + w * 4096;
  const char* xa = x + (size_t)(m * 128 + w * 32) * 1024;
  const char* wb = wqkv + (size_t)((w & 1) * 32) * 3072 + ((w >> 1) ? col1 : col0) * 2;
  produce([&](int idx) {
    const int ks = (idx + rot) & 7;
    const unsigned slot = lbase + (idx & 3) * 32768;
    glds4(slot, xa + ks * 128, voffA[0], voffA[1], voffA[2], voffA[3]);
    glds4(slot + 16384, wb + (size_t)(ks * 64) * 3072, voffB[0], voffB[1], voffB[2], voffB[3]);
  });
}

template <bool QT>
DI void k1_gemm(TSP char* lds, char* ws, int m, int nt, int w, int lane) {
  const int i = lane & 15, g = lane >> 4, q = i >> 2, p = lane & 3;
  f32x4 acc[8][2];
#pragma unroll
  for (int a = 0; a < 8; a++) { acc[a][0] = f32x4{0, 0, 0, 0}; acc[a][1] = f32x4{0, 0, 0, 0}; }
  const int aoff = i * 128 + ((g ^ (i >> 1)) * 16);
  const int boff = 16384 + (8 * g + q) * 128 + ((w ^ (((q >> 1) & 1) | ((g & 1) << 1))) * 32) + p * 8;
  s16x8 A[2][2][8]; s16x4 B[2][2][2][2];
  auto rd1 = [&](int buf, const char* slot, int r) {
    const int ks = r / 12, rr = r % 12;
    const int t = rr == 0 ? 0 : rr == 1 ? 1 : rr == 3 ? 2 : rr == 4 ? 3 : -1;
    const int a = rr == 2 ? 0 : rr - 4;
    if (t >= 0) B[buf][ks][t >> 1][t & 1] = trd(slot + boff + (t >> 1) * 8192 + ks * 4096 + (t & 1) * 512);
    else A[buf][ks][a] = lrd(slot + (aoff ^ (ks * 64)) + a * 2048);
  };
  auto mf1 = [&](int buf, int ks, int mt, int hh) {
    const s16x8 bf = cat(B[buf][ks][hh][0], B[buf][ks][hh][1]);
    if (QT) mfa(acc[mt][hh], bf, A[buf][ks][mt]); else mfa(acc[mt][hh], A[buf][ks][mt], bf);
  };
  auto region = [&](int cur, const char* slot) {
#pragma unroll
    for (int mi = 0; mi < 32; mi++) {
      mf1(cur, mi >> 4, (mi & 15) >> 1, mi & 1);
      SCHED();
      if (mi < 24) { rd1(cur ^ 1, slot, mi); SCHED(); }
    }
  };
  __builtin_amdgcn_s_waitcnt(0xC07F);
  BAR();
  TS(0, 1);
#pragma unroll
  for (int r = 0; r < 24; r++) rd1(0, lds, r);
  SCHED();
#pragma unroll 1
  for (int it = 0; it < 3; it++) {
    BAR(); SCHED();
    region(0, lds + ((2 * it + 1) & 3) * 32768);
    BAR(); SCHED();
    region(1, lds + ((2 * it + 2) & 3) * 32768);
  }
  BAR(); SCHED();
  region(0, lds + 3 * 32768);
  asm volatile("s_nop 7");
  SCHED();
  auto mmh = [&](int hh) {
#pragma unroll
    for (int mt = 0; mt < 8; mt++)
#pragma unroll
      for (int ks = 0; ks < 2; ks++) {
        const s16x8 bf = cat(B[1][ks][hh][0], B[1][ks][hh][1]);
        acc[mt][hh] = QT ? mma(bf, A[1][ks][mt], acc[mt][hh]) : mma(A[1][ks][mt], bf, acc[mt][hh]);
      }
  };
  mmh(0);
  SCHED();
  TS(0, 2);
  if (QT) {
    mmh(1);
#pragma unroll
    for (int hh = 0; hh < 2; hh++) {
      const int head = (nt - 8) * 8 + hh * 4 + w;
#pragma unroll
      for (int u = 0; u < 4; u++) {
        u32x4 v = {pk(acc[2 * u][hh][0], acc[2 * u][hh][1]), pk(acc[2 * u][hh][2], acc[2 * u][hh][3]),
                   pk(acc[2 * u + 1][hh][0], acc[2 * u + 1][hh][1]), pk(acc[2 * u + 1][hh][2], acc[2 * u + 1][hh][3])};
        *(u32x4*)(ws + WS_PQ + ((size_t)(head * 64 + m * 4 + u) * 64 + lane) * 16) = v;
      }
    }
  } else {
    mmh(1);
    u32x4 pa[4];
#pragma unroll
    for (int u = 0; u < 4; u++)
      pa[u] = u32x4{pk(phi(acc[2 * u][0][0]), phi(acc[2 * u][0][1])), pk(phi(acc[2 * u][0][2]), phi(acc[2 * u][0][3])),
                    pk(phi(acc[2 * u + 1][0][0]), phi(acc[2 * u + 1][0][1])), pk(phi(acc[2 * u + 1][0][2]), phi(acc[2 * u + 1][0][3]))};
#pragma unroll
    for (int t = 0; t < 16; t++) { SGB(0x008, 1); SGB(0x002, 2); }
    SCHED();
    f32x4 kv = {0, 0, 0, 0}, z = {0, 0, 0, 0};
    const u32x4 ones = {0x3f803f80u, 0x3f803f80u, 0x3f803f80u, 0x3f803f80u};
#pragma unroll
    for (int u = 0; u < 4; u++) {
      u32x4 b = {pk(acc[2 * u][1][0], acc[2 * u][1][1]), pk(acc[2 * u][1][2], acc[2 * u][1][3]),
                 pk(acc[2 * u + 1][1][0], acc[2 * u + 1][1][1]), pk(acc[2 * u + 1][1][2], acc[2 * u + 1][1][3])};
      z = mma(as8(pa[u]), as8(ones), z);
      kv = mma(as8(pa[u]), as8(b), kv);
    }
    const int head = nt * 4 + w;
    u32x4 v = {pk(kv[0], kv[1]), pk(kv[2], kv[3]), pk(z[0], z[1]), pk(z[2], z[3])};
    *(u32x4*)(ws + WS_KVP + ((size_t)(m * 32 + head) * 64 + lane) * 16) = v;
  }
}

DI float dpp_sum16(float v) {
  v += __builtin_bit_cast(float, __builtin_amdgcn_update_dpp(0, __builtin_bit_cast(int, v), 0x128, 0xf, 0xf, false));
  v += __builtin_bit_cast(float, __builtin_amdgcn_update_dpp(0, __builtin_bit_cast(int, v), 0x124, 0xf, 0xf, false));
  v += __builtin_bit_cast(float, __builtin_amdgcn_update_dpp(0, __builtin_bit_cast(int, v), 0x122, 0xf, 0xf, false));
  v += __builtin_bit_cast(float, __builtin_amdgcn_update_dpp(0, __builtin_bit_cast(int, v), 0x121, 0xf, 0xf, false));
  return v;
}

DI void k1_aux(TSP char* lds, const char* x, const char* wr, const char* wi, const char* gw, char* ws, int a, int w, int lane) {
  const int i = lane & 15, g = lane >> 4;
  char* LX = lds;
  char* LS = lds + 32768;
  char* LF = lds + 69632;
  const int amt = (a >> 1) & 1;
  const int n2s = perm32(amt, 8 * (a & 1) + w);
  {
    const unsigned v = lane * 16;
    const int k0 = 8 * (a & 7) + 64 * (4 * (a >> 3) + (w & 3));
    const char* fsrc = ((w >> 2) ? wi : wr) + (size_t)k0 * 514;
    glds4(laddr_of(LF) + w * 4352, fsrc, v, v, v, v);
    if (lane == 0) asm volatile("s_mov_b32 m0, %0\n\ts_nop 0\n\tglobal_load_lds_dwordx4 %2, %1" :: "s"(laddr_of(LF) + w * 4352 + 4096), "s"(fsrc + 4096), "v"(v) : "memory");
    glds4(laddr_of(LX) + w * 4096, x + (size_t)(128 * (a >> 2) + n2s) * 1024, v, v + 31 * 1024, v + 62 * 1024, v + 93 * 1024);
  }
  u32x4 gwv = *(const u32x4*)(gw + lane * 16);
  u32x4 b1re, b1im, a3re, a3im, a4re, a4im;
  float twc[4], tws[4];
  const float fi = (float)i;
  const float g4 = (float)(4 * g);
#pragma unroll
  for (int d = 0; d < 4; d++) {
    float c[2], s[2];
#pragma unroll
    for (int e = 0; e < 2; e++) {
      const int j = 2 * d + e;
      const float ang = (g4 + (float)(16 * (j >> 2) + (j & 3))) * (fi * (1.f / 32.f));
      c[e] = fcos(ang);
      s[e] = (i == 0) ? (e ? -1.f : 1.f) : fsin(-ang);
    }
    b1re[d] = pk(c[0], c[1]); b1im[d] = pk(s[0], s[1]);
    const float n2i = (g4 + (float)d) * fi;
    {
      const float ang = n2i * (1.f / 16.f);
      const float cs = fcos(ang), sn = fsin(ang);
      a3re[d] = pk(cs, sn);
      a3im[d] = pk(sn, cs) ^ ((i >= 8) ? 0x80000000u : 0x00008000u);
    }
    {
      const float ang = n2i * (1.f / 32.f);
      const float cs = fcos(ang);
      const float sn = (i == 0) ? ((d & 1) ? -1.f : 1.f) : fsin(-ang);
      const unsigned u = pk(cs, sn);
      const int sh = (i & 1) * 16;
      a4re[d] = (u & 0xffffu) << sh;
      a4im[d] = (u >> 16) << sh;
    }
    {
      const float ang = n2i * (1.f / 512.f);
      twc[d] = fcos(ang); tws[d] = fsin(ang);
      PIN(twc[d]); PIN(tws[d]);
    }
  }
  PIN(b1re); PIN(b1im); PIN(a3re); PIN(a3im); PIN(a4re); PIN(a4im);
  int soff[4];
#pragma unroll
  for (int r = 0; r < 4; r++) {
    const int k2 = 4 * g + r;
    const int k = i + 32 * k2;
    const int col = (i == 0) ? 16 * k2 : (k2 < 8 ? k : 512 - k);
    soff[r] = col * 128 + ((w ^ (col & 7)) * 16);
    PIN(soff[r]);
  }
  TS(0, 1);
  WAITV(0);
  BAR();
  TS(0, 2);
  const int sl = lane & 7;
  const int wline = ((((a & 7) >> 1) * 2 + ((a >> 3) & 1)) * 64 + (a >> 4) * 16 + (a & 1) * 8) * 16;
  const int cb = (lane >> 3) * 16 + (w & 3) + 128 * (w >> 2);
  const char* fp = LF + sl * 514 + cb * 2;
  {
    unsigned o[4][4];
#pragma unroll
    for (int a4 = 0; a4 < 4; a4++) {
      u32x4 fv;
#pragma unroll
      for (int r = 0; r < 4; r++)
        fv[r] = (unsigned)*(const unsigned short*)(fp + r * 4352 + a4 * 8) | ((unsigned)*(const unsigned short*)(fp + (4 + r) * 4352 + a4 * 8) << 16);
      const char* row = LX + (w * 4 + a4) * 1024;
      s16x8 xf = trd2(row + lane * 8, row + 512 + lane * 8);
      st_wt(ws + WS_WT + (size_t)(cb + 4 * a4) * 8192 + wline + sl * 16, fv);
      const f32x4 z4 = {0, 0, 0, 0};
      f32x4 are = mma(xf, as8(b1re), z4), aim = mma(xf, as8(b1im), z4);
      f32x4 tr, ti;
#pragma unroll
      for (int r = 0; r < 4; r++) { tr[r] = are[r] * twc[r] + aim[r] * tws[r]; ti[r] = aim[r] * twc[r] - are[r] * tws[r]; }
      asm("" : "+v"(tr), "+v"(ti));
      u32x4 bb;
#pragma unroll
      for (int r = 0; r < 4; r++) bb[r] = pk(tr[r], ti[r]);
      f32x4 xre = mma(as8(a3re), as8(bb), z4), xim = mma(as8(a3im), as8(bb), z4);
      f32x4 sre = mma(as8(a4re), as8(bb), z4), sim = mma(as8(a4im), as8(bb), z4);
#pragma unroll
      for (int r = 0; r < 4; r++) o[r][a4] = pk((i == 0) ? sre[r] : xre[r], (i == 0) ? sim[r] : xim[r]);
      PIN(o[0][a4]); PIN(o[1][a4]); PIN(o[2][a4]); PIN(o[3][a4]);
    }
    if (lane == 0) {
      u32x4 v = {o[0][0] >> 16, o[0][1] >> 16, o[0][2] >> 16, o[0][3] >> 16};
      *(u32x4*)(LS + 256 * 128 + w * 16) = v;
#pragma unroll
      for (int a4 = 0; a4 < 4; a4++) o[0][a4] &= 0xffffu;
    }
#pragma unroll
    for (int r = 0; r < 4; r++) {
      u32x4 v = {o[r][0], o[r][1], o[r][2], o[r][3]};
      *(u32x4*)(LS + soff[r]) = v;
    }
  }
  BAR();
  TS(0, 4);
  const int sc = w * 8 + (lane >> 3);
  const int xline = (amt * 4 + (a >> 4)) * 1024 + ((a >> 2) & 3) * 256 + (a & 1) * 128;
  const u32x4 ones = {0x3f803f80u, 0x3f803f80u, 0x3f803f80u, 0x3f803f80u};
  float gsum[4];
#pragma unroll
  for (int j = 0; j < 4; j++) {
    const int c = j * 64 + sc;
    u32x4 v0 = *(const u32x4*)(LS + c * 128 + ((sl ^ (c & 7)) * 16));
    u32x4 xv = *(const u32x4*)(LX + (w * 4 + j) * 1024 + lane * 16);
    st_wt(ws + WS_X1T + (size_t)c * 8192 + xline + sl * 16, v0);
    u32x4 pr;
#pragma unroll
    for (int d = 0; d < 4; d++) pr[d] = pk(bflo(xv[d]) * bflo(gwv[d]), bfhi(xv[d]) * bfhi(gwv[d]));
    gsum[j] = mma(as8(ones), as8(pr), f32x4{0, 0, 0, 0})[0];
    PIN(gsum[j]);
  }
  TS(0, 5);
  if (w == 6 && lane < 8) st_wt(ws + WS_X1T + (size_t)256 * 8192 + xline + lane * 16, *(const u32x4*)(LS + 256 * 128 + lane * 16));
  if (w == 7 && lane < 8) {
    u32x4 v;
#pragma unroll
    for (int r = 0; r < 4; r++) v[r] = (unsigned)*(const unsigned short*)(LF + sl * 514 + 512 + r * 4352) | ((unsigned)*(const unsigned short*)(LF + sl * 514 + 512 + (4 + r) * 4352) << 16);
    st_wt(ws + WS_WT + (size_t)256 * 8192 + wline + sl * 16, v);
  }
  {
    float gs = (g == 0) ? gsum[0] : (g == 1) ? gsum[1] : (g == 2) ? gsum[2] : gsum[3];
    gs = rbf(dpp_sum16(gs));
    const float gg = rbf(__builtin_amdgcn_rcpf(1.f + __builtin_amdgcn_exp2f(gs * -1.44269504088896341f)));
    const int s = 32 * (4 * (a >> 2) + g) + n2s;
    if (i == 0) __hip_atomic_store((unsigned*)(ws + WS_GT + (s >> 6) * 256 + (s & 15) * 16 + ((s >> 4) & 3) * 4), pk(gg, 1.f - gg), __ATOMIC_RELAXED, __HIP_MEMORY_SCOPE_AGENT);
  }
}

extern "C" __global__ __launch_bounds__(512, 2) void gm_k1(const char* x, const char* wqkv, const char* wr, const char* wi, const char* gw, const char* wo, char* ws) {
  __shared__ __attribute__((aligned(16))) char lds[131072];
  const int b = blockIdx.x, xcd = b & 7, ii = b >> 3;
  const int w = __builtin_amdgcn_readfirstlane(threadIdx.x >> 6), lane = threadIdx.x & 63;
  TSDECL
  TS(0, 0);
  const int ml = ii & 3, nl = ii >> 2;
  const int m = (xcd >> 1) * 4 + ml;
  const int nt = nl < 4 ? (xcd & 1) * 4 + nl : 8 + (xcd & 1) * 2 + (nl - 4);
  if (w < 4 || ii >= 24) {
    if (ii >= 24) { k1_aux(TSA lds, x, wr, wi, gw, ws, xcd * 8 + (ii - 24), w, lane); TS(0, 3); TSFLUSH(0); return; }
    unsigned pfc = 0;
    {
      unsigned long long pc; int o0, o1;
      asm volatile("s_getpc_b64 %0\n.Lgm_pc%=:\n\ts_mov_b32 %1, gm_k1-.Lgm_pc%=\n\ts_mov_b32 %2, gm_kend-.Lgm_pc%=" : "=s"(pc), "=s"(o0), "=s"(o1));
      const unsigned long long c0 = pc + (long long)o0, c1 = pc + (long long)o1;
      const unsigned long long pa = c0 + (unsigned long long)((ii * 4 + w) * 64 + lane) * 128;
      if (pa < c1) pfc = *(const __attribute__((address_space(1))) unsigned*)pa;
    }
    if (nl < 4) k1_gemm<false>(TSA lds, ws, m, nt, w, lane);
    else k1_gemm<true>(TSA lds, ws, m, nt, w, lane);
    asm volatile("" :: "v"(pfc));
    TS(0, 3); TSFLUSH(0);
    return;
  }
  const int col0 = nl >= 4 ? (nt - 8) * 128 : 512 + nt * 64;
  const int col1 = nl >= 4 ? col0 + 64 : col0 + 512;
  k1_prod(lds, x, wqkv, m, col0, col1, ((2 * ml + nl) & ROT1), w - 4, lane);
  if (ii < 16) {
    const unsigned pf = *(const unsigned*)(wo + (size_t)((ii * 4 + (w - 4)) * 64 + lane) * 128);
    asm volatile("" :: "v"(pf));
  }
}

DI void k2_fft(TSP char* lds, char* ws, int c, int w, int lane) {
  const int i = lane & 15, g = lane >> 4;
  u32x4 wf[2];
  asm volatile("s_mov_b32 m0, %0\n\ts_nop 0\n\tglobal_load_lds_dwordx4 %2, %1\n\tglobal_load_lds_dwordx4 %2, %1 offset:1024"
               :: "s"(laddr_of(lds) + 8192 + w * 2048), "s"(ws + WS_X1T + (size_t)c * 8192 + w * 2048), "v"(lane * 16) : "memory");
#pragma unroll
  for (int mt = 0; mt < 2; mt++) wf[mt] = *(const u32x4*)(ws + WS_WT + (size_t)c * 8192 + ((w * 2 + mt) * 64 + lane) * 16);
  const int k1 = 16 * w + i;
  const float fk1 = (float)k1, fg = (float)g;
  u32x4 fre[4], fim[4], fc[2], fs[2], fn[2];
  {
    const float st = fk1 * (1.f / 64.f);
    const float a0 = fg * 4.f * st;
    const bool odd = i & 1;
    const unsigned m2 = odd ? 0x80008000u : 0u;
    const unsigned mre = (i & 2) ? 0x80008000u : 0u;
    const unsigned mim = ((i + 1) & 2) ? 0x80008000u : 0u;
#pragma unroll
    for (int d = 0; d < 4; d++) {
      const float ang = __builtin_fmaf((float)d, st, a0);
      const float cs = fcos(ang), sn = fsin(ang);
      const unsigned r0 = pk(cs, sn), i0 = pk(sn, cs) ^ 0x8000u;
      const unsigned r1 = (odd ? i0 : r0) ^ mre, i1 = (odd ? r0 : i0) ^ mim;
      fre[0][d] = r0; fim[0][d] = i0;
      fre[1][d] = r1; fim[1][d] = i1;
      fre[2][d] = r0 ^ m2; fim[2][d] = i0 ^ m2;
      fre[3][d] = r1 ^ m2; fim[3][d] = i1 ^ m2;
    }
#pragma unroll
    for (int ks = 0; ks < 4; ks++) { PIN(fre[ks]); PIN(fim[ks]); }
  }
  float c1[2][4], s1[2][4], c2[2][4], s2[2][4];
#pragma unroll
  for (int mt = 0; mt < 2; mt++) {
    const float fa = (float)perm32(mt, i);
    const float st = fa * (1.f / 32.f), a0 = fg * 8.f * st;
    const float st1 = fk1 * (1.f / 2048.f), b0 = (fg * 8.f + (float)(4 * mt)) * st1;
    const float st2 = fa * (1.f / 2048.f), d0 = (fg * 4.f + (float)(16 * w)) * st2;
#pragma unroll
    for (int d = 0; d < 4; d++) {
      const float x0 = __builtin_fmaf((float)(2 * d), st, a0), x1 = __builtin_fmaf((float)(2 * d + 1), st, a0);
      fc[mt][d] = pk(fcos(x0), fcos(x1));
      fs[mt][d] = pk(fsin(x0), fsin(x1));
      fn[mt][d] = fs[mt][d] ^ 0x80008000u;
      const float t1 = __builtin_fmaf((float)d, st1, b0);
      c1[mt][d] = fcos(t1); s1[mt][d] = fsin(t1);
      const float t2 = __builtin_fmaf((float)d, st2, d0);
      c2[mt][d] = fcos(t2); s2[mt][d] = fsin(t2);
      PIN(c1[mt][d]); PIN(s1[mt][d]); PIN(c2[mt][d]); PIN(s2[mt][d]);
    }
    PIN(fc[mt]); PIN(fs[mt]); PIN(fn[mt]);
  }
  SCHED();
  TS(1, 1);
  WAITV(2);
  BAR();
  TS(1, 2);
  const f32x4 z4 = {0, 0, 0, 0};
  f32x4 re[2], im[2];
#pragma unroll
  for (int mt = 0; mt < 2; mt++) { re[mt] = z4; im[mt] = z4; }
#pragma unroll
  for (int ks = 0; ks < 4; ks++)
#pragma unroll
    for (int mt = 0; mt < 2; mt++) {
      s16x8 xa = lrd(lds + 8192 + ((mt * 4 + ks) * 64 + lane) * 16);
      re[mt] = mma(xa, as8(fre[ks]), re[mt]); im[mt] = mma(xa, as8(fim[ks]), im[mt]);
    }
  u32x4 pr, pi;
#pragma unroll
  for (int mt = 0; mt < 2; mt++) {
    float tr[4], ti[4];
#pragma unroll
    for (int r = 0; r < 4; r++) { tr[r] = re[mt][r] * c1[mt][r] + im[mt][r] * s1[mt][r]; ti[r] = im[mt][r] * c1[mt][r] - re[mt][r] * s1[mt][r]; }
    pr[2 * mt] = pk(tr[0], tr[1]); pr[2 * mt + 1] = pk(tr[2], tr[3]);
    pi[2 * mt] = pk(ti[0], ti[1]); pi[2 * mt + 1] = pk(ti[2], ti[3]);
  }
#pragma unroll
  for (int mt = 0; mt < 2; mt++) {
    re[mt] = mma(as8(fs[mt]), as8(pi), mma(as8(fc[mt]), as8(pr), z4));
    im[mt] = mma(as8(fn[mt]), as8(pr), mma(as8(fc[mt]), as8(pi), z4));
  }
#pragma unroll
  for (int mt = 0; mt < 2; mt++) {
    float tr[4], ti[4];
#pragma unroll
    for (int r = 0; r < 4; r++) {
      const float a = bflo(wf[mt][r]), b = bfhi(wf[mt][r]);
      tr[r] = re[mt][r] * a - im[mt][r] * b; ti[r] = re[mt][r] * b + im[mt][r] * a;
    }
    pr[2 * mt] = pk(tr[0], tr[1]); pr[2 * mt + 1] = pk(tr[2], tr[3]);
    pi[2 * mt] = pk(ti[0], ti[1]); pi[2 * mt + 1] = pk(ti[2], ti[3]);
  }
#pragma unroll
  for (int nt = 0; nt < 2; nt++) {
    re[nt] = mma(as8(pi), as8(fn[nt]), mma(as8(pr), as8(fc[nt]), z4));
    im[nt] = mma(as8(pi), as8(fc[nt]), mma(as8(pr), as8(fs[nt]), z4));
  }
#pragma unroll
  for (int nt = 0; nt < 2; nt++) {
    u32x4 v;
#pragma unroll
    for (int r = 0; r < 4; r++) v[r] = pk(re[nt][r] * c2[nt][r] - im[nt][r] * s2[nt][r], -re[nt][r] * s2[nt][r] - im[nt][r] * c2[nt][r]);
    *(u32x4*)(lds + ((nt * 4 + w) * 64 + lane) * 16) = v;
  }
  BAR();
#pragma unroll
  for (int mt = 0; mt < 2; mt++) { re[mt] = z4; im[mt] = z4; }
#pragma unroll
  for (int ks = 0; ks < 4; ks++)
#pragma unroll
    for (int mt = 0; mt < 2; mt++) {
      s16x8 ya = lrd(lds + ((mt * 4 + ks) * 64 + lane) * 16);
      re[mt] = mma(ya, as8(fre[ks]), re[mt]); im[mt] = mma(ya, as8(fim[ks]), im[mt]);
    }
  u32x4 vr = {pk(re[0][0], re[0][1]), pk(re[0][2], re[0][3]), pk(re[1][0], re[1][1]), pk(re[1][2], re[1][3])};
  u32x4 vi = {pk(im[0][0], im[0][1]), pk(im[0][2], im[0][3]), pk(im[1][0], im[1][1]), pk(im[1][2], im[1][3])};
  char* yt = ws + WS_YT + (32 * k1 + 8 * g) * 2;
  const int rr = (c == 256) ? 1 : 2 * c;
  *(u32x4*)(yt + (size_t)rr * 4096) = vr;
  if (c & 255) *(u32x4*)(yt + (size_t)(rr + 1) * 4096) = vi;
}

DI void k2_att(TSP char* lds, char* ws, int bb, int w, int lane) {
  const int t = bb & 15, mb = bb >> 4;
  u32x4 pq[2], kl[2][4];
#pragma unroll
  for (int h = 0; h < 2; h++)
#pragma unroll
    for (int pp = 0; pp < 4; pp++) kl[h][pp] = *(const u32x4*)(ws + WS_KVP + ((size_t)((4 * w + pp) * 32 + 2 * t + h) * 64 + lane) * 16);
#pragma unroll
  for (int h = 0; h < 2; h++) pq[h] = *(const u32x4*)(ws + WS_PQ + ((size_t)((2 * t + h) * 64 + mb * 4 + w) * 64 + lane) * 16);
#pragma unroll
  for (int h = 0; h < 2; h++) {
    f32x4 lo, hi;
#pragma unroll
    for (int d = 0; d < 4; d++) {
      lo[d] = (bflo(kl[h][0][d]) + bflo(kl[h][1][d])) + (bflo(kl[h][2][d]) + bflo(kl[h][3][d]));
      hi[d] = (bfhi(kl[h][0][d]) + bfhi(kl[h][1][d])) + (bfhi(kl[h][2][d]) + bfhi(kl[h][3][d]));
    }
    *(f32x4*)(lds + (((w * 2 + h) * 2 + 0) * 64 + lane) * 16) = lo;
    *(f32x4*)(lds + (((w * 2 + h) * 2 + 1) * 64 + lane) * 16) = hi;
  }
  TS(1, 1);
  BAR();
  TS(1, 2);
  f32x4 ra[2][4], rb[2][4];
#pragma unroll
  for (int h = 0; h < 2; h++)
#pragma unroll
    for (int ww = 0; ww < 4; ww++) {
      ra[h][ww] = *(const f32x4*)(lds + (((ww * 2 + h) * 2 + 0) * 64 + lane) * 16);
      rb[h][ww] = *(const f32x4*)(lds + (((ww * 2 + h) * 2 + 1) * 64 + lane) * 16);
    }
#pragma unroll
  for (int h = 0; h < 2; h++)
#pragma unroll
    for (int d = 0; d < 4; d++) pq[h][d] = pk(phi(bflo(pq[h][d])), phi(bfhi(pq[h][d])));
  u32x2 kvf[2], zf[2];
#pragma unroll
  for (int h = 0; h < 2; h++) {
    f32x4 a = (ra[h][0] + ra[h][1]) + (ra[h][2] + ra[h][3]), b = (rb[h][0] + rb[h][1]) + (rb[h][2] + rb[h][3]);
    kvf[h] = u32x2{pk(a[0], b[0]), pk(a[1], b[1])};
    zf[h] = u32x2{pk(a[2], b[2]), pk(a[3], b[3])};
  }
  const f32x4 z4 = {0, 0, 0, 0};
#pragma unroll
  for (int st = 0; st < 2; st++) {
    u32x4 o;
#pragma unroll
    for (int h = 0; h < 2; h++) {
      u32x2 b = {pq[h][2 * st], pq[h][2 * st + 1]};
      f32x4 num = mma16(as4(kvf[h]), as4(b), z4);
      f32x4 den = mma16(as4(zf[h]), as4(b), z4);
      const float rd = __builtin_amdgcn_rcpf(den[0] + 1e-6f);
      o[2 * h] = pk(num[0] * rd, num[1] * rd);
      o[2 * h + 1] = pk(num[2] * rd, num[3] * rd);
    }
    *(u32x4*)(ws + WS_OF + ((size_t)((mb * 8 + w * 2 + st) * 16 + t) * 64 + lane) * 16) = o;
  }
}

extern "C" __global__ __launch_bounds__(256, 2) void gm_k2(char* ws) {
  __shared__ __attribute__((aligned(16))) char lds[16384];
  const int b = blockIdx.x;
  const int w = __builtin_amdgcn_readfirstlane(threadIdx.x >> 6), lane = threadIdx.x & 63;
  TSDECL
  TS(1, 0);
  if (b >= 256) k2_fft(TSA lds, ws, b - 256, w, lane);
  else k2_att(TSA lds, ws, b, w, lane);
  TS(1, 3); TSFLUSH(1);
}

extern "C" __global__ __launch_bounds__(768, 3) void gm_k3(const char* wo, char* ws, char* out) {
  __shared__ __attribute__((aligned(16))) char lds[147456];
  const int b = blockIdx.x, xcd = b & 7, ii = b >> 3;
  const int w12 = __builtin_amdgcn_readfirstlane(threadIdx.x >> 6), lane = threadIdx.x & 63;
  const int w = w12 & 3;
  TSDECL
  TS(2, 0);
  const int ml = ii & 3, ntile = ii >> 2;
  const int mtile = xcd * 4 + ml;
  const int rot = (ml + ntile) & ROT3;
  if (w12 < 8) {
    const int i = lane & 15, g = lane >> 4, q = i >> 2, p = lane & 3;
    const int wn = w & 1, wsn = w >> 1;
    const int fsw = (2 * g + (q >> 1)) & 3;
    f32x4 acc[2][2];
#pragma unroll
    for (int a = 0; a < 2; a++)
#pragma unroll
      for (int c = 0; c < 2; c++) acc[a][c] = f32x4{0, 0, 0, 0};
    const char* xb = lds + 49152 + ((8 * wn + 2 * wsn) * 64 + lane) * 16;
    if (w12 >= 4) {
      const float sgn = (i & 1) ? -1.f : 1.f;
      const unsigned k0v = (i & 1) ? 0xb5803580u : 0x35803580u;
      const float fn = (float)(64 * ntile + 16 * w + i) * (1.f / 512.f);
      const float t2 = 2.f * fcos(fn * 16.f);
      f32x2 c0[4], c1[4];
#pragma unroll
      for (int d = 0; d < 4; d++) {
        const float kf = (float)(2 * g + 64 * rot) + (float)(8 * (d >> 1) + (d & 1));
        const float a0 = kf * fn, a1 = (kf + 16.f) * fn;
        c0[d] = f32x2{fcos(a0), fsin(a0)} * 0x1p-19f;
        c1[d] = f32x2{fcos(a1), fsin(a1)} * 0x1p-19f;
      }
      u32x4 gf[16];
#pragma unroll
      for (int j = 0; j < 4; j++) {
#pragma unroll
        for (int h = 0; h < 2; h++) {
#pragma unroll
          for (int d = 0; d < 4; d++) {
            gf[4 * j + 2 * h][d] = pk(c0[d][0], c0[d][1]);
            gf[4 * j + 2 * h + 1][d] = pk(c1[d][0], c1[d][1]);
            c0[d] = c1[d] * t2 - c0[d];
          }
#pragma unroll
          for (int d = 0; d < 4; d++) c1[d] = c0[d] * t2 - c1[d];
        }
        gf[4 * j][0] = (((j + rot) & 3) == 0 && g == 0) ? k0v : gf[4 * j][0];
        if (j < 3) {
          const float sg = (((j + rot) & 3) == 3) ? sgn : 1.f;
#pragma unroll
          for (int d = 0; d < 4; d++) { c0[d] *= sg; c1[d] *= sg; }
        }
      }
#pragma unroll
      for (int k = 0; k < 16; k++) PIN(gf[k]);
      const int toffB = 16384 + (4 * g + q) * 128 + (fsw * 32) + p * 8;
      f32x4 accb[4];
#pragma unroll
      for (int c = 0; c < 4; c++) accb[c] = f32x4{0, 0, 0, 0};
      s16x8 tb_[2][2][4];
      auto rd = [&](int h, const char* slot) {
#pragma unroll
        for (int ks = 0; ks < 2; ks++)
#pragma unroll
          for (int c = 0; c < 4; c++) tb_[h][ks][c] = trd2(slot + (toffB ^ (32 * c)) + ks * 4096, slot + (toffB ^ (32 * c)) + ks * 4096 + 2048);
      };
      auto mm = [&](int h, int u) {
#pragma unroll
        for (int ks = 0; ks < 2; ks++)
#pragma unroll
          for (int c = 0; c < 4; c++) accb[c] = mma(as8(gf[2 * u + ks]), tb_[h][ks][c], accb[c]);
      };
      BAR();
      rd(0, lds);
      SCHED();
#pragma unroll
      for (int u = 1; u < 8; u++) {
        BAR();
        SCHED();
        rd(u & 1, lds + (u >= 6 ? u - 6 : u) * 24576); mm((u - 1) & 1, u - 1);
#pragma unroll
        for (int t = 0; t < 8; t++) { SGB(0x100, 2); SGB(0x008, 1); }
        SCHED();
      }
      mm(1, 7);
#pragma unroll
      for (int c = 0; c < 4; c++) *(f32x4*)(lds + 49152 + ((w * 4 + c) * 64 + lane) * 16) = accb[c];
      BAR();
      return;
    }
    const int toffA = 8192 + (4 * g + q) * 128 + (((2 * wn) ^ fsw) * 32) + p * 8;
    s16x8 ta_[2][2][2], ra_[2][2][2];
    auto rd = [&](int h, const char* slot) {
#pragma unroll
      for (int ks = 0; ks < 2; ks++)
#pragma unroll
        for (int a = 0; a < 2; a++) {
          ta_[h][ks][a] = trd2(slot + (toffA ^ (32 * a)) + ks * 4096, slot + (toffA ^ (32 * a)) + ks * 4096 + 2048);
          ra_[h][ks][a] = lrd(slot + (2 * wsn + a) * 2048 + ks * 1024 + lane * 16);
        }
    };
    auto mm = [&](int h) {
#pragma unroll
      for (int ks = 0; ks < 2; ks++)
#pragma unroll
        for (int a = 0; a < 2; a++)
#pragma unroll
          for (int c = 0; c < 2; c++) acc[a][c] = mma(ta_[h][ks][a], ra_[h][ks][c], acc[a][c]);
    };
    __builtin_amdgcn_s_waitcnt(0xC07F);
    auto ilv = [&]() {
#pragma unroll
      for (int t = 0; t < 6; t++) { SGB(0x008, 1); SGB(0x100, 2); }
      SGB(0x008, 2);
      SCHED();
    };
    BAR();
    TS(2, 1);
    rd(0, lds);
    SCHED();
    u32x4 gv = {0, 0, 0, 0};
#pragma unroll 1
    for (int j = 0; j < 3; j++) {
      BAR(); SCHED();
      rd(1, lds + (2 * j + 1) * 24576); mm(0); ilv();
      if (j == 2) gv = *(const u32x4*)(ws + WS_GT + mtile * 256 + i * 16);
      BAR(); SCHED();
      rd(0, lds + (j == 2 ? 0 : 2 * j + 2) * 24576); mm(1); ilv();
    }
    BAR(); SCHED();
    TS(2, 4);
    rd(1, lds + 24576); mm(0); ilv();
    mm(1); SCHED();
    BAR();
    TS(2, 2);
    const unsigned g0 = wsn ? gv[2] : gv[0], g1 = wsn ? gv[3] : gv[1];
#pragma unroll
    for (int a = 0; a < 2; a++) {
      unsigned pv[2][2];
#pragma unroll
      for (int c = 0; c < 2; c++) {
        const f32x4 bb = *(const f32x4*)(xb + (a * 4 + c) * 1024);
        const float ga = bflo(c ? g1 : g0), gb = bfhi(c ? g1 : g0);
        pv[c][0] = pk(ga * acc[a][c][0] + gb * bb[0], ga * acc[a][c][1] + gb * bb[1]);
        pv[c][1] = pk(ga * acc[a][c][2] + gb * bb[2], ga * acc[a][c][3] + gb * bb[3]);
      }
      auto s0 = __builtin_amdgcn_permlane16_swap(pv[0][0], pv[1][0], false, false);
      auto s1 = __builtin_amdgcn_permlane16_swap(pv[0][1], pv[1][1], false, false);
      const int s = 64 * mtile + 32 * wsn + 16 * (g & 1) + i;
      u32x4 v = {s0[0], s1[0], s0[1], s1[1]};
      *(u32x4*)(out + (size_t)s * 1024 + (64 * ntile + 32 * wn + 16 * a + 8 * (g >> 1)) * 2) = v;
    }
    TS(2, 3); TSFLUSH(2);
    return;
  }
  {
    const int swz = (((lane & 7) >> 1) ^ ((lane >> 4) & 3)) * 32 + (lane & 1) * 16;
    const unsigned voffW = (lane >> 3) * 1024 + swz, voffY = (lane >> 3) * 4096 + swz, voffR = lane * 16;
    const unsigned voffW1 = voffW + 7168, voffY1 = voffY + 31744;
    const char* ra = ws + WS_OF + (size_t)(4 * mtile + w) * 16384;
    const char* ta = wo + (size_t)(w * 16) * 1024 + ntile * 128;
    const char* tb = ws + WS_YT + (size_t)(w * 16) * 4096 + mtile * 128;
    const unsigned la = laddr_of(lds) + w * 2048;
    auto iss = [&](int u) {
      const int gu = (u + 2 * rot) & 7;
      const unsigned sl = la + (u >= 6 ? u - 6 : u) * 24576;
      asm volatile("s_mov_b32 m0, %0\n\ts_nop 0\n\t"
                   "global_load_lds_dwordx4 %7, %4\n\tglobal_load_lds_dwordx4 %8, %4 offset:1024\n\t"
                   "s_mov_b32 m0, %1\n\ts_nop 0\n\t"
                   "global_load_lds_dwordx4 %6, %3\n\tglobal_load_lds_dwordx4 %6, %3 offset:1024\n\t"
                   "s_mov_b32 m0, %2\n\ts_nop 0\n\t"
                   "global_load_lds_dwordx4 %9, %5\n\tglobal_load_lds_dwordx4 %10, %5 offset:1024"
                   :: "s"(sl + 16384), "s"(sl), "s"(sl + 8192), "s"(ra + gu * 2048), "s"(tb + (size_t)(gu * 64) * 4096), "s"(ta + (size_t)(gu * 64) * 1024),
                      "v"(voffR), "v"(voffY), "v"(voffY1), "v"(voffW), "v"(voffW1) : "memory");
    };
    iss(0); iss(1); iss(2);
#pragma unroll 1
    for (int u = 0; u < 5; u++) { WAITV(12); BAR(); iss(u + 3); }
    WAITV(12); BAR();
    WAITV(6); BAR();
    WAITV(0); BAR();
  }
}

extern "C" __global__ void gm_kend() {}

extern "C" void kernel_launch(void* const* d_in, const int* in_sizes, int n_in, void* d_out, int out_size, void* d_ws, size_t ws_size, hipStream_t stream) {
  const char* x = (const char*)d_in[0];
  const char* wqkv = (const char*)d_in[1];
  const char* wo = (const char*)d_in[2];
  const char* wr = (const char*)d_in[4];
  const char* wi = (const char*)d_in[5];
  const char* gw = (const char*)d_in[6];
  char* ws = (char*)d_ws;
  gm_k1<<<256, 512, 0, stream>>>(x, wqkv, wr, wi, gw, wo, ws);
  gm_k2<<<513, 256, 0, stream>>>(ws);
  gm_k3<<<256, 768, 0, stream>>>(wo, ws, (char*)d_out);
}
```

```cpp
#include <hip/hip_runtime.h>
#include <stdint.h>

typedef short s16x4 __attribute__((ext_vector_type(4)));
typedef short s16x8 __attribute__((ext_vector_type(8)));
typedef float f32x4 __attribute__((ext_vector_type(4)));
typedef float f32x2 __attribute__((ext_vector_type(2)));
typedef unsigned u32x4 __attribute__((ext_vector_type(4)));
typedef unsigned u32x2 __attribute__((ext_vector_type(2)));
typedef __bf16 bfx2 __attribute__((ext_vector_type(2)));
#define LAS __attribute__((address_space(3)))
#define DI __device__ __forceinline__
#define ROT1 6
#define SLP 2

static constexpr unsigned WS_X1T = 0;
static constexpr unsigned WS_WT  = WS_X1T + (9u << 18);
static constexpr unsigned WS_PQ  = WS_WT + (9u << 18);
static constexpr unsigned WS_KVP = WS_PQ + (2u << 20);
static constexpr unsigned WS_GT  = WS_KVP + (512u << 10);
static constexpr unsigned WS_OF  = WS_GT + (16u << 10);
static constexpr unsigned WS_YT  = WS_OF + (2u << 20);
static constexpr unsigned WS_E   = WS_YT + (2u << 20);
static constexpr unsigned WS_F1  = WS_E + 4096;
static constexpr unsigned WS_F2K = WS_F1 + 8 * 16 * 128;
static constexpr unsigned WS_F2Q = WS_F2K + 32 * 4 * 128;
static constexpr unsigned WS_F4  = WS_F2Q + 64 * 128;
static constexpr unsigned WS_F3  = WS_F4 + 256 * 128;
static constexpr unsigned WS_END = WS_F3 + 16 * 32 * 128;
static constexpr int SPECIAL = 255;

static constexpr int L_WO = 0, L_Y = 65536, L_FFT = 131072, L_ATT = 147456, L_FL = 163840 - 256;

DI unsigned pk(float lo, float hi) { f32x2 p = {lo, hi}; return __builtin_bit_cast(unsigned, __builtin_convertvector(p, bfx2)); }
DI float bflo(unsigned u) { return __builtin_bit_cast(float, u << 16); }
DI float bfhi(unsigned u) { return __builtin_bit_cast(float, u & 0xffff0000u); }
DI float rbf(float v) { return bflo(pk(v, 0.f)); }
DI s16x8 as8(u32x4 v) { return __builtin_bit_cast(s16x8, v); }
DI s16x4 as4(u32x2 v) { return __builtin_bit_cast(s16x4, v); }
DI f32x4 mma(s16x8 a, s16x8 b, f32x4 c) { return __builtin_amdgcn_mfma_f32_16x16x32_bf16(a, b, c, 0, 0, 0); }
DI f32x4 mma16(s16x4 a, s16x4 b, f32x4 c) { return __builtin_amdgcn_mfma_f32_16x16x16bf16_1k(a, b, c, 0, 0, 0); }
DI void mfa(f32x4& c, s16x8 a, s16x8 b) { asm volatile("v_mfma_f32_16x16x32_bf16 %0, %1, %2, %0" : "+v"(c) : "v"(a), "v"(b)); }
DI s16x4 trd(const char* l) { return __builtin_amdgcn_ds_read_tr16_b64_v4i16((LAS s16x4*)l); }
DI s16x8 cat(s16x4 a, s16x4 b) { return __builtin_shufflevector(a, b, 0, 1, 2, 3, 4, 5, 6, 7); }
DI s16x8 trd2(const char* l0, const char* l1) { return cat(trd(l0), trd(l1)); }
DI s16x8 lrd(const char* l) { return *(const s16x8*)l; }
DI float fsin(float rev) { return __builtin_amdgcn_sinf(rev); }
DI float fcos(float rev) { return __builtin_amdgcn_cosf(rev); }
DI unsigned laddr_of(const char* l) { return (unsigned)(size_t)(const LAS char*)l; }
DI void glds4(unsigned laddr, const char* sb, unsigned v0, unsigned v1, unsigned v2, unsigned v3) {
  asm volatile("s_mov_b32 m0, %0\n\ts_nop 0\n\t"
               "global_load_lds_dwordx4 %2, %1\n\t"
               "global_load_lds_dwordx4 %3, %1 offset:1024\n\t"
               "global_load_lds_dwordx4 %4, %1 offset:2048\n\t"
               "global_load_lds_dwordx4 %5, %1 offset:3072"
               :: "s"(laddr), "s"(sb), "v"(v0), "v"(v1), "v"(v2), "v"(v3) : "memory");
}
DI void glds4c(unsigned laddr, const char* sb, unsigned v0, unsigned v1, unsigned v2, unsigned v3) {
  asm volatile("s_mov_b32 m0, %0\n\ts_nop 0\n\t"
               "global_load_lds_dwordx4 %2, %1 sc1\n\t"
               "global_load_lds_dwordx4 %3, %1 offset:1024 sc1\n\t"
               "global_load_lds_dwordx4 %4, %1 offset:2048 sc1\n\t"
               "global_load_lds_dwordx4 %5, %1 offset:3072 sc1"
               :: "s"(laddr), "s"(sb), "v"(v0), "v"(v1), "v"(v2), "v"(v3) : "memory");
}
DI int perm32(int t, int i) { return 8 * (i >> 2) + 4 * t + (i & 3); }
DI float phi(float t) {
  float e = __builtin_amdgcn_exp2f(t * 1.44269504088896341f);
  e = __builtin_amdgcn_fmed3f(e, 0.f, 1.f);
  return fmaxf(t + 1e-6f, 1e-6f) + e;
}
#define TSP
#define TSA
#define TSDECL
#define TS(slot)
#define TSFLUSH()
#define SCHED() __builtin_amdgcn_sched_barrier(0)
#define SGB(mask, n) __builtin_amdgcn_sched_group_barrier(mask, n, 0)
#define PIN(x) asm volatile("" : "+v"(x))
#define WAITV(n) asm volatile("s_waitcnt vmcnt(" #n ")" ::: "memory")
#define BAR() asm volatile("s_waitcnt lgkmcnt(0)\n\ts_barrier" ::: "memory")
#define FENCE() asm volatile("" ::: "memory")

typedef __amdgpu_buffer_rsrc_t rsrc_t;
DI u32x4 ldc(rsrc_t r, unsigned off) { return __builtin_amdgcn_raw_buffer_load_b128(r, off, 0, 16); }
DI void stc(rsrc_t r, unsigned off, u32x4 v) { __builtin_amdgcn_raw_buffer_store_b128(v, r, off, 0, 16); }
DI void stc1(rsrc_t r, unsigned off, unsigned v) { __builtin_amdgcn_raw_buffer_store_b32(v, r, off, 0, 16); }
template <int SL> DI void gwait(rsrc_t r, unsigned off, unsigned ep) {
#pragma unroll 1
  for (int sp = 0; sp < 200000; sp++) {
    FENCE();
    const u32x4 f = ldc(r, off);
    if (__all(f[0] == ep && f[1] == ep && f[2] == ep && f[3] == ep)) break;
    __builtin_amdgcn_s_sleep(SL);
  }
  FENCE();
}
DI void linc(char* p, int lane) { if (lane == 0) asm volatile("ds_add_u32 %0, %1" :: "v"(laddr_of(p)), "v"(1u) : "memory"); }
DI void lset(char* p) { asm volatile("ds_write_b32 %0, %1" :: "v"(laddr_of(p)), "v"(1u) : "memory"); }
DI void lwait(const char* p, unsigned want) {
#pragma unroll 1
  for (int sp = 0; sp < 4000000; sp++) {
    if ((unsigned)__builtin_amdgcn_readfirstlane(*(const volatile LAS unsigned*)p) >= want) break;
    __builtin_amdgcn_s_sleep(1);
  }
  FENCE();
}

DI unsigned load_epoch(const char* p) { unsigned e; asm volatile("s_load_dword %0, %1, 0x0\n\ts_waitcnt lgkmcnt(0)" : "=s"(e) : "s"(p) : "memory"); return e + 1u; }

DI void wo_dma(char* lds, const char* wo, int ntile, int hf, int w, int lane) {
  const unsigned v = (lane >> 3) * 1024 + ((((lane & 7) >> 1) ^ ((lane >> 4) & 3)) * 32) + (lane & 1) * 16;
  const char* src = wo + (size_t)(hf * 256 + w * 64) * 1024 + ntile * 128;
  const unsigned la = laddr_of(lds) + L_WO + hf * 32768 + w * 8192;
  glds4(la, src, v, v + 7168, v + 2 * 7168, v + 3 * 7168);
  glds4(la + 4096, src + 32768, v, v + 7168, v + 2 * 7168, v + 3 * 7168);
}

DI void p1_prod(char* lds, const char* x, const char* wqkv, const char* wo, int m, int col0, int col1, int rot, int ntile, unsigned& ep, const char* epp, int w, int lane) {
  unsigned voffA[4], voffB[4];
#pragma unroll
  for (int j = 0; j < 4; j++) {
    const int par = j & 1;
    voffA[j] = (lane >> 3) * 1024 + (((lane & 7) ^ ((4 * par + (lane >> 4)) & 7)) * 16) + j * (8192 - 1024);
    voffB[j] = (lane >> 3) * 3072 + ((((lane & 7) >> 1) ^ (((lane >> 4) & 1) | (par << 1))) * 32) + (lane & 1) * 16 + j * (24576 - 1024);
  }
  const unsigned lbase = laddr_of(lds) + w * 4096;
  const char* xa = x + (size_t)(m * 128 + w * 32) * 1024;
  const char* wb = wqkv + (size_t)((w & 1) * 32) * 3072 + ((w >> 1) ? col1 : col0) * 2;
  auto issue = [&](int idx) {
    const int ks = (idx + rot) & 7;
    const unsigned slot = lbase + (idx & 3) * 32768;
    glds4(slot, xa + ks * 128, voffA[0], voffA[1], voffA[2], voffA[3]);
    glds4(slot + 16384, wb + (size_t)(ks * 64) * 3072, voffB[0], voffB[1], voffB[2], voffB[3]);
  };
  issue(0); issue(1);
  ep = load_epoch(epp);
  WAITV(8); BAR();
  issue(2); issue(3);
#pragma unroll 1
  for (int j = 1; j < 5; j++) { WAITV(16); BAR(); issue(j + 3); }
  WAITV(16); BAR();
  wo_dma(lds, wo, ntile, 0, w, lane);
  WAITV(16); BAR();
  wo_dma(lds, wo, ntile, 1, w, lane);
  WAITV(16); BAR();
}

template <bool QT>
DI void p1_gemm(TSP char* lds, rsrc_t rs, int m, int nt, unsigned& ep, const char* epp, int w, int lane) {
  const int i = lane & 15, g = lane >> 4, q = i >> 2, p = lane & 3;
  f32x4 acc[8][2];
#pragma unroll
  for (int a = 0; a < 8; a++) { acc[a][0] = f32x4{0, 0, 0, 0}; acc[a][1] = f32x4{0, 0, 0, 0}; }
  const int aoff = i * 128 + ((g ^ (i >> 1)) * 16);
  const int boff = 16384 + (8 * g + q) * 128 + ((w ^ (((q >> 1) & 1) | ((g & 1) << 1))) * 32) + p * 8;
  s16x8 A[2][2][8]; s16x4 B[2][2][2][2];
  auto rd1 = [&](int buf, const char* slot, int r) {
    const int ks = r / 12, rr = r % 12;
    const int t = rr == 0 ? 0 : rr == 1 ? 1 : rr == 3 ? 2 : rr == 4 ? 3 : -1;
    const int a = rr == 2 ? 0 : rr - 4;
    if (t >= 0) B[buf][ks][t >> 1][t & 1] = trd(slot + boff + (t >> 1) * 8192 + ks * 4096 + (t & 1) * 512);
    else A[buf][ks][a] = lrd(slot + (aoff ^ (ks * 64)) + a * 2048);
  };
  auto mf1 = [&](int buf, int ks, int mt, int hh) {
    const s16x8 bf = cat(B[buf][ks][hh][0], B[buf][ks][hh][1]);
    if (QT) mfa(acc[mt][hh], bf, A[buf][ks][mt]); else mfa(acc[mt][hh], A[buf][ks][mt], bf);
  };
  auto region = [&](int cur, const char* slot) {
#pragma unroll
    for (int mi = 0; mi < 32; mi++) {
      mf1(cur, mi >> 4, (mi & 15) >> 1, mi & 1);
      SCHED();
      if (mi < 24) { rd1(cur ^ 1, slot, mi); SCHED(); }
    }
  };
  ep = load_epoch(epp);
  __builtin_amdgcn_s_waitcnt(0xC07F);
  BAR();
  TS(1);
#pragma unroll
  for (int r = 0; r < 24; r++) rd1(0, lds, r);
  SCHED();
#pragma unroll 1
  for (int it = 0; it < 3; it++) {
    BAR(); SCHED();
    region(0, lds + ((2 * it + 1) & 3) * 32768);
    BAR(); SCHED();
    region(1, lds + ((2 * it + 2) & 3) * 32768);
  }
  BAR(); SCHED();
  region(0, lds + 3 * 32768);
  asm volatile("s_nop 7");
  SCHED();
  auto mmh = [&](int hh) {
#pragma unroll
    for (int mt = 0; mt < 8; mt++)
#pragma unroll
      for (int ks = 0; ks < 2; ks++) {
        const s16x8 bf = cat(B[1][ks][hh][0], B[1][ks][hh][1]);
        acc[mt][hh] = QT ? mma(bf, A[1][ks][mt], acc[mt][hh]) : mma(A[1][ks][mt], bf, acc[mt][hh]);
      }
  };
  mmh(0);
  SCHED();
  TS(2);
  if (QT) {
    mmh(1);
#pragma unroll
    for (int hh = 0; hh < 2; hh++) {
      const int head = (nt - 8) * 8 + hh * 4 + w;
#pragma unroll
      for (int u = 0; u < 4; u++) {
        u32x4 v = {pk(acc[2 * u][hh][0], acc[2 * u][hh][1]), pk(acc[2 * u][hh][2], acc[2 * u][hh][3]),
                   pk(acc[2 * u + 1][hh][0], acc[2 * u + 1][hh][1]), pk(acc[2 * u + 1][hh][2], acc[2 * u + 1][hh][3])};
        stc(rs, WS_PQ + ((head * 64 + m * 4 + u) * 64 + lane) * 16, v);
      }
    }
    WAITV(0);
    if (lane == 0) stc1(rs, WS_F2Q + (m * 4 + nt - 8) * 128 + w * 4, ep);
  } else {
    mmh(1);
    u32x4 pa[4];
#pragma unroll
    for (int u = 0; u < 4; u++)
      pa[u] = u32x4{pk(phi(acc[2 * u][0][0]), phi(acc[2 * u][0][1])), pk(phi(acc[2 * u][0][2]), phi(acc[2 * u][0][3])),
                    pk(phi(acc[2 * u + 1][0][0]), phi(acc[2 * u + 1][0][1])), pk(phi(acc[2 * u + 1][0][2]), phi(acc[2 * u + 1][0][3]))};
#pragma unroll
    for (int t = 0; t < 16; t++) { SGB(0x008, 1); SGB(0x002, 2); }
    SCHED();
    f32x4 kv = {0, 0, 0, 0}, z = {0, 0, 0, 0};
    const u32x4 ones = {0x3f803f80u, 0x3f803f80u, 0x3f803f80u, 0x3f803f80u};
#pragma unroll
    for (int u = 0; u < 4; u++) {
      u32x4 b = {pk(acc[2 * u][1][0], acc[2 * u][1][1]), pk(acc[2 * u][1][2], acc[2 * u][1][3]),
                 pk(acc[2 * u + 1][1][0], acc[2 * u + 1][1][1]), pk(acc[2 * u + 1][1][2], acc[2 * u + 1][1][3])};
      z = mma(as8(pa[u]), as8(ones), z);
      kv = mma(as8(pa[u]), as8(b), kv);
    }
    const int head = nt * 4 + w;
    u32x4 v = {pk(kv[0], kv[1]), pk(kv[2], kv[3]), pk(z[0], z[1]), pk(z[2], z[3])};
    stc(rs, WS_KVP + ((m * 32 + head) * 64 + lane) * 16, v);
    WAITV(0);
    if (lane == 0) stc1(rs, WS_F2K + (head * 4 + (m >> 2)) * 128 + (m & 3) * 4, ep);
  }
}

DI float dpp_sum16(float v) {
  v += __builtin_bit_cast(float, __builtin_amdgcn_update_dpp(0, __builtin_bit_cast(int, v), 0x128, 0xf, 0xf, false));
  v += __builtin_bit_cast(float, __builtin_amdgcn_update_dpp(0, __builtin_bit_cast(int, v), 0x124, 0xf, 0xf, false));
  v += __builtin_bit_cast(float, __builtin_amdgcn_update_dpp(0, __builtin_bit_cast(int, v), 0x122, 0xf, 0xf, false));
  v += __builtin_bit_cast(float, __builtin_amdgcn_update_dpp(0, __builtin_bit_cast(int, v), 0x121, 0xf, 0xf, false));
  return v;
}

DI void p1_aux(TSP char* lds, const char* x, const char* wr, const char* wi, const char* gw, rsrc_t rs, int a, unsigned& ep, const char* epp, int w, int lane) {
  const int i = lane & 15, g = lane >> 4;
  char* LX = lds;
  char* LS = lds + 32768;
  char* LF = lds + 69632;
  const int amt = (a >> 1) & 1;
  const int n2s = perm32(amt, 8 * (a & 1) + w);
  {
    const unsigned v = lane * 16;
    const int k0 = 8 * (a & 7) + 64 * (4 * (a >> 3) + (w & 3));
    const char* fsrc = ((w >> 2) ? wi : wr) + (size_t)k0 * 514;
    glds4(laddr_of(LF) + w * 4352, fsrc, v, v, v, v);
    if (lane == 0) asm volatile("s_mov_b32 m0, %0\n\ts_nop 0\n\tglobal_load_lds_dwordx4 %2, %1" :: "s"(laddr_of(LF) + w * 4352 + 4096), "s"(fsrc + 4096), "v"(v) : "memory");
    glds4(laddr_of(LX) + w * 4096, x + (size_t)(128 * (a >> 2) + n2s) * 1024, v, v + 31 * 1024, v + 62 * 1024, v + 93 * 1024);
  }
  u32x4 gwv = *(const u32x4*)(gw + lane * 16);
  u32x4 b1re, b1im, a3re, a3im, a4re, a4im;
  float twc[4], tws[4];
  const float fi = (float)i;
  const float g4 = (float)(4 * g);
#pragma unroll
  for (int d = 0; d < 4; d++) {
    float c[2], s[2];
#pragma unroll
    for (int e = 0; e < 2; e++) {
      const int j = 2 * d + e;
      const float ang = (g4 + (float)(16 * (j >> 2) + (j & 3))) * (fi * (1.f / 32.f));
      c[e] = fcos(ang);
      s[e] = (i == 0) ? (e ? -1.f : 1.f) : fsin(-ang);
    }
    b1re[d] = pk(c[0], c[1]); b1im[d] = pk(s[0], s[1]);
    const float n2i = (g4 + (float)d) * fi;
    {
      const float ang = n2i * (1.f / 16.f);
      const float cs = fcos(ang), sn = fsin(ang);
      a3re[d] = pk(cs, sn);
      a3im[d] = pk(sn, cs) ^ ((i >= 8) ? 0x80000000u : 0x00008000u);
    }
    {
      const float ang = n2i * (1.f / 32.f);
      const float cs = fcos(ang);
      const float sn = (i == 0) ? ((d & 1) ? -1.f : 1.f) : fsin(-ang);
      const unsigned u = pk(cs, sn);
      const int sh = (i & 1) * 16;
      a4re[d] = (u & 0xffffu) << sh;
      a4im[d] = (u >> 16) << sh;
    }
    {
      const float ang = n2i * (1.f / 512.f);
      twc[d] = fcos(ang); tws[d] = fsin(ang);
      PIN(twc[d]); PIN(tws[d]);
    }
  }
  PIN(b1re); PIN(b1im); PIN(a3re); PIN(a3im); PIN(a4re); PIN(a4im);
  int soff[4];
#pragma unroll
  for (int r = 0; r < 4; r++) {
    const int k2 = 4 * g + r;
    const int k = i + 32 * k2;
    const int col = (i == 0) ? 16 * k2 : (k2 < 8 ? k : 512 - k);
    soff[r] = col * 128 + ((w ^ (col & 7)) * 16);
    PIN(soff[r]);
  }
  ep = load_epoch(epp);
  WAITV(0);
  BAR();
  TS(1);
  const int sl = lane & 7;
  const int wline = ((((a & 7) >> 1) * 2 + ((a >> 3) & 1)) * 64 + (a >> 4) * 16 + (a & 1) * 8) * 16;
  const int cb = (lane >> 3) * 16 + (w & 3) + 128 * (w >> 2);
  const char* fp = LF + sl * 514 + cb * 2;
  {
    unsigned o[4][4];
#pragma unroll
    for (int a4 = 0; a4 < 4; a4++) {
      u32x4 fv;
#pragma unroll
      for (int r = 0; r < 4; r++)
        fv[r] = (unsigned)*(const unsigned short*)(fp + r * 4352 + a4 * 8) | ((unsigned)*(const unsigned short*)(fp + (4 + r) * 4352 + a4 * 8) << 16);
      const char* row = LX + (w * 4 + a4) * 1024;
      s16x8 xf = trd2(row + lane * 8, row + 512 + lane * 8);
      stc(rs, WS_WT + (cb + 4 * a4) * 8192 + wline + sl * 16, fv);
      const f32x4 z4 = {0, 0, 0, 0};
      f32x4 are = mma(xf, as8(b1re), z4), aim = mma(xf, as8(b1im), z4);
      f32x4 tr, ti;
#pragma unroll
      for (int r = 0; r < 4; r++) { tr[r] = are[r] * twc[r] + aim[r] * tws[r]; ti[r] = aim[r] * twc[r] - are[r] * tws[r]; }
      asm("" : "+v"(tr), "+v"(ti));
      u32x4 bb;
#pragma unroll
      for (int r = 0; r < 4; r++) bb[r] = pk(tr[r], ti[r]);
      f32x4 xre = mma(as8(a3re), as8(bb), z4), xim = mma(as8(a3im), as8(bb), z4);
      f32x4 sre = mma(as8(a4re), as8(bb), z4), sim = mma(as8(a4im), as8(bb), z4);
#pragma unroll
      for (int r = 0; r < 4; r++) o[r][a4] = pk((i == 0) ? sre[r] : xre[r], (i == 0) ? sim[r] : xim[r]);
      PIN(o[0][a4]); PIN(o[1][a4]); PIN(o[2][a4]); PIN(o[3][a4]);
    }
    if (lane == 0) {
      u32x4 v = {o[0][0] >> 16, o[0][1] >> 16, o[0][2] >> 16, o[0][3] >> 16};
      *(u32x4*)(LS + 256 * 128 + w * 16) = v;
#pragma unroll
      for (int a4 = 0; a4 < 4; a4++) o[0][a4] &= 0xffffu;
    }
#pragma unroll
    for (int r = 0; r < 4; r++) {
      u32x4 v = {o[r][0], o[r][1], o[r][2], o[r][3]};
      *(u32x4*)(LS + soff[r]) = v;
    }
  }
  BAR();
  TS(2);
  const int sc = w * 8 + (lane >> 3);
  const int xline = (amt * 4 + (a >> 4)) * 1024 + ((a >> 2) & 3) * 256 + (a & 1) * 128;
  const u32x4 ones = {0x3f803f80u, 0x3f803f80u, 0x3f803f80u, 0x3f803f80u};
  float gsum[4];
#pragma unroll
  for (int j = 0; j < 4; j++) {
    const int c = j * 64 + sc;
    u32x4 v0 = *(const u32x4*)(LS + c * 128 + ((sl ^ (c & 7)) * 16));
    u32x4 xv = *(const u32x4*)(LX + (w * 4 + j) * 1024 + lane * 16);
    stc(rs, WS_X1T + c * 8192 + xline + sl * 16, v0);
    u32x4 pr;
#pragma unroll
    for (int d = 0; d < 4; d++) pr[d] = pk(bflo(xv[d]) * bflo(gwv[d]), bfhi(xv[d]) * bfhi(gwv[d]));
    gsum[j] = mma(as8(ones), as8(pr), f32x4{0, 0, 0, 0})[0];
    PIN(gsum[j]);
  }
  if (w == 6 && lane < 8) stc(rs, WS_X1T + 256 * 8192 + xline + lane * 16, *(const u32x4*)(LS + 256 * 128 + lane * 16));
  if (w == 7 && lane < 8) {
    u32x4 v;
#pragma unroll
    for (int r = 0; r < 4; r++) v[r] = (unsigned)*(const unsigned short*)(LF + sl * 514 + 512 + r * 4352) | ((unsigned)*(const unsigned short*)(LF + sl * 514 + 512 + (4 + r) * 4352) << 16);
    stc(rs, WS_WT + 256 * 8192 + wline + sl * 16, v);
  }
  {
    float gs = (g == 0) ? gsum[0] : (g == 1) ? gsum[1] : (g == 2) ? gsum[2] : gsum[3];
    gs = rbf(dpp_sum16(gs));
    const float gg = rbf(__builtin_amdgcn_rcpf(1.f + __builtin_amdgcn_exp2f(gs * -1.44269504088896341f)));
    const int s = 32 * (4 * (a >> 2) + g) + n2s;
    if (i == 0) stc1(rs, WS_GT + (s >> 6) * 256 + (s & 15) * 16 + ((s >> 4) & 3) * 4, pk(gg, 1.f - gg));
  }
  WAITV(0);
  BAR();
  if (w == 0 && lane < 8) stc1(rs, WS_F1 + (lane * 16 + (a >> 2)) * 128 + (a & 3) * 4, ep);
}

DI void p2_fft(TSP char* lb, char* fl, char* f1seen, const char* ws, rsrc_t rs, int c, int rep, unsigned ep, int w, int lane) {
  const int i = lane & 15, g = lane >> 4;
  const int k1 = 16 * w + i;
  const float fk1 = (float)k1, fg = (float)g;
  u32x4 fre[4], fim[4], fc[2], fs[2], fn[2];
  {
    const float st = fk1 * (1.f / 64.f);
    const float a0 = fg * 4.f * st;
    const bool odd = i & 1;
    const unsigned m2 = odd ? 0x80008000u : 0u;
    const unsigned mre = (i & 2) ? 0x80008000u : 0u;
    const unsigned mim = ((i + 1) & 2) ? 0x80008000u : 0u;
#pragma unroll
    for (int d = 0; d < 4; d++) {
      const float ang = __builtin_fmaf((float)d, st, a0);
      const float cs = fcos(ang), sn = fsin(ang);
      const unsigned r0 = pk(cs, sn), i0 = pk(sn, cs) ^ 0x8000u;
      const unsigned r1 = (odd ? i0 : r0) ^ mre, i1 = (odd ? r0 : i0) ^ mim;
      fre[0][d] = r0; fim[0][d] = i0;
      fre[1][d] = r1; fim[1][d] = i1;
      fre[2][d] = r0 ^ m2; fim[2][d] = i0 ^ m2;
      fre[3][d] = r1 ^ m2; fim[3][d] = i1 ^ m2;
    }
#pragma unroll
    for (int ks = 0; ks < 4; ks++) { PIN(fre[ks]); PIN(fim[ks]); }
  }
  float c1[2][4], s1[2][4], c2[2][4], s2[2][4];
#pragma unroll
  for (int mt = 0; mt < 2; mt++) {
    const float fa = (float)perm32(mt, i);
    const float st = fa * (1.f / 32.f), a0 = fg * 8.f * st;
    const float st1 = fk1 * (1.f / 2048.f), b0 = (fg * 8.f + (float)(4 * mt)) * st1;
    const float st2 = fa * (1.f / 2048.f), d0 = (fg * 4.f + (float)(16 * w)) * st2;
#pragma unroll
    for (int d = 0; d < 4; d++) {
      const float x0 = __builtin_fmaf((float)(2 * d), st, a0), x1 = __builtin_fmaf((float)(2 * d + 1), st, a0);
      fc[mt][d] = pk(fcos(x0), fcos(x1));
      fs[mt][d] = pk(fsin(x0), fsin(x1));
      fn[mt][d] = fs[mt][d] ^ 0x80008000u;
      const float t1 = __builtin_fmaf((float)d, st1, b0);
      c1[mt][d] = fcos(t1); s1[mt][d] = fsin(t1);
      const float t2 = __builtin_fmaf((float)d, st2, d0);
      c2[mt][d] = fcos(t2); s2[mt][d] = fsin(t2);
      PIN(c1[mt][d]); PIN(s1[mt][d]); PIN(c2[mt][d]); PIN(s2[mt][d]);
    }
    PIN(fc[mt]); PIN(fs[mt]); PIN(fn[mt]);
  }
  TS(4);
  if (w == 0) { gwait<SLP>(rs, WS_F1 + (rep * 16 + i) * 128, ep); lset(f1seen); }
  else lwait(f1seen, 1);
  TS(5);
  asm volatile("s_mov_b32 m0, %0\n\ts_nop 0\n\tglobal_load_lds_dwordx4 %2, %1 sc1\n\tglobal_load_lds_dwordx4 %2, %1 offset:1024 sc1"
               :: "s"(laddr_of(lb) + 8192 + w * 2048), "s"(ws + WS_X1T + (size_t)c * 8192 + w * 2048), "v"(lane * 16) : "memory");
  u32x4 wf[2];
#pragma unroll
  for (int mt = 0; mt < 2; mt++) wf[mt] = ldc(rs, WS_WT + c * 8192 + ((w * 2 + mt) * 64 + lane) * 16);
  WAITV(2);
  linc(fl, lane);
  lwait(fl, 4);
  TS(6);
  const f32x4 z4 = {0, 0, 0, 0};
  f32x4 re[2], im[2];
#pragma unroll
  for (int mt = 0; mt < 2; mt++) { re[mt] = z4; im[mt] = z4; }
#pragma unroll
  for (int ks = 0; ks < 4; ks++)
#pragma unroll
    for (int mt = 0; mt < 2; mt++) {
      s16x8 xa = lrd(lb + 8192 + ((mt * 4 + ks) * 64 + lane) * 16);
      re[mt] = mma(xa, as8(fre[ks]), re[mt]); im[mt] = mma(xa, as8(fim[ks]), im[mt]);
    }
  u32x4 pr, pi;
#pragma unroll
  for (int mt = 0; mt < 2; mt++) {
    float tr[4], ti[4];
#pragma unroll
    for (int r = 0; r < 4; r++) { tr[r] = re[mt][r] * c1[mt][r] + im[mt][r] * s1[mt][r]; ti[r] = im[mt][r] * c1[mt][r] - re[mt][r] * s1[mt][r]; }
    pr[2 * mt] = pk(tr[0], tr[1]); pr[2 * mt + 1] = pk(tr[2], tr[3]);
    pi[2 * mt] = pk(ti[0], ti[1]); pi[2 * mt + 1] = pk(ti[2], ti[3]);
  }
#pragma unroll
  for (int mt = 0; mt < 2; mt++) {
    re[mt] = mma(as8(fs[mt]), as8(pi), mma(as8(fc[mt]), as8(pr), z4));
    im[mt] = mma(as8(fn[mt]), as8(pr), mma(as8(fc[mt]), as8(pi), z4));
  }
#pragma unroll
  for (int mt = 0; mt < 2; mt++) {
    float tr[4], ti[4];
#pragma unroll
    for (int r = 0; r < 4; r++) {
      const float a = bflo(wf[mt][r]), b = bfhi(wf[mt][r]);
      tr[r] = re[mt][r] * a - im[mt][r] * b; ti[r] = re[mt][r] * b + im[mt][r] * a;
    }
    pr[2 * mt] = pk(tr[0], tr[1]); pr[2 * mt + 1] = pk(tr[2], tr[3]);
    pi[2 * mt] = pk(ti[0], ti[1]); pi[2 * mt + 1] = pk(ti[2], ti[3]);
  }
#pragma unroll
  for (int nt = 0; nt < 2; nt++) {
    re[nt] = mma(as8(pi), as8(fn[nt]), mma(as8(pr), as8(fc[nt]), z4));
    im[nt] = mma(as8(pi), as8(fc[nt]), mma(as8(pr), as8(fs[nt]), z4));
  }
#pragma unroll
  for (int nt = 0; nt < 2; nt++) {
    u32x4 v;
#pragma unroll
    for (int r = 0; r < 4; r++) v[r] = pk(re[nt][r] * c2[nt][r] - im[nt][r] * s2[nt][r], -re[nt][r] * s2[nt][r] - im[nt][r] * c2[nt][r]);
    *(u32x4*)(lb + ((nt * 4 + w) * 64 + lane) * 16) = v;
  }
  linc(fl + 4, lane);
  lwait(fl + 4, 4);
#pragma unroll
  for (int mt = 0; mt < 2; mt++) { re[mt] = z4; im[mt] = z4; }
#pragma unroll
  for (int ks = 0; ks < 4; ks++)
#pragma unroll
    for (int mt = 0; mt < 2; mt++) {
      s16x8 ya = lrd(lb + ((mt * 4 + ks) * 64 + lane) * 16);
      re[mt] = mma(ya, as8(fre[ks]), re[mt]); im[mt] = mma(ya, as8(fim[ks]), im[mt]);
    }
  u32x4 vr = {pk(re[0][0], re[0][1]), pk(re[0][2], re[0][3]), pk(re[1][0], re[1][1]), pk(re[1][2], re[1][3])};
  u32x4 vi = {pk(im[0][0], im[0][1]), pk(im[0][2], im[0][3]), pk(im[1][0], im[1][1]), pk(im[1][2], im[1][3])};
  const unsigned yt = WS_YT + (32 * k1 + 8 * g) * 2;
  const int rr = (c == 256) ? 1 : 2 * c;
  stc(rs, yt + rr * 4096, vr);
  if (c & 255) stc(rs, yt + (rr + 1) * 4096, vi);
  TS(7);
}

DI void p2_att(TSP char* lds, rsrc_t rs, int t, int mb, unsigned ep, int w, int lane) {
  char* lb = lds + L_ATT;
  const int h = w >> 1, hf = w & 1;
  gwait<SLP>(rs, (lane & 3) < 2 ? WS_F2K + ((2 * t + h) * 4 + 2 * hf + (lane & 1)) * 128 : WS_F2Q + ((2 * mb + h) * 4 + (t >> 2)) * 128, ep);
  TS(4);
  u32x4 pq[2][2], kl[8];
#pragma unroll
  for (int pp = 0; pp < 8; pp++) kl[pp] = ldc(rs, WS_KVP + (((8 * hf + pp) * 32 + 2 * t + h) * 64 + lane) * 16);
#pragma unroll
  for (int u = 0; u < 2; u++)
#pragma unroll
    for (int hh = 0; hh < 2; hh++) pq[u][hh] = ldc(rs, WS_PQ + (((2 * t + hh) * 64 + mb * 8 + 2 * w + u) * 64 + lane) * 16);
  {
    f32x4 lo, hi;
#pragma unroll
    for (int d = 0; d < 4; d++) {
      lo[d] = ((bflo(kl[0][d]) + bflo(kl[1][d])) + (bflo(kl[2][d]) + bflo(kl[3][d]))) + ((bflo(kl[4][d]) + bflo(kl[5][d])) + (bflo(kl[6][d]) + bflo(kl[7][d])));
      hi[d] = ((bfhi(kl[0][d]) + bfhi(kl[1][d])) + (bfhi(kl[2][d]) + bfhi(kl[3][d]))) + ((bfhi(kl[4][d]) + bfhi(kl[5][d])) + (bfhi(kl[6][d]) + bfhi(kl[7][d])));
    }
    *(f32x4*)(lb + ((w * 2 + 0) * 64 + lane) * 16) = lo;
    *(f32x4*)(lb + ((w * 2 + 1) * 64 + lane) * 16) = hi;
  }
  linc(lds + L_FL + 12, lane);
#pragma unroll
  for (int u = 0; u < 2; u++)
#pragma unroll
    for (int hh = 0; hh < 2; hh++) {
#pragma unroll
      for (int d = 0; d < 4; d++) pq[u][hh][d] = pk(phi(bflo(pq[u][hh][d])), phi(bfhi(pq[u][hh][d])));
      PIN(pq[u][hh]);
    }
  lwait(lds + L_FL + 12, 4);
  TS(5);
  u32x2 kvf[2], zf[2];
#pragma unroll
  for (int hh = 0; hh < 2; hh++) {
    const f32x4 a = *(const f32x4*)(lb + ((hh * 4 + 0) * 64 + lane) * 16) + *(const f32x4*)(lb + ((hh * 4 + 2) * 64 + lane) * 16);
    const f32x4 b = *(const f32x4*)(lb + ((hh * 4 + 1) * 64 + lane) * 16) + *(const f32x4*)(lb + ((hh * 4 + 3) * 64 + lane) * 16);
    kvf[hh] = u32x2{pk(a[0], b[0]), pk(a[1], b[1])};
    zf[hh] = u32x2{pk(a[2], b[2]), pk(a[3], b[3])};
  }
  const f32x4 z4 = {0, 0, 0, 0};
#pragma unroll
  for (int st = 0; st < 4; st++) {
    u32x4 o;
#pragma unroll
    for (int hh = 0; hh < 2; hh++) {
      u32x2 b = {pq[st >> 1][hh][2 * (st & 1)], pq[st >> 1][hh][2 * (st & 1) + 1]};
      f32x4 num = mma16(as4(kvf[hh]), as4(b), z4);
      f32x4 den = mma16(as4(zf[hh]), as4(b), z4);
      const float rd = __builtin_amdgcn_rcpf(den[0] + 1e-6f);
      o[2 * hh] = pk(num[0] * rd, num[1] * rd);
      o[2 * hh + 1] = pk(num[2] * rd, num[3] * rd);
    }
    stc(rs, WS_OF + (((mb * 16 + w * 4 + st) * 16 + t) * 64 + lane) * 16, o);
  }
  TS(6);
  WAITV(0);
  if (lane == 0) stc1(rs, WS_F4 + ((mb * 4 + w) * 4 + (t >> 2)) * 128 + (t & 3) * 4, ep);
}

extern "C" __global__ __launch_bounds__(512, 2) void gm_all(const char* x, const char* wqkv, const char* wr, const char* wi, const char* gw, const char* wo, char* ws, char* out) {
  __shared__ __attribute__((aligned(16))) char lds[163840];
  const int b = blockIdx.x, xcd = b & 7, ii = b >> 3;
  const int w8 = __builtin_amdgcn_readfirstlane(threadIdx.x >> 6), lane = threadIdx.x & 63;
  const int w = w8 & 3;
  TSDECL
  TS(0);
  unsigned ep;
  const char* epp = ws + WS_E;
  const rsrc_t rs = __builtin_amdgcn_make_buffer_rsrc(ws, 0, WS_END, 0x00020000);
  char* FL = lds + L_FL;
  const int ml = ii & 3, nl = ii >> 2;
  const int mtile = xcd * 4 + ml, ntile = nl;
  const bool aux = ii >= 24;
  {
    const int m = (xcd >> 1) * 4 + ml;
    const int nt = nl < 4 ? (xcd & 1) * 4 + nl : 8 + (xcd & 1) * 2 + (nl - 4);
    if (w8 < 4 || aux) {
      if (w8 == 0) *(LAS unsigned*)(FL + lane * 4) = 0;
      if (!aux) __builtin_amdgcn_s_setprio(2);
      if (aux) {
        p1_aux(TSA lds, x, wr, wi, gw, rs, xcd * 8 + (ii - 24), ep, epp, w8, lane);
      }
      else if (nl < 4) p1_gemm<false>(TSA lds, rs, m, nt, ep, epp, w, lane);
      else p1_gemm<true>(TSA lds, rs, m, nt, ep, epp, w, lane);
    } else {
      const int col0 = nl >= 4 ? (nt - 8) * 128 : 512 + nt * 64;
      const int col1 = nl >= 4 ? col0 + 64 : col0 + 512;
      p1_prod(lds, x, wqkv, wo, m, col0, col1, (2 * ml + nl) & ROT1, ntile, ep, epp, w, lane);
    }
  }
  if (w8 < 4) linc(FL + 80, lane);
  TS(3);
  const int i = lane & 15, g = lane >> 4, q = i >> 2, p = lane & 3;
  const int fsw = (2 * g + (q >> 1)) & 3;
  const int col = (b + 1) & 255;
  u32x4 gf[16];
  if (w8 >= 4) {
    const float sgn = (i & 1) ? -1.f : 1.f;
    const unsigned k0v = (i & 1) ? 0xb5803580u : 0x35803580u;
    const float fn = (float)(64 * ntile + 16 * w + i) * (1.f / 512.f);
    const float t2 = 2.f * fcos(fn * 16.f);
    f32x2 c0[4], c1[4];
#pragma unroll
    for (int d = 0; d < 4; d++) {
      const float kf = (float)(2 * g + 64 * w) + (float)(8 * (d >> 1) + (d & 1));
      const float a0 = kf * fn, a1 = (kf + 16.f) * fn;
      c0[d] = f32x2{fcos(a0), fsin(a0)} * 0x1p-19f;
      c1[d] = f32x2{fcos(a1), fsin(a1)} * 0x1p-19f;
    }
#pragma unroll
    for (int j = 0; j < 4; j++) {
#pragma unroll
      for (int h = 0; h < 2; h++) {
#pragma unroll
        for (int d = 0; d < 4; d++) {
          gf[4 * j + 2 * h][d] = pk(c0[d][0], c0[d][1]);
          gf[4 * j + 2 * h + 1][d] = pk(c1[d][0], c1[d][1]);
          c0[d] = c1[d] * t2 - c0[d];
        }
#pragma unroll
        for (int d = 0; d < 4; d++) c1[d] = c0[d] * t2 - c1[d];
      }
      gf[4 * j][0] = (((j + w) & 3) == 0 && g == 0) ? k0v : gf[4 * j][0];
      if (j < 3) {
        const float sg = (((j + w) & 3) == 3) ? sgn : 1.f;
#pragma unroll
        for (int d = 0; d < 4; d++) { c0[d] *= sg; c1[d] *= sg; }
      }
    }
#pragma unroll
    for (int k = 0; k < 16; k++) PIN(gf[k]);
    if (!aux) { WAITV(0); linc(FL, lane); }
  }
  if (w8 >= 4 || b == SPECIAL) {
    const bool pg = w8 >= 4;
    p2_fft(TSA pg ? lds + L_FFT : lds + L_WO, pg ? FL + 4 : FL + 20, pg ? FL + 16 : FL + 28, ws, rs, pg ? col : 256, xcd, ep, w, lane);
    WAITV(0);
    if (!pg) lset(FL + 64 + w * 4);
  }
  if (w8 < 4) {
  if (b < 128) p2_att(TSA lds, rs, b & 15, b >> 4, ep, w, lane);
  if (aux) {
    if (b == SPECIAL) {
#pragma unroll
      for (int j = 0; j < 4; j++) lwait(FL + 64 + j * 4, 1);
    }
    wo_dma(lds, wo, ntile, 0, w, lane); wo_dma(lds, wo, ntile, 1, w, lane);
    WAITV(0); linc(FL, lane);
  }
  TS(7);
  {
    const int stg = 4 * mtile + w;
    const int toffA = L_WO + (4 * g + q) * 128 + (fsw * 32) + p * 8;
    lwait(FL, 4);
    s16x8 wf[8][4];
#pragma unroll
    for (int ks = 0; ks < 8; ks++)
#pragma unroll
      for (int a = 0; a < 4; a++) { wf[ks][a] = trd2(lds + (toffA ^ (32 * a)) + ks * 4096, lds + (toffA ^ (32 * a)) + ks * 4096 + 2048); }
#pragma unroll
    for (int ks = 0; ks < 8; ks++)
#pragma unroll
      for (int a = 0; a < 4; a++) PIN(wf[ks][a]);
    if (w == 0) {
      if (b >= 128) lwait(FL + 84, 1);
      gwait<SLP>(rs, WS_F4 + (mtile * 4 + (lane & 3)) * 128, ep);
      lwait(FL + 16, 1);
      lset(FL + 88);
    } else lwait(FL + 88, 1);
    TS(8);
    u32x4 of[16];
#pragma unroll
    for (int ks = 0; ks < 16; ks++) of[ks] = ldc(rs, WS_OF + ((stg * 16 + ks) * 64 + lane) * 16);
    const unsigned gv = __builtin_amdgcn_raw_buffer_load_b32(rs, WS_GT + mtile * 256 + i * 16 + w * 4, 0, 16);
    SCHED();
    f32x4 acc[4];
#pragma unroll
    for (int a = 0; a < 4; a++) acc[a] = f32x4{0, 0, 0, 0};
#pragma unroll
    for (int ks = 0; ks < 8; ks++) {
#pragma unroll
      for (int a = 0; a < 4; a++) {
        acc[a] = mma(wf[ks][a], as8(of[ks]), acc[a]);
        wf[ks][a] = trd2(lds + (toffA ^ (32 * a)) + (ks + 8) * 4096, lds + (toffA ^ (32 * a)) + (ks + 8) * 4096 + 2048);
      }
      SCHED();
    }
#pragma unroll
    for (int ks = 0; ks < 8; ks++)
#pragma unroll
      for (int a = 0; a < 4; a++) acc[a] = mma(wf[ks][a], as8(of[ks + 8]), acc[a]);
    TS(9);
    lwait(FL + 48, 4);
    TS(10);
    const float ga = bflo(gv), gb = bfhi(gv);
    unsigned pv[4][2];
#pragma unroll
    for (int a = 0; a < 4; a++) {
      const f32x4 bb = *(const f32x4*)(lds + L_FFT + ((w * 4 + a) * 64 + lane) * 16);
      pv[a][0] = pk(ga * acc[a][0] + gb * bb[0], ga * acc[a][1] + gb * bb[1]);
      pv[a][1] = pk(ga * acc[a][2] + gb * bb[2], ga * acc[a][3] + gb * bb[3]);
    }
#pragma unroll
    for (int a = 0; a < 2; a++) {
      auto s0 = __builtin_amdgcn_permlane16_swap(pv[2 * a][0], pv[2 * a + 1][0], false, false);
      auto s1 = __builtin_amdgcn_permlane16_swap(pv[2 * a][1], pv[2 * a + 1][1], false, false);
      u32x4 v = {s0[0], s1[0], s0[1], s1[1]};
      { char* op = out + (size_t)(16 * stg + i) * 1024 + (64 * ntile + 32 * a + 16 * (g & 1) + 8 * (g >> 1)) * 2; asm volatile("global_store_dwordx4 %0, %1, off sc1\n\ts_nop 1" :: "v"(op), "v"(v) : "memory"); }
    }
  }
  TS(11);
  TSFLUSH();
  return;
  }
  {
    if (b == SPECIAL) {
#pragma unroll
      for (int j = 0; j < 4; j++) lwait(FL + 64 + j * 4, 1);
    }
    if (lane == 0) stc1(rs, WS_F3 + (w * 32 + (col >> 3)) * 128 + (col & 7) * 4, ep);
    if (w == 0) lset(FL + 84);
    TS(8);
    gwait<SLP>(rs, WS_F3 + ((mtile >> 3) * 32 + 8 * w + (i >> 1)) * 128 + (i & 1) * 16, ep);
    lwait(FL + 80, 4);
    TS(9);
    {
      const unsigned v = (lane >> 3) * 4096 + ((((lane & 7) >> 1) ^ ((lane >> 4) & 3)) * 32) + (lane & 1) * 16;
      const char* src = ws + WS_YT + (size_t)(w * 128) * 4096 + mtile * 128;
      const unsigned la = laddr_of(lds) + L_Y + w * 16384;
#pragma unroll
      for (int j = 0; j < 4; j++) glds4c(la + j * 4096, src + (size_t)j * 131072, v, v + 31744, v + 2 * 31744, v + 3 * 31744);
    }
    WAITV(0);
    linc(FL + 32, lane);
    TS(10);
    const int toffB = L_Y + (4 * g + q) * 128 + (fsw * 32) + p * 8;
    f32x4 accb[4];
#pragma unroll
    for (int c = 0; c < 4; c++) accb[c] = f32x4{0, 0, 0, 0};
    s16x8 tb_[2][2][4];
    auto rd = [&](int u) {
      const char* slot = lds + (((u >> 1) + w) & 3) * 16384 + (u & 1) * 8192;
#pragma unroll
      for (int ks = 0; ks < 2; ks++)
#pragma unroll
        for (int c = 0; c < 4; c++) tb_[u & 1][ks][c] = trd2(slot + (toffB ^ (32 * c)) + ks * 4096, slot + (toffB ^ (32 * c)) + ks * 4096 + 2048);
    };
    auto mm = [&](int u) {
#pragma unroll
      for (int ks = 0; ks < 2; ks++)
#pragma unroll
        for (int c = 0; c < 4; c++) accb[c] = mma(as8(gf[2 * u + ks]), tb_[u & 1][ks][c], accb[c]);
    };
    rd(0);
    SCHED();
#pragma unroll
    for (int u = 1; u < 8; u++) {
      if (u == 2) lwait(FL + 32, 4);
      rd(u); mm(u - 1);
#pragma unroll
      for (int t = 0; t < 8; t++) { SGB(0x100, 2); SGB(0x008, 1); }
      SCHED();
    }
    mm(7);
#pragma unroll
    for (int c = 0; c < 4; c++) *(f32x4*)(lds + L_FFT + ((c * 4 + w) * 64 + lane) * 16) = accb[c];
    linc(FL + 48, lane);
    if (b == 0 && w == 0 && lane == 0) stc1(rs, WS_E, ep);
    TS(11);
    TSFLUSH();
  }
}

extern "C" void kernel_launch(void* const* d_in, const int* in_sizes, int n_in, void* d_out, int out_size, void* d_ws, size_t ws_size, hipStream_t stream) {
  gm_all<<<256, 512, 0, stream>>>((const char*)d_in[0], (const char*)d_in[1], (const char*)d_in[4], (const char*)d_in[5], (const char*)d_in[6],
                                  (const char*)d_in[2], (char*)d_ws, (char*)d_out);
}
```

```cpp
#include <hip/hip_runtime.h>
#include <stdint.h>

typedef short s16x4 __attribute__((ext_vector_type(4)));
typedef short s16x8 __attribute__((ext_vector_type(8)));
typedef float f32x4 __attribute__((ext_vector_type(4)));
typedef float f32x2 __attribute__((ext_vector_type(2)));
typedef unsigned u32x4 __attribute__((ext_vector_type(4)));
typedef unsigned u32x2 __attribute__((ext_vector_type(2)));
typedef __bf16 bfx2 __attribute__((ext_vector_type(2)));
#define LAS __attribute__((address_space(3)))
#define DI __device__ __forceinline__
#define ROT1 6
#define PITC 8320
#define PITY 4224
#define SLP 2

static constexpr unsigned CP = PITC, YP = PITY;
static constexpr unsigned WS_X1T = 0;
static constexpr unsigned WS_WT  = WS_X1T + (9u << 18);
static constexpr unsigned WS_PQ  = WS_WT + (9u << 18);
static constexpr unsigned WS_KVP = WS_PQ + (2u << 20);
static constexpr unsigned WS_GT  = WS_KVP + (512u << 10);
static constexpr unsigned WS_KVR = WS_GT + (16u << 10);
static constexpr unsigned WS_YT  = WS_KVR + (32u << 10);
static constexpr unsigned WS_E   = WS_YT + (9u << 18);
static constexpr unsigned WS_CNT = WS_E + 4096;
static constexpr unsigned WS_F0  = WS_CNT + 128 * 128;
static constexpr unsigned WS_F1  = WS_F0 + 8 * 128;
static constexpr unsigned WS_F2K = WS_F1 + 8 * 128;
static constexpr unsigned WS_F2Q = WS_F2K + 8 * 128;
static constexpr unsigned WS_F3  = WS_F2Q + 16 * 128;
static constexpr unsigned WS_END = WS_F3 + 16 * 128;
static constexpr int SPECIAL = 255;

static constexpr int L_WO = 0, L_Y = 65536, L_FFT = 131072, L_ATT = 147456, L_FL = 163840 - 256;

DI unsigned pk(float lo, float hi) { f32x2 p = {lo, hi}; return __builtin_bit_cast(unsigned, __builtin_convertvector(p, bfx2)); }
DI float bflo(unsigned u) { return __builtin_bit_cast(float, u << 16); }
DI float bfhi(unsigned u) { return __builtin_bit_cast(float, u & 0xffff0000u); }
DI float rbf(float v) { return bflo(pk(v, 0.f)); }
DI s16x8 as8(u32x4 v) { return __builtin_bit_cast(s16x8, v); }
DI s16x4 as4(u32x2 v) { return __builtin_bit_cast(s16x4, v); }
DI f32x4 mma(s16x8 a, s16x8 b, f32x4 c) { return __builtin_amdgcn_mfma_f32_16x16x32_bf16(a, b, c, 0, 0, 0); }
DI f32x4 mma16(s16x4 a, s16x4 b, f32x4 c) { return __builtin_amdgcn_mfma_f32_16x16x16bf16_1k(a, b, c, 0, 0, 0); }
DI void mfa(f32x4& c, s16x8 a, s16x8 b) { asm volatile("v_mfma_f32_16x16x32_bf16 %0, %1, %2, %0" : "+v"(c) : "v"(a), "v"(b)); }
DI s16x4 trd(const char* l) { return __builtin_amdgcn_ds_read_tr16_b64_v4i16((LAS s16x4*)l); }
DI s16x8 cat(s16x4 a, s16x4 b) { return __builtin_shufflevector(a, b, 0, 1, 2, 3, 4, 5, 6, 7); }
DI s16x8 trd2(const char* l0, const char* l1) { return cat(trd(l0), trd(l1)); }
DI s16x8 lrd(const char* l) { return *(const s16x8*)l; }
DI float fsin(float rev) { return __builtin_amdgcn_sinf(rev); }
DI float fcos(float rev) { return __builtin_amdgcn_cosf(rev); }
DI unsigned laddr_of(const char* l) { return (unsigned)(size_t)(const LAS char*)l; }
DI void glds4(unsigned laddr, const char* sb, unsigned v0, unsigned v1, unsigned v2, unsigned v3) {
  asm volatile("s_mov_b32 m0, %0\n\ts_nop 0\n\t"
               "global_load_lds_dwordx4 %2, %1\n\t"
               "global_load_lds_dwordx4 %3, %1 offset:1024\n\t"
               "global_load_lds_dwordx4 %4, %1 offset:2048\n\t"
               "global_load_lds_dwordx4 %5, %1 offset:3072"
               :: "s"(laddr), "s"(sb), "v"(v0), "v"(v1), "v"(v2), "v"(v3) : "memory");
}
DI void glds4c(unsigned laddr, const char* sb, unsigned v0, unsigned v1, unsigned v2, unsigned v3) {
  asm volatile("s_mov_b32 m0, %0\n\ts_nop 0\n\t"
               "global_load_lds_dwordx4 %2, %1 sc1\n\t"
               "global_load_lds_dwordx4 %3, %1 offset:1024 sc1\n\t"
               "global_load_lds_dwordx4 %4, %1 offset:2048 sc1\n\t"
               "global_load_lds_dwordx4 %5, %1 offset:3072 sc1"
               :: "s"(laddr), "s"(sb), "v"(v0), "v"(v1), "v"(v2), "v"(v3) : "memory");
}
DI int perm32(int t, int i) { return 8 * (i >> 2) + 4 * t + (i & 3); }
DI float phi(float t) {
  float e = __builtin_amdgcn_exp2f(t * 1.44269504088896341f);
  e = __builtin_amdgcn_fmed3f(e, 0.f, 1.f);
  return fmaxf(t + 1e-6f, 1e-6f) + e;
}
#define TSP
#define TSA
#define TSDECL
#define TS(slot)
#define TSFLUSH()
#define SCHED() __builtin_amdgcn_sched_barrier(0)
#define SGB(mask, n) __builtin_amdgcn_sched_group_barrier(mask, n, 0)
#define PIN(x) asm volatile("" : "+v"(x))
#define WAITV(n) asm volatile("s_waitcnt vmcnt(" #n ")" ::: "memory")
#define BAR() asm volatile("s_waitcnt lgkmcnt(0)\n\ts_barrier" ::: "memory")
#define FENCE() asm volatile("" ::: "memory")

typedef __amdgpu_buffer_rsrc_t rsrc_t;
DI u32x4 ldc(rsrc_t r, unsigned off) { return __builtin_amdgcn_raw_buffer_load_b128(r, off, 0, 16); }
DI void stc(rsrc_t r, unsigned off, u32x4 v) { __builtin_amdgcn_raw_buffer_store_b128(v, r, off, 0, 16); }
DI void stc1(rsrc_t r, unsigned off, unsigned v) { __builtin_amdgcn_raw_buffer_store_b32(v, r, off, 0, 16); }
template <int SL> DI void gwait(rsrc_t r, unsigned off, unsigned ep) {
#pragma unroll 1
  for (int sp = 0; sp < 200000; sp++) {
    FENCE();
    const u32x4 f = ldc(r, off);
    if (__all(f[0] == ep && f[1] == ep && f[2] == ep && f[3] == ep)) break;
    __builtin_amdgcn_s_sleep(SL);
  }
  FENCE();
}
DI unsigned ticket(rsrc_t r, int c, int lane) {
  unsigned old = 0;
  if (lane == 0) old = __builtin_amdgcn_raw_ptr_buffer_atomic_add_i32(1, r, WS_CNT + c * 128, 0, 16);
  old = __builtin_amdgcn_readfirstlane(old);
  FENCE();
  return old;
}
DI void arrive(rsrc_t r, int c, unsigned n, unsigned flag, int nrep, unsigned ep, int lane) {
  if (ticket(r, c, lane) == n - 1 && lane < nrep) stc(r, flag + lane * 128, u32x4{ep, ep, ep, ep});
}
DI void linc(char* p, int lane) { if (lane == 0) asm volatile("ds_add_u32 %0, %1" :: "v"(laddr_of(p)), "v"(1u) : "memory"); }
DI void lset(char* p) { asm volatile("ds_write_b32 %0, %1" :: "v"(laddr_of(p)), "v"(1u) : "memory"); }
DI void lwait(const char* p, unsigned want) {
#pragma unroll 1
  for (int sp = 0; sp < 4000000; sp++) {
    if ((unsigned)__builtin_amdgcn_readfirstlane(*(const volatile LAS unsigned*)p) >= want) break;
    __builtin_amdgcn_s_sleep(1);
  }
  FENCE();
}

DI unsigned load_epoch(const char* p) { unsigned e; asm volatile("s_load_dword %0, %1, 0x0\n\ts_waitcnt lgkmcnt(0)" : "=s"(e) : "s"(p) : "memory"); return e + 1u; }

DI void wo_dma(char* lds, const char* wo, int ntile, int j, int w, int lane) {
  const int blk = ((lane & 7) >> 1) ^ ((lane >> 4) & 3);
  const unsigned v = (lane >> 3) * 1024 + (blk & 1) * 32 + (blk >> 1) * 512 + (lane & 1) * 16;
  glds4(laddr_of(lds) + L_WO + j * 16384 + w * 4096, wo + (size_t)(j * 128 + w * 32) * 1024 + ntile * 64, v, v + 7168, v + 2 * 7168, v + 3 * 7168);
}

DI void p1_prod(char* lds, const char* x, const char* wqkv, const char* wo, rsrc_t rs, int xcd, int m, int col0, int col1, int rot, int ntile, unsigned& ep, const char* epp, int w, int lane) {
  unsigned voffA[4], voffB[4];
#pragma unroll
  for (int j = 0; j < 4; j++) {
    const int par = j & 1;
    voffA[j] = (lane >> 3) * 1024 + (((lane & 7) ^ ((4 * par + (lane >> 4)) & 7)) * 16) + j * (8192 - 1024);
    voffB[j] = (lane >> 3) * 3072 + ((((lane & 7) >> 1) ^ (((lane >> 4) & 1) | (par << 1))) * 32) + (lane & 1) * 16 + j * (24576 - 1024);
  }
  const unsigned lbase = laddr_of(lds) + w * 4096;
  const char* xa = x + (size_t)(m * 128 + w * 32) * 1024;
  const char* wb = wqkv + (size_t)((w & 1) * 32) * 3072 + ((w >> 1) ? col1 : col0) * 2;
  auto issue = [&](int idx) {
    const int ks = (idx + rot) & 7;
    const unsigned slot = lbase + (idx & 3) * 32768;
    glds4(slot, xa + ks * 128, voffA[0], voffA[1], voffA[2], voffA[3]);
    glds4(slot + 16384, wb + (size_t)(ks * 64) * 3072, voffB[0], voffB[1], voffB[2], voffB[3]);
  };
  issue(0); issue(1);
  WAITV(8); BAR();
  issue(2); issue(3);
  ep = load_epoch(epp);
#pragma unroll 1
  for (int j = 1; j < 5; j++) { WAITV(16); BAR(); issue(j + 3); }
  WAITV(16); BAR();
  wo_dma(lds, wo, ntile, 0, w, lane);
  WAITV(12); BAR();
  wo_dma(lds, wo, ntile, 1, w, lane);
  WAITV(8); BAR();
  if (w == 0) { gwait<SLP>(rs, WS_F0 + xcd * 128, ep); lset(lds + L_FL + 100); }
  wo_dma(lds, wo, ntile, 2, w, lane);
  wo_dma(lds, wo, ntile, 3, w, lane);
}

template <bool QT>
DI void p1_gemm(TSP char* lds, rsrc_t rs, int m, int nt, unsigned& ep, const char* epp, int w, int lane) {
  const int i = lane & 15, g = lane >> 4, q = i >> 2, p = lane & 3;
  f32x4 acc[8][2];
#pragma unroll
  for (int a = 0; a < 8; a++) { acc[a][0] = f32x4{0, 0, 0, 0}; acc[a][1] = f32x4{0, 0, 0, 0}; }
  const int aoff = i * 128 + ((g ^ (i >> 1)) * 16);
  const int boff = 16384 + (8 * g + q) * 128 + ((w ^ (((q >> 1) & 1) | ((g & 1) << 1))) * 32) + p * 8;
  s16x8 A[2][2][8]; s16x4 B[2][2][2][2];
  auto rd1 = [&](int buf, const char* slot, int r) {
    const int ks = r / 12, rr = r % 12;
    const int t = rr == 0 ? 0 : rr == 1 ? 1 : rr == 3 ? 2 : rr == 4 ? 3 : -1;
    const int a = rr == 2 ? 0 : rr - 4;
    if (t >= 0) B[buf][ks][t >> 1][t & 1] = trd(slot + boff + (t >> 1) * 8192 + ks * 4096 + (t & 1) * 512);
    else A[buf][ks][a] = lrd(slot + (aoff ^ (ks * 64)) + a * 2048);
  };
  auto mf1 = [&](int buf, int ks, int mt, int hh) {
    const s16x8 bf = cat(B[buf][ks][hh][0], B[buf][ks][hh][1]);
    if (QT) mfa(acc[mt][hh], bf, A[buf][ks][mt]); else mfa(acc[mt][hh], A[buf][ks][mt], bf);
  };
  auto region = [&](int cur, const char* slot) {
#pragma unroll
    for (int mi = 0; mi < 32; mi++) {
      mf1(cur, mi >> 4, (mi & 15) >> 1, mi & 1);
      SCHED();
      if (mi < 24) { rd1(cur ^ 1, slot, mi); SCHED(); }
    }
  };
  ep = load_epoch(epp);
  const bool zw = !QT && blockIdx.x == 16 && w == 3;
  if (zw) { stc1(rs, WS_CNT + lane * 128, 0u); stc1(rs, WS_CNT + (64 + lane) * 128, 0u); }
  __builtin_amdgcn_s_waitcnt(0xC07F);
  BAR();
  TS(1);
#pragma unroll
  for (int r = 0; r < 24; r++) rd1(0, lds, r);
  SCHED();
#pragma unroll 1
  for (int it = 0; it < 3; it++) {
    BAR(); SCHED();
    region(0, lds + ((2 * it + 1) & 3) * 32768);
    BAR(); SCHED();
    region(1, lds + ((2 * it + 2) & 3) * 32768);
    if (zw && it == 0) {
      WAITV(0);
      if (lane < 8) stc(rs, WS_F0 + lane * 128, u32x4{ep, ep, ep, ep});
    }
  }
  BAR(); SCHED();
  region(0, lds + 3 * 32768);
  asm volatile("s_nop 7");
  SCHED();
  auto mmh = [&](int hh) {
#pragma unroll
    for (int mt = 0; mt < 8; mt++)
#pragma unroll
      for (int ks = 0; ks < 2; ks++) {
        const s16x8 bf = cat(B[1][ks][hh][0], B[1][ks][hh][1]);
        acc[mt][hh] = QT ? mma(bf, A[1][ks][mt], acc[mt][hh]) : mma(A[1][ks][mt], bf, acc[mt][hh]);
      }
  };
  mmh(0);
  SCHED();
  TS(2);
  if (QT) {
    mmh(1);
#pragma unroll
    for (int mt = 0; mt < 8; mt++) {
      u32x4 v = {pk(phi(acc[mt][0][0]), phi(acc[mt][0][1])), pk(phi(acc[mt][0][2]), phi(acc[mt][0][3])),
                 pk(phi(acc[mt][1][0]), phi(acc[mt][1][1])), pk(phi(acc[mt][1][2]), phi(acc[mt][1][3]))};
      stc(rs, WS_PQ + (((m * 8 + mt) * 16 + (nt - 8) * 4 + w) * 64 + lane) * 16, v);
    }
    lwait(lds + L_FL + 100, 1);
    WAITV(0);
    arrive(rs, 34 + m, 16, WS_F2Q + m * 128, 1, ep, lane);
  } else {
    mmh(1);
    u32x4 pa[4];
#pragma unroll
    for (int u = 0; u < 4; u++)
      pa[u] = u32x4{pk(phi(acc[2 * u][0][0]), phi(acc[2 * u][0][1])), pk(phi(acc[2 * u][0][2]), phi(acc[2 * u][0][3])),
                    pk(phi(acc[2 * u + 1][0][0]), phi(acc[2 * u + 1][0][1])), pk(phi(acc[2 * u + 1][0][2]), phi(acc[2 * u + 1][0][3]))};
#pragma unroll
    for (int t = 0; t < 16; t++) { SGB(0x008, 1); SGB(0x002, 2); }
    SCHED();
    f32x4 kv = {0, 0, 0, 0}, z = {0, 0, 0, 0};
    const u32x4 ones = {0x3f803f80u, 0x3f803f80u, 0x3f803f80u, 0x3f803f80u};
#pragma unroll
    for (int u = 0; u < 4; u++) {
      u32x4 b = {pk(acc[2 * u][1][0], acc[2 * u][1][1]), pk(acc[2 * u][1][2], acc[2 * u][1][3]),
                 pk(acc[2 * u + 1][1][0], acc[2 * u + 1][1][1]), pk(acc[2 * u + 1][1][2], acc[2 * u + 1][1][3])};
      z = mma(as8(pa[u]), as8(ones), z);
      kv = mma(as8(pa[u]), as8(b), kv);
    }
    const int head = nt * 4 + w;
    u32x4 v = {pk(kv[0], kv[1]), pk(kv[2], kv[3]), pk(z[0], z[1]), pk(z[2], z[3])};
    stc(rs, WS_KVP + ((m * 32 + head) * 64 + lane) * 16, v);
    lwait(lds + L_FL + 100, 1);
    WAITV(0);
    if (ticket(rs, 1 + head, lane) == 15) {
      TS(4);
      u32x4 kl[16];
#pragma unroll
      for (int pp = 0; pp < 16; pp++) kl[pp] = ldc(rs, WS_KVP + ((pp * 32 + head) * 64 + lane) * 16);
      u32x4 r;
#pragma unroll
      for (int d = 0; d < 4; d++) {
        float lo[4], hi[4];
#pragma unroll
        for (int j = 0; j < 4; j++) {
          lo[j] = (bflo(kl[4 * j][d]) + bflo(kl[4 * j + 1][d])) + (bflo(kl[4 * j + 2][d]) + bflo(kl[4 * j + 3][d]));
          hi[j] = (bfhi(kl[4 * j][d]) + bfhi(kl[4 * j + 1][d])) + (bfhi(kl[4 * j + 2][d]) + bfhi(kl[4 * j + 3][d]));
        }
        r[d] = pk((lo[0] + lo[1]) + (lo[2] + lo[3]), (hi[0] + hi[1]) + (hi[2] + hi[3]));
      }
      const int u = (head >> 3) * 4 + (head & 3), hh = (head >> 2) & 1;
      const bool zr = (i & 3) == hh + 2 * (u & 1);
      __builtin_amdgcn_raw_buffer_store_b64(u32x2{r[0], r[1]}, rs, WS_KVR + (u * 64 + lane) * 16 + hh * 8, 0, 16);
      if (zr) stc(rs, WS_KVR + 16384 + ((u >> 1) * 64 + lane) * 16, hh ? u32x4{0u, 0u, r[2], r[3]} : u32x4{r[2], r[3], 0u, 0u});
      TS(5);
      WAITV(0);
      arrive(rs, 33, 32, WS_F2K, 8, ep, lane);
      TS(6);
    }
  }
}

DI float dpp_sum16(float v) {
  v += __builtin_bit_cast(float, __builtin_amdgcn_update_dpp(0, __builtin_bit_cast(int, v), 0x128, 0xf, 0xf, false));
  v += __builtin_bit_cast(float, __builtin_amdgcn_update_dpp(0, __builtin_bit_cast(int, v), 0x124, 0xf, 0xf, false));
  v += __builtin_bit_cast(float, __builtin_amdgcn_update_dpp(0, __builtin_bit_cast(int, v), 0x122, 0xf, 0xf, false));
  v += __builtin_bit_cast(float, __builtin_amdgcn_update_dpp(0, __builtin_bit_cast(int, v), 0x121, 0xf, 0xf, false));
  return v;
}

DI void p1_rows(TSP char* lds, const char* x, const char* gw, u32x4& gwv, rsrc_t rs, int a, unsigned& ep, const char* epp, int w, int lane) {
  const int i = lane & 15, g = lane >> 4;
  char* LX = lds;
  char* LS = lds + 32768;
  const char* LC = lds + 106496;
  const int amt = (a >> 1) & 1;
  {
    const unsigned v = lane * 16;
    const char* src = x + (size_t)(128 * (a >> 2) + perm32(amt, 8 * (a & 1) + w)) * 1024;
    glds4(laddr_of(LX) + w * 4096, src, v, v + 31 * 1024, v + 62 * 1024, v + 93 * 1024);
    glds4(laddr_of(LX) + w * 4096 + 16384, src + 8192, v, v + 31 * 1024, v + 62 * 1024, v + 93 * 1024);
  }
  int soff[4];
#pragma unroll
  for (int r = 0; r < 4; r++) {
    const int k2 = 4 * g + r;
    const int k = i + 32 * k2;
    const int col = (i == 0) ? 16 * k2 : (k2 < 8 ? k : 512 - k);
    soff[r] = col * 128 + ((w ^ (col & 7)) * 16);
    PIN(soff[r]);
  }
  TS(12);
  BAR();
  TS(13);
  const u32x4 b1re = *(const u32x4*)(LC + lane * 16), b1im = *(const u32x4*)(LC + 1024 + lane * 16);
  const u32x4 a3re = *(const u32x4*)(LC + 2048 + lane * 16), a3im = *(const u32x4*)(LC + 3072 + lane * 16);
  const u32x4 a4re = *(const u32x4*)(LC + 4096 + lane * 16), a4im = *(const u32x4*)(LC + 5120 + lane * 16);
  const f32x4 twc = *(const f32x4*)(LC + 6144 + lane * 16), tws = *(const f32x4*)(LC + 7168 + lane * 16);
  ep = load_epoch(epp);
  WAITV(0);
  TS(1);
#pragma unroll 1
  for (int hf = 0; hf < 2; hf++) {
    unsigned o[4][4];
#pragma unroll
    for (int a4 = 0; a4 < 4; a4++) {
      const char* row = LX + ((w + 4 * hf) * 4 + a4) * 1024;
      s16x8 xf = trd2(row + lane * 8, row + 512 + lane * 8);
      const f32x4 z4 = {0, 0, 0, 0};
      f32x4 are = mma(xf, as8(b1re), z4), aim = mma(xf, as8(b1im), z4);
      f32x4 tr = are * twc + aim * tws, ti = aim * twc - are * tws;
      asm("" : "+v"(tr), "+v"(ti));
      u32x4 bb;
#pragma unroll
      for (int r = 0; r < 4; r++) bb[r] = pk(tr[r], ti[r]);
      f32x4 xre = mma(as8(a3re), as8(bb), z4), xim = mma(as8(a3im), as8(bb), z4);
      f32x4 sre = mma(as8(a4re), as8(bb), z4), sim = mma(as8(a4im), as8(bb), z4);
#pragma unroll
      for (int r = 0; r < 4; r++) o[r][a4] = pk((i == 0) ? sre[r] : xre[r], (i == 0) ? sim[r] : xim[r]);
    }
    if (lane == 0) {
      u32x4 v = {o[0][0] >> 16, o[0][1] >> 16, o[0][2] >> 16, o[0][3] >> 16};
      *(u32x4*)(LS + 256 * 128 + (w + 4 * hf) * 16) = v;
#pragma unroll
      for (int a4 = 0; a4 < 4; a4++) o[0][a4] &= 0xffffu;
    }
#pragma unroll
    for (int r = 0; r < 4; r++) {
      u32x4 v = {o[r][0], o[r][1], o[r][2], o[r][3]};
      *(u32x4*)(LS + (soff[r] ^ (hf * 64))) = v;
    }
  }
  TS(14);
  linc(lds + L_FL + 108, lane);
  gwv = *(const u32x4*)(gw + lane * 16);
  lwait(lds + L_FL + 108, 4);
  TS(2);
  const int sl = lane & 7;
  const int sc = w * 8 + (lane >> 3);
  const int xline = (amt * 4 + (a >> 4)) * 1024 + ((a >> 2) & 3) * 256 + (a & 1) * 128;
  u32x4 v0[8];
#pragma unroll
  for (int j = 0; j < 8; j++) v0[j] = *(const u32x4*)(LS + (j * 32 + sc) * 128 + ((sl ^ (sc & 7)) * 16));
#pragma unroll
  for (int j = 0; j < 8; j++) stc(rs, WS_X1T + (j * 32 + sc) * CP + xline + sl * 16, v0[j]);
  if (w == 3 && lane < 8) stc(rs, WS_X1T + 256 * CP + xline + lane * 16, *(const u32x4*)(LS + 256 * 128 + lane * 16));
  TS(15);
}

DI void p1_filt(TSP char* lds, const char* wr, const char* wi, rsrc_t rs, int a, unsigned& ep, const char* epp, int w, int lane) {
  const int i = lane & 15, g = lane >> 4;
  char* LF = lds + 69632;
  {
    const float fi = (float)i;
    char* cp = lds + 106496 + lane * 16 + w * 4;
    float c[2], s[2];
#pragma unroll
    for (int e = 0; e < 2; e++) {
      const float ang = (float)(4 * g + 16 * (w >> 1) + 2 * (w & 1) + e) * (fi * (1.f / 32.f));
      c[e] = fcos(ang);
      s[e] = (i == 0) ? (e ? -1.f : 1.f) : fsin(-ang);
    }
    *(unsigned*)(cp) = pk(c[0], c[1]);
    *(unsigned*)(cp + 1024) = pk(s[0], s[1]);
    const float n2i = (float)(4 * g + w) * fi;
    {
      const float ang = n2i * (1.f / 16.f);
      const float cs = fcos(ang), sn = fsin(ang);
      *(unsigned*)(cp + 2048) = pk(cs, sn);
      *(unsigned*)(cp + 3072) = pk(sn, cs) ^ ((i >= 8) ? 0x80000000u : 0x00008000u);
    }
    {
      const float ang = n2i * (1.f / 32.f);
      const float cs = fcos(ang);
      const float sn = (i == 0) ? ((w & 1) ? -1.f : 1.f) : fsin(-ang);
      const unsigned u = pk(cs, sn);
      const int sh = (i & 1) * 16;
      *(unsigned*)(cp + 4096) = (u & 0xffffu) << sh;
      *(unsigned*)(cp + 5120) = (u >> 16) << sh;
    }
    *(float*)(cp + 6144) = fcos(n2i * (1.f / 512.f));
    *(float*)(cp + 7168) = fsin(n2i * (1.f / 512.f));
  }
  TS(12);
  BAR();
  {
    const unsigned v = lane * 16;
    const size_t off = (size_t)(8 * (a & 7) + 64 * (4 * (a >> 3) + w)) * 514;
    glds4(laddr_of(LF) + w * 4352, wr + off, v, v, v, v);
    glds4(laddr_of(LF) + (w + 4) * 4352, wi + off, v, v, v, v);
    if (lane == 0) {
      asm volatile("s_mov_b32 m0, %0\n\ts_nop 0\n\tglobal_load_lds_dwordx4 %2, %1" :: "s"(laddr_of(LF) + w * 4352 + 4096), "s"(wr + off + 4096), "v"(v) : "memory");
      asm volatile("s_mov_b32 m0, %0\n\ts_nop 0\n\tglobal_load_lds_dwordx4 %2, %1" :: "s"(laddr_of(LF) + (w + 4) * 4352 + 4096), "s"(wi + off + 4096), "v"(v) : "memory");
    }
  }
  ep = load_epoch(epp);
  WAITV(0);
  linc(lds + L_FL + 112, lane);
  lwait(lds + L_FL + 112, 4);
  TS(1);
  const int sl = lane & 7;
  const int wline = ((((a & 7) >> 1) * 2 + ((a >> 3) & 1)) * 64 + (a >> 4) * 16 + (a & 1) * 8) * 16;
#pragma unroll 1
  for (int hf = 0; hf < 2; hf++) {
    const int cb = (lane >> 3) * 16 + w + 128 * hf;
    const char* fp = LF + sl * 514 + cb * 2;
#pragma unroll
    for (int j = 0; j < 4; j++) {
      u32x4 fv;
#pragma unroll
      for (int r = 0; r < 4; r++) fv[r] = (unsigned)*(const unsigned short*)(fp + r * 4352 + j * 8) | ((unsigned)*(const unsigned short*)(fp + (4 + r) * 4352 + j * 8) << 16);
      stc(rs, WS_WT + (cb + 4 * j) * CP + wline + sl * 16, fv);
    }
  }
  if (w == 3 && lane < 8) {
    u32x4 v;
#pragma unroll
    for (int r = 0; r < 4; r++) v[r] = (unsigned)*(const unsigned short*)(LF + sl * 514 + 512 + r * 4352) | ((unsigned)*(const unsigned short*)(LF + sl * 514 + 512 + (4 + r) * 4352) << 16);
    stc(rs, WS_WT + 256 * CP + wline + sl * 16, v);
  }
}
DI void p1_aux_finish(char* lds, rsrc_t rs, int xcd, unsigned ep, int w8, int lane) {
  if (w8 == 4) gwait<SLP>(rs, WS_F0 + xcd * 128, ep);
  WAITV(0);
  BAR();
  if (w8 == 1) arrive(rs, 0, 64, WS_F1, 8, ep, lane);
}

DI void p1_gate(char* lds, u32x4 gwv, rsrc_t rs, int a, int w, int lane) {
  const int i = lane & 15, g = lane >> 4;
  const u32x4 ones = {0x3f803f80u, 0x3f803f80u, 0x3f803f80u, 0x3f803f80u};
#pragma unroll 1
  for (int hf = 0; hf < 2; hf++) {
    float gsum[4];
#pragma unroll
    for (int j = 0; j < 4; j++) {
      u32x4 xv = *(const u32x4*)(lds + ((w + 4 * hf) * 4 + j) * 1024 + lane * 16);
      u32x4 pr;
#pragma unroll
      for (int d = 0; d < 4; d++) pr[d] = pk(bflo(xv[d]) * bflo(gwv[d]), bfhi(xv[d]) * bfhi(gwv[d]));
      gsum[j] = mma(as8(ones), as8(pr), f32x4{0, 0, 0, 0})[0];
    }
    float gs = (g == 0) ? gsum[0] : (g == 1) ? gsum[1] : (g == 2) ? gsum[2] : gsum[3];
    gs = rbf(dpp_sum16(gs));
    const float gg = rbf(__builtin_amdgcn_rcpf(1.f + __builtin_amdgcn_exp2f(gs * -1.44269504088896341f)));
    const int s = 32 * (4 * (a >> 2) + g) + perm32((a >> 1) & 1, 8 * (a & 1) + w + 4 * hf);
    if (i == 0) stc1(rs, WS_GT + (s >> 6) * 256 + (s & 15) * 16 + ((s >> 4) & 3) * 4, pk(gg, 1.f - gg));
  }
}

template <class F> DI void p2_fft(TSP F idle, char* lb, char* fl, char* f1seen, const char* ws, rsrc_t rs, int c, int rep, unsigned ep, int w, int lane) {
  const int i = lane & 15, g = lane >> 4;
  const int k1 = 16 * w + i;
  const float fk1 = (float)k1, fg = (float)g;
  u32x4 fre[4], fim[4], fc[2], fs[2], fn[2];
  {
    const float st = fk1 * (1.f / 64.f);
    const float a0 = fg * 4.f * st;
    const bool odd = i & 1;
    const unsigned m2 = odd ? 0x80008000u : 0u;
    const unsigned mre = (i & 2) ? 0x80008000u : 0u;
    const unsigned mim = ((i + 1) & 2) ? 0x80008000u : 0u;
#pragma unroll
    for (int d = 0; d < 4; d++) {
      const float ang = __builtin_fmaf((float)d, st, a0);
      const float cs = fcos(ang), sn = fsin(ang);
      const unsigned r0 = pk(cs, sn), i0 = pk(sn, cs) ^ 0x8000u;
      const unsigned r1 = (odd ? i0 : r0) ^ mre, i1 = (odd ? r0 : i0) ^ mim;
      fre[0][d] = r0; fim[0][d] = i0;
      fre[1][d] = r1; fim[1][d] = i1;
      fre[2][d] = r0 ^ m2; fim[2][d] = i0 ^ m2;
      fre[3][d] = r1 ^ m2; fim[3][d] = i1 ^ m2;
    }
#pragma unroll
    for (int ks = 0; ks < 4; ks++) { PIN(fre[ks]); PIN(fim[ks]); }
  }
  float c1[2][4], s1[2][4], c2[2][4], s2[2][4];
#pragma unroll
  for (int mt = 0; mt < 2; mt++) {
    const float fa = (float)perm32(mt, i);
    const float st = fa * (1.f / 32.f), a0 = fg * 8.f * st;
    const float st1 = fk1 * (1.f / 2048.f), b0 = (fg * 8.f + (float)(4 * mt)) * st1;
    const float st2 = fa * (1.f / 2048.f), d0 = (fg * 4.f + (float)(16 * w)) * st2;
#pragma unroll
    for (int d = 0; d < 4; d++) {
      const float x0 = __builtin_fmaf((float)(2 * d), st, a0), x1 = __builtin_fmaf((float)(2 * d + 1), st, a0);
      fc[mt][d] = pk(fcos(x0), fcos(x1));
      fs[mt][d] = pk(fsin(x0), fsin(x1));
      fn[mt][d] = fs[mt][d] ^ 0x80008000u;
      const float t1 = __builtin_fmaf((float)d, st1, b0);
      c1[mt][d] = fcos(t1); s1[mt][d] = fsin(t1);
      const float t2 = __builtin_fmaf((float)d, st2, d0);
      c2[mt][d] = fcos(t2); s2[mt][d] = fsin(t2);
      PIN(c1[mt][d]); PIN(s1[mt][d]); PIN(c2[mt][d]); PIN(s2[mt][d]);
    }
    PIN(fc[mt]); PIN(fs[mt]); PIN(fn[mt]);
  }
  idle();
  TS(4);
  if (w == 0) { gwait<SLP>(rs, WS_F1 + rep * 128, ep); lset(f1seen); }
  else lwait(f1seen, 1);
  TS(5);
  asm volatile("s_mov_b32 m0, %0\n\ts_nop 0\n\tglobal_load_lds_dwordx4 %2, %1 sc1\n\tglobal_load_lds_dwordx4 %2, %1 offset:1024 sc1"
               :: "s"(laddr_of(lb) + 8192 + w * 2048), "s"(ws + WS_X1T + (size_t)c * CP + w * 2048), "v"(lane * 16) : "memory");
  u32x4 wf[2];
#pragma unroll
  for (int mt = 0; mt < 2; mt++) wf[mt] = ldc(rs, WS_WT + c * CP + ((w * 2 + mt) * 64 + lane) * 16);
  WAITV(2);
  linc(fl, lane);
  lwait(fl, 4);
  TS(6);
  const f32x4 z4 = {0, 0, 0, 0};
  f32x4 re[2], im[2];
#pragma unroll
  for (int mt = 0; mt < 2; mt++) { re[mt] = z4; im[mt] = z4; }
#pragma unroll
  for (int ks = 0; ks < 4; ks++)
#pragma unroll
    for (int mt = 0; mt < 2; mt++) {
      s16x8 xa = lrd(lb + 8192 + ((mt * 4 + ks) * 64 + lane) * 16);
      re[mt] = mma(xa, as8(fre[ks]), re[mt]); im[mt] = mma(xa, as8(fim[ks]), im[mt]);
    }
  u32x4 pr, pi;
#pragma unroll
  for (int mt = 0; mt < 2; mt++) {
    float tr[4], ti[4];
#pragma unroll
    for (int r = 0; r < 4; r++) { tr[r] = re[mt][r] * c1[mt][r] + im[mt][r] * s1[mt][r]; ti[r] = im[mt][r] * c1[mt][r] - re[mt][r] * s1[mt][r]; }
    pr[2 * mt] = pk(tr[0], tr[1]); pr[2 * mt + 1] = pk(tr[2], tr[3]);
    pi[2 * mt] = pk(ti[0], ti[1]); pi[2 * mt + 1] = pk(ti[2], ti[3]);
  }
#pragma unroll
  for (int mt = 0; mt < 2; mt++) {
    re[mt] = mma(as8(fs[mt]), as8(pi), mma(as8(fc[mt]), as8(pr), z4));
    im[mt] = mma(as8(fn[mt]), as8(pr), mma(as8(fc[mt]), as8(pi), z4));
  }
#pragma unroll
  for (int mt = 0; mt < 2; mt++) {
    float tr[4], ti[4];
#pragma unroll
    for (int r = 0; r < 4; r++) {
      const float a = bflo(wf[mt][r]), b = bfhi(wf[mt][r]);
      tr[r] = re[mt][r] * a - im[mt][r] * b; ti[r] = re[mt][r] * b + im[mt][r] * a;
    }
    pr[2 * mt] = pk(tr[0], tr[1]); pr[2 * mt + 1] = pk(tr[2], tr[3]);
    pi[2 * mt] = pk(ti[0], ti[1]); pi[2 * mt + 1] = pk(ti[2], ti[3]);
  }
#pragma unroll
  for (int nt = 0; nt < 2; nt++) {
    re[nt] = mma(as8(pi), as8(fn[nt]), mma(as8(pr), as8(fc[nt]), z4));
    im[nt] = mma(as8(pi), as8(fc[nt]), mma(as8(pr), as8(fs[nt]), z4));
  }
#pragma unroll
  for (int nt = 0; nt < 2; nt++) {
    u32x4 v;
#pragma unroll
    for (int r = 0; r < 4; r++) v[r] = pk(re[nt][r] * c2[nt][r] - im[nt][r] * s2[nt][r], -re[nt][r] * s2[nt][r] - im[nt][r] * c2[nt][r]);
    *(u32x4*)(lb + ((nt * 4 + w) * 64 + lane) * 16) = v;
  }
  linc(fl + 4, lane);
  lwait(fl + 4, 4);
#pragma unroll
  for (int mt = 0; mt < 2; mt++) { re[mt] = z4; im[mt] = z4; }
#pragma unroll
  for (int ks = 0; ks < 4; ks++)
#pragma unroll
    for (int mt = 0; mt < 2; mt++) {
      s16x8 ya = lrd(lb + ((mt * 4 + ks) * 64 + lane) * 16);
      re[mt] = mma(ya, as8(fre[ks]), re[mt]); im[mt] = mma(ya, as8(fim[ks]), im[mt]);
    }
  u32x4 vr = {pk(re[0][0], re[0][1]), pk(re[0][2], re[0][3]), pk(re[1][0], re[1][1]), pk(re[1][2], re[1][3])};
  u32x4 vi = {pk(im[0][0], im[0][1]), pk(im[0][2], im[0][3]), pk(im[1][0], im[1][1]), pk(im[1][2], im[1][3])};
  const unsigned yt = WS_YT + (32 * k1 + 8 * g) * 2;
  const int rr = (c == 256) ? 1 : (c & 1) * 256 + (c & ~1);
  stc(rs, yt + rr * YP, vr);
  if (c & 255) stc(rs, yt + (rr + 1) * YP, vi);
  TS(7);
}

extern "C" __global__ __launch_bounds__(512, 2) void gm_all(const char* x, const char* wqkv, const char* wr, const char* wi, const char* gw, const char* wo, char* ws, char* out) {
  __shared__ __attribute__((aligned(16))) char lds[163840];
  const int b = blockIdx.x, xcd = b & 7, ii = b >> 3;
  const int w8 = __builtin_amdgcn_readfirstlane(threadIdx.x >> 6), lane = threadIdx.x & 63;
  const int w = w8 & 3;
  TSDECL
  TS(0);
  if (ii < 2 && w8 < 4) {
    unsigned long long pc; int o0, o1;
    asm volatile("s_getpc_b64 %0\n.Lgm_pc%=:\n\ts_mov_b32 %1, gm_all-.Lgm_pc%=\n\ts_mov_b32 %2, gm_end-.Lgm_pc%=" : "=s"(pc), "=s"(o0), "=s"(o1));
    const unsigned off = (ii * 256 + threadIdx.x) * 128;
    if (off < (unsigned)(o1 - o0))
      asm volatile("s_mov_b32 m0, %0\n\ts_nop 0\n\tglobal_load_lds_dword %2, %1" :: "s"(laddr_of(lds) + L_ATT + 8192), "s"(pc + (long long)o0), "v"(off) : "memory");
  }
  unsigned ep;
  const char* epp = ws + WS_E;
  const rsrc_t rs = __builtin_amdgcn_make_buffer_rsrc(ws, 0, WS_END, 0x00020000);
  char* FL = lds + L_FL;
  const int ml = ii & 3, nl = ii >> 2;
  const int mtile = xcd * 4 + ml, ntile = nl;
  const bool aux = ii >= 24;
  {
    const int m = (xcd >> 1) * 4 + ml;
    const int nt = nl < 4 ? (xcd & 1) * 4 + nl : 8 + (xcd & 1) * 2 + (nl - 4);
    if (w8 < 4 || aux) {
      if (w8 == 0) *(LAS unsigned*)(FL + lane * 4) = 0;
      if (w8 >= 4) p1_filt(TSA lds, wr, wi, rs, xcd * 8 + (ii - 24), ep, epp, w, lane);
      else {
        __builtin_amdgcn_s_setprio(2);
        if (aux) {
          u32x4 gwv;
          p1_rows(TSA lds, x, gw, gwv, rs, xcd * 8 + (ii - 24), ep, epp, w, lane);
          p1_gate(lds, gwv, rs, xcd * 8 + (ii - 24), w, lane);
          p1_aux_finish(lds, rs, xcd, ep, w8, lane);
#pragma unroll 1
          for (int j = 0; j < 4; j++) wo_dma(lds, wo, ntile, j, w, lane);
        }
        else if (nl < 4) p1_gemm<false>(TSA lds, rs, m, nt, ep, epp, w, lane);
        else p1_gemm<true>(TSA lds, rs, m, nt, ep, epp, w, lane);
        __builtin_amdgcn_s_setprio(0);
      }
    } else {
      const int col0 = nl >= 4 ? (nt - 8) * 128 : 512 + nt * 64;
      const int col1 = nl >= 4 ? col0 + 64 : col0 + 512;
      p1_prod(lds, x, wqkv, wo, rs, xcd, m, col0, col1, (2 * ml + nl) & ROT1, ntile, ep, epp, w, lane);
    }
  }
  TS(3);
  const int i = lane & 15, g = lane >> 4, q = i >> 2, p = lane & 3;
  const int fsw = (2 * g + (q >> 1)) & 3;
  const int col = (b + 1) & 255;
  if (w8 >= 4 || b == SPECIAL) __builtin_amdgcn_s_setprio(1);
  if (w8 >= 4 || b == SPECIAL) {
    const bool pg = w8 >= 4;
    p2_fft(TSA [&]() { if (pg) { if (aux) p1_aux_finish(lds, rs, xcd, ep, w8, lane); else { WAITV(0); linc(FL, lane); } } }, pg ? lds + L_FFT : lds + L_Y, pg ? FL + 4 : FL + 20, pg ? FL + 16 : FL + 28, ws, rs, pg ? col : 256, xcd, ep, w, lane);
  }
  if (w8 < 4) {
    WAITV(0);
    linc(FL + 80, lane);
    if (aux) linc(FL, lane);
    TS(7);
    const int stg = 4 * mtile + w;
    const int toffA = L_WO + (4 * g + q) * 128 + (fsw * 32) + p * 8;
    auto wfrd = [&](int u, int a) { const char* pp = lds + (toffA ^ (32 * a)) + (u >> 2) * 16384 + (u & 3) * 2048; return trd2(pp, pp + 8192); };
    if (w == 0) { gwait<SLP>(rs, WS_F2Q + (mtile >> 1) * 128, ep); lset(FL + 104); }
    lwait(FL, 4);
    s16x8 wf[4][4];
#pragma unroll
    for (int u = 0; u < 4; u++)
#pragma unroll
      for (int a = 0; a < 4; a++) wf[u][a] = wfrd(u, a);
    linc(FL + 12, lane);
    lwait(FL + 104, 1);
    u32x4 pq[16];
#pragma unroll
    for (int u = 0; u < 16; u++) pq[u] = ldc(rs, WS_PQ + ((stg * 16 + u) * 64 + lane) * 16);
#pragma unroll
    for (int u = 0; u < 4; u++)
#pragma unroll
      for (int a = 0; a < 4; a++) PIN(wf[u][a]);
    lwait(FL + 12, 4);
    if (w == 0) { gwait<SLP>(rs, WS_F2K + xcd * 128, ep); lset(FL + 88); }
    else lwait(FL + 88, 1);
    TS(8);
    asm volatile("s_mov_b32 m0, %0\n\ts_nop 0\n\tglobal_load_lds_dwordx4 %2, %1 sc1\n\tglobal_load_lds_dwordx4 %2, %1 offset:1024 sc1"
                 :: "s"(laddr_of(lds) + L_ATT + w * 2048), "s"(ws + WS_KVR + 16384 + w * 2048), "v"(lane * 16) : "memory");
    glds4c(laddr_of(lds) + w * 4096, ws + WS_KVR + w * 4096, lane * 16, lane * 16, lane * 16, lane * 16);
    WAITV(0);
    linc(FL + 92, lane);
    lwait(FL + 16, 1);
    const unsigned gv = __builtin_amdgcn_raw_buffer_load_b32(rs, WS_GT + mtile * 256 + i * 16 + w * 4, 0, 16);
    lwait(FL + 92, 4);
    TS(13);
    f32x4 acc[4];
#pragma unroll
    for (int a = 0; a < 4; a++) acc[a] = f32x4{0, 0, 0, 0};
    const f32x4 z4 = {0, 0, 0, 0};
    __builtin_amdgcn_s_setprio(2);
    f32x4 rd[8];
#pragma unroll
    for (int v = 0; v < 8; v++) {
      const f32x4 eps = {1e-6f, 1e-6f, 1e-6f, 1e-6f};
      const s16x8 zv = lrd(lds + L_ATT + v * 1024 + lane * 16);
      const f32x4 d0 = mma(zv, as8(pq[2 * v]), eps), d1 = mma(zv, as8(pq[2 * v + 1]), eps);
      rd[v] = f32x4{d0[0], d0[1], d1[2], d1[3]};
    }
    u32x4 kvn = *(const u32x4*)(lds + lane * 16), kvn2 = *(const u32x4*)(lds + 1024 + lane * 16);
    SCHED();
#pragma unroll
    for (int v = 0; v < 8; v++)
#pragma unroll
      for (int r = 0; r < 4; r++) rd[v][r] = __builtin_amdgcn_rcpf(rd[v][r]);
    f32x4 n0 = mma16(as4(u32x2{kvn[0], kvn[1]}), as4(u32x2{pq[0][0], pq[0][1]}), z4);
    f32x4 n1 = mma16(as4(u32x2{kvn[2], kvn[3]}), as4(u32x2{pq[0][2], pq[0][3]}), z4);
    SCHED();
#pragma unroll
    for (int u = 0; u < 16; u++) {
      f32x4 m0 = z4, m1 = z4;
      if (u + 1 < 16) {
        m0 = mma16(as4(u32x2{kvn2[0], kvn2[1]}), as4(u32x2{pq[u + 1][0], pq[u + 1][1]}), z4);
        m1 = mma16(as4(u32x2{kvn2[2], kvn2[3]}), as4(u32x2{pq[u + 1][2], pq[u + 1][3]}), z4);
      }
      if (u + 2 < 16) kvn2 = *(const u32x4*)(lds + (u + 2) * 1024 + lane * 16);
      n0 *= rd[u >> 1][2 * (u & 1)]; n1 *= rd[u >> 1][2 * (u & 1) + 1];
      const u32x4 o = {pk(n0[0], n0[1]), pk(n0[2], n0[3]), pk(n1[0], n1[1]), pk(n1[2], n1[3])};
#pragma unroll
      for (int a = 0; a < 4; a++) {
        acc[a] = mma(wf[u & 3][a], as8(o), acc[a]);
        if (u < 12) wf[u & 3][a] = wfrd(u + 4, a);
      }
      n0 = m0; n1 = m1;
      SCHED();
    }
    TS(9);
    lwait(FL + 48, 4);
    TS(10);
    const float ga = bflo(gv), gb = bfhi(gv);
    unsigned pv[4][2];
#pragma unroll
    for (int a = 0; a < 2; a++) {
      const f32x4 t0 = *(const f32x4*)(lds + L_FFT + ((w * 4 + a) * 64 + lane) * 16);
      const f32x4 t1 = *(const f32x4*)(lds + L_FFT + ((w * 4 + a + 2) * 64 + lane) * 16);
      const f32x4 v0 = acc[a] * ga + (t0 + t1) * gb, v1 = acc[a + 2] * ga + (t0 - t1) * gb;
      pv[a][0] = pk(v0[0], v0[1]); pv[a][1] = pk(v0[2], v0[3]);
      pv[a + 2][0] = pk(v1[0], v1[1]); pv[a + 2][1] = pk(v1[2], v1[3]);
    }
#pragma unroll
    for (int a = 0; a < 2; a++) {
      auto s0 = __builtin_amdgcn_permlane16_swap(pv[2 * a][0], pv[2 * a + 1][0], false, false);
      auto s1 = __builtin_amdgcn_permlane16_swap(pv[2 * a][1], pv[2 * a + 1][1], false, false);
      u32x4 v = {s0[0], s1[0], s0[1], s1[1]};
      char* op = out + (size_t)(16 * stg + i) * 1024 + (256 * a + 32 * ntile + 16 * (g & 1) + 8 * (g >> 1)) * 2;
      asm volatile("global_store_dwordx4 %0, %1, off sc1\n\ts_nop 1" :: "v"(op), "v"(v) : "memory");
    }
    TS(11);
    TSFLUSH();
    return;
  }
  {
    WAITV(0);
    if (b == SPECIAL) lwait(FL + 80, 4);
    const int pos = (col & 1) * 128 + (col >> 1);
    arrive(rs, 50 + w * 4 + (pos >> 6), 64, WS_F3 + (w * 4 + (pos >> 6)) * 128, 1, ep, lane);
    const float sgn = (i & 1) ? -1.f : 1.f;
    const unsigned k0v = (i & 1) ? 0xb5803580u : 0x35803580u;
    const float fn = (float)(32 * ntile + 16 * (w & 1) + i) * (1.f / 512.f);
    const float t2 = 2.f * fcos(fn * 32.f);
    f32x2 c0[4], c1[4];
#pragma unroll
    for (int d = 0; d < 4; d++) {
      const float kf = (float)(4 * g + 128 * (w & 1) + (w >> 1)) + (float)(16 * (d >> 1) + 2 * (d & 1));
      const float a0 = kf * fn, a1 = (kf + 32.f) * fn;
      c0[d] = f32x2{fcos(a0), fsin(a0)} * 0x1p-19f;
      c1[d] = f32x2{fcos(a1), fsin(a1)} * 0x1p-19f;
    }
    u32x4 gf[8];
#pragma unroll
    for (int j = 0; j < 2; j++) {
#pragma unroll
      for (int h = 0; h < 2; h++) {
#pragma unroll
        for (int d = 0; d < 4; d++) {
          gf[4 * j + 2 * h][d] = pk(c0[d][0], c0[d][1]);
          gf[4 * j + 2 * h + 1][d] = pk(c1[d][0], c1[d][1]);
          c0[d] = c1[d] * t2 - c0[d];
        }
#pragma unroll
        for (int d = 0; d < 4; d++) c1[d] = c0[d] * t2 - c1[d];
      }
      gf[4 * j][0] = (w == j && g == 0) ? k0v : gf[4 * j][0];
      if (j == 0) {
        const float sg = (w & 1) ? sgn : 1.f;
#pragma unroll
        for (int d = 0; d < 4; d++) { c0[d] *= sg; c1[d] *= sg; }
      }
    }
#pragma unroll
    for (int k = 0; k < 8; k++) PIN(gf[k]);
    TS(8);
    gwait<SLP>(rs, WS_F3 + ((mtile >> 3) * 4 + w) * 128, ep);
    lwait(FL + 80, 4);
    linc(FL + 96, lane);
    TS(9);
    {
      const unsigned v = (lane >> 3) * YP + ((((lane & 7) >> 1) ^ ((lane >> 4) & 3)) * 32) + (lane & 1) * 16;
      const char* src = ws + WS_YT + (size_t)(w * 128) * YP + mtile * 128;
      const unsigned la = laddr_of(lds) + L_Y + w * 16384;
#pragma unroll
      for (int j = 0; j < 4; j++) glds4c(la + j * 4096, src + (size_t)j * (32 * YP), v, v + (8 * YP - 1024), v + 2 * (8 * YP - 1024), v + 3 * (8 * YP - 1024));
    }
    WAITV(0);
    lset(FL + 32 + w * 4);
    TS(10);
    const int toffB = L_Y + (4 * g + q) * 128 + (fsw * 32) + p * 8;
    f32x4 accb[4];
#pragma unroll
    for (int c = 0; c < 4; c++) accb[c] = f32x4{0, 0, 0, 0};
    s16x8 tb_[2][2][4];
    auto rd = [&](int u) {
      const char* slot = lds + (w ^ (u >> 1)) * 16384 + (u & 1) * 8192;
#pragma unroll
      for (int ks = 0; ks < 2; ks++)
#pragma unroll
        for (int c = 0; c < 4; c++) tb_[u & 1][ks][c] = trd2(slot + (toffB ^ (32 * c)) + ks * 4096, slot + (toffB ^ (32 * c)) + ks * 4096 + 2048);
    };
    auto mm = [&](int u) {
#pragma unroll
      for (int ks = 0; ks < 2; ks++)
#pragma unroll
        for (int c = 0; c < 4; c++) accb[c] = mma(as8(gf[2 * u + ks]), tb_[u & 1][ks][c], accb[c]);
    };
    rd(0);
    SCHED();
#pragma unroll
    for (int u = 1; u < 4; u++) {
      if (u == 2) lwait(FL + 32 + (w ^ 1) * 4, 1);
      rd(u); mm(u - 1);
#pragma unroll
      for (int t = 0; t < 8; t++) { SGB(0x100, 2); SGB(0x008, 1); }
      SCHED();
    }
    mm(3);
    lwait(FL + 96, 4);
    if (b == 0 && w == 1 && lane == 0) stc1(rs, WS_E, ep);
#pragma unroll
    for (int c = 0; c < 4; c++) *(f32x4*)(lds + L_FFT + ((c * 4 + w) * 64 + lane) * 16) = accb[c];
    linc(FL + 48, lane);
    TS(11);
    TSFLUSH();
  }
}

extern "C" __global__ void gm_end() {}

extern "C" void kernel_launch(void* const* d_in, const int* in_sizes, int n_in, void* d_out, int out_size, void* d_ws, size_t ws_size, hipStream_t stream) {
  gm_all<<<256, 512, 0, stream>>>((const char*)d_in[0], (const char*)d_in[1], (const char*)d_in[4], (const char*)d_in[5], (const char*)d_in[6],
                                  (const char*)d_in[2], (char*)d_ws, (char*)d_out);
}
```

```cpp
#define STAMPS
#define PROBE_K 0
#define PROBE_CLS 0
#define PROBE_S 0
#define PROBE_SKIP 255
#include <hip/hip_runtime.h>
#include <stdint.h>

typedef short s16x4 __attribute__((ext_vector_type(4)));
typedef short s16x8 __attribute__((ext_vector_type(8)));
typedef float f32x4 __attribute__((ext_vector_type(4)));
typedef float f32x2 __attribute__((ext_vector_type(2)));
typedef unsigned u32x4 __attribute__((ext_vector_type(4)));
typedef unsigned u32x2 __attribute__((ext_vector_type(2)));
typedef __bf16 bfx2 __attribute__((ext_vector_type(2)));
#define LAS __attribute__((address_space(3)))
#define DI __device__ __forceinline__
#define ROT1 6
#define PITC 8320
#define PITY 4224
#define SLP 2

static constexpr unsigned CP = PITC, YP = PITY;
static constexpr unsigned WS_X1T = 0;
static constexpr unsigned WS_WT  = WS_X1T + (9u << 18);
static constexpr unsigned WS_PQ  = WS_WT + (9u << 18);
static constexpr unsigned WS_KVP = WS_PQ + (2u << 20);
static constexpr unsigned WS_GT  = WS_KVP + (512u << 10);
static constexpr unsigned WS_KVR = WS_GT + (16u << 10);
static constexpr unsigned WS_YT  = WS_KVR + (32u << 10);
static constexpr unsigned WS_E   = WS_YT + (9u << 18);
static constexpr unsigned WS_CNT = WS_E + 4096;
static constexpr unsigned WS_F0  = WS_CNT + 128 * 128;
static constexpr unsigned WS_F1  = WS_F0 + 8 * 128;
static constexpr unsigned WS_F2K = WS_F1 + 8 * 128;
static constexpr unsigned WS_F2Q = WS_F2K + 8 * 128;
static constexpr unsigned WS_F3  = WS_F2Q + 16 * 128;
static constexpr unsigned WS_END = WS_F3 + 16 * 128;
static constexpr int SPECIAL = 255;

static constexpr int L_WO = 0, L_Y = 65536, L_FFT = 131072, L_ATT = 147456, L_FL = 163840 - 256;

DI unsigned pk(float lo, float hi) { f32x2 p = {lo, hi}; return __builtin_bit_cast(unsigned, __builtin_convertvector(p, bfx2)); }
DI float bflo(unsigned u) { return __builtin_bit_cast(float, u << 16); }
DI float bfhi(unsigned u) { return __builtin_bit_cast(float, u & 0xffff0000u); }
DI float rbf(float v) { return bflo(pk(v, 0.f)); }
DI s16x8 as8(u32x4 v) { return __builtin_bit_cast(s16x8, v); }
DI s16x4 as4(u32x2 v) { return __builtin_bit_cast(s16x4, v); }
DI f32x4 mma(s16x8 a, s16x8 b, f32x4 c) { return __builtin_amdgcn_mfma_f32_16x16x32_bf16(a, b, c, 0, 0, 0); }
DI f32x4 mma16(s16x4 a, s16x4 b, f32x4 c) { return __builtin_amdgcn_mfma_f32_16x16x16bf16_1k(a, b, c, 0, 0, 0); }
DI void mfa(f32x4& c, s16x8 a, s16x8 b) { asm volatile("v_mfma_f32_16x16x32_bf16 %0, %1, %2, %0" : "+v"(c) : "v"(a), "v"(b)); }
DI s16x4 trd(const char* l) { return __builtin_amdgcn_ds_read_tr16_b64_v4i16((LAS s16x4*)l); }
DI s16x8 cat(s16x4 a, s16x4 b) { return __builtin_shufflevector(a, b, 0, 1, 2, 3, 4, 5, 6, 7); }
DI s16x8 trd2(const char* l0, const char* l1) { return cat(trd(l0), trd(l1)); }
DI s16x8 lrd(const char* l) { return *(const s16x8*)l; }
DI float fsin(float rev) { return __builtin_amdgcn_sinf(rev); }
DI float fcos(float rev) { return __builtin_amdgcn_cosf(rev); }
DI unsigned laddr_of(const char* l) { return (unsigned)(size_t)(const LAS char*)l; }
DI void glds4(unsigned laddr, const char* sb, unsigned v0, unsigned v1, unsigned v2, unsigned v3) {
  asm volatile("s_mov_b32 m0, %0\n\ts_nop 0\n\t"
               "global_load_lds_dwordx4 %2, %1\n\t"
               "global_load_lds_dwordx4 %3, %1 offset:1024\n\t"
               "global_load_lds_dwordx4 %4, %1 offset:2048\n\t"
               "global_load_lds_dwordx4 %5, %1 offset:3072"
               :: "s"(laddr), "s"(sb), "v"(v0), "v"(v1), "v"(v2), "v"(v3) : "memory");
}
DI void glds4c(unsigned laddr, const char* sb, unsigned v0, unsigned v1, unsigned v2, unsigned v3) {
  asm volatile("s_mov_b32 m0, %0\n\ts_nop 0\n\t"
               "global_load_lds_dwordx4 %2, %1 sc1\n\t"
               "global_load_lds_dwordx4 %3, %1 offset:1024 sc1\n\t"
               "global_load_lds_dwordx4 %4, %1 offset:2048 sc1\n\t"
               "global_load_lds_dwordx4 %5, %1 offset:3072 sc1"
               :: "s"(laddr), "s"(sb), "v"(v0), "v"(v1), "v"(v2), "v"(v3) : "memory");
}
DI int perm32(int t, int i) { return 8 * (i >> 2) + 4 * t + (i & 3); }
DI float phi(float t) {
  float e = __builtin_amdgcn_exp2f(t * 1.44269504088896341f);
  e = __builtin_amdgcn_fmed3f(e, 0.f, 1.f);
  return fmaxf(t + 1e-6f, 1e-6f) + e;
}
struct TsT { unsigned long long r[16]; };
#define TSP TsT& ts_,
#define TSA ts_,
#define TSDECL TsT ts_; for (int q_ = 0; q_ < 16; q_++) ts_.r[q_] = 0;
#define TS(slot) do { ts_.r[slot] = __builtin_amdgcn_s_memrealtime(); } while (0)
#define TSFLUSH() do { const int ii_ = blockIdx.x >> 3; const int cls_ = ii_ >= 24 ? 2 : ((ii_ >> 2) < 4 ? 0 : 1); \
  if ((int)(threadIdx.x >> 8) == PROBE_K && cls_ == PROBE_CLS && blockIdx.x != PROBE_SKIP && ts_.r[PROBE_S] != 0) { const unsigned long long tgt_ = ts_.r[PROBE_S] + 3000ull; \
    for (int sp_ = 0; sp_ < 100000 && __builtin_amdgcn_s_memrealtime() < tgt_; sp_++) __builtin_amdgcn_s_sleep(1); } } while (0)
#define SCHED() __builtin_amdgcn_sched_barrier(0)
#define SGB(mask, n) __builtin_amdgcn_sched_group_barrier(mask, n, 0)
#define PIN(x) asm volatile("" : "+v"(x))
#define WAITV(n) asm volatile("s_waitcnt vmcnt(" #n ")" ::: "memory")
#define BAR() asm volatile("s_waitcnt lgkmcnt(0)\n\ts_barrier" ::: "memory")
#define FENCE() asm volatile("" ::: "memory")

typedef __amdgpu_buffer_rsrc_t rsrc_t;
DI u32x4 ldc(rsrc_t r, unsigned off) { return __builtin_amdgcn_raw_buffer_load_b128(r, off, 0, 16); }
DI void stc(rsrc_t r, unsigned off, u32x4 v) { __builtin_amdgcn_raw_buffer_store_b128(v, r, off, 0, 16); }
DI void stc1(rsrc_t r, unsigned off, unsigned v) { __builtin_amdgcn_raw_buffer_store_b32(v, r, off, 0, 16); }
template <int SL> DI void gwait(rsrc_t r, unsigned off, unsigned ep) {
#pragma unroll 1
  for (int sp = 0; sp < 200000; sp++) {
    FENCE();
    const u32x4 f = ldc(r, off);
    if (__all(f[0] == ep && f[1] == ep && f[2] == ep && f[3] == ep)) break;
    __builtin_amdgcn_s_sleep(SL);
  }
  FENCE();
}
DI unsigned ticket(rsrc_t r, int c, int lane) {
  unsigned old = 0;
  if (lane == 0) old = __builtin_amdgcn_raw_ptr_buffer_atomic_add_i32(1, r, WS_CNT + c * 128, 0, 16);
  old = __builtin_amdgcn_readfirstlane(old);
  FENCE();
  return old;
}
DI void arrive(rsrc_t r, int c, unsigned n, unsigned flag, int nrep, unsigned ep, int lane) {
  if (ticket(r, c, lane) == n - 1 && lane < nrep) stc(r, flag + lane * 128, u32x4{ep, ep, ep, ep});
}
DI void linc(char* p, int lane) { if (lane == 0) asm volatile("ds_add_u32 %0, %1" :: "v"(laddr_of(p)), "v"(1u) : "memory"); }
DI void lset(char* p) { asm volatile("ds_write_b32 %0, %1" :: "v"(laddr_of(p)), "v"(1u) : "memory"); }
DI void lwait(const char* p, unsigned want) {
#pragma unroll 1
  for (int sp = 0; sp < 4000000; sp++) {
    if ((unsigned)__builtin_amdgcn_readfirstlane(*(const volatile LAS unsigned*)p) >= want) break;
    __builtin_amdgcn_s_sleep(1);
  }
  FENCE();
}

DI unsigned load_epoch(const char* p) { unsigned e; asm volatile("s_load_dword %0, %1, 0x0\n\ts_waitcnt lgkmcnt(0)" : "=s"(e) : "s"(p) : "memory"); return e + 1u; }

DI void wo_dma(char* lds, const char* wo, int ntile, int j, int w, int lane) {
  const int blk = ((lane & 7) >> 1) ^ ((lane >> 4) & 3);
  const unsigned v = (lane >> 3) * 1024 + (blk & 1) * 32 + (blk >> 1) * 512 + (lane & 1) * 16;
  glds4(laddr_of(lds) + L_WO + j * 16384 + w * 4096, wo + (size_t)(j * 128 + w * 32) * 1024 + ntile * 64, v, v + 7168, v + 2 * 7168, v + 3 * 7168);
}

DI void p1_prod(char* lds, const char* x, const char* wqkv, const char* wo, rsrc_t rs, int xcd, int m, int col0, int col1, int rot, int ntile, unsigned& ep, const char* epp, int w, int lane) {
  unsigned voffA[4], voffB[4];
#pragma unroll
  for (int j = 0; j < 4; j++) {
    const int par = j & 1;
    voffA[j] = (lane >> 3) * 1024 + (((lane & 7) ^ ((4 * par + (lane >> 4)) & 7)) * 16) + j * (8192 - 1024);
    voffB[j] = (lane >> 3) * 3072 + ((((lane & 7) >> 1) ^ (((lane >> 4) & 1) | (par << 1))) * 32) + (lane & 1) * 16 + j * (24576 - 1024);
  }
  const unsigned lbase = laddr_of(lds) + w * 4096;
  const char* xa = x + (size_t)(m * 128 + w * 32) * 1024;
  const char* wb = wqkv + (size_t)((w & 1) * 32) * 3072 + ((w >> 1) ? col1 : col0) * 2;
  auto issue = [&](int idx) {
    const int ks = (idx + rot) & 7;
    const unsigned slot = lbase + (idx & 3) * 32768;
    glds4(slot, xa + ks * 128, voffA[0], voffA[1], voffA[2], voffA[3]);
    glds4(slot + 16384, wb + (size_t)(ks * 64) * 3072, voffB[0], voffB[1], voffB[2], voffB[3]);
  };
  issue(0); issue(1);
  WAITV(8); BAR();
  issue(2); issue(3);
  ep = load_epoch(epp);
#pragma unroll 1
  for (int j = 1; j < 5; j++) { WAITV(16); BAR(); issue(j + 3); }
  WAITV(16); BAR();
  wo_dma(lds, wo, ntile, 0, w, lane);
  WAITV(12); BAR();
  wo_dma(lds, wo, ntile, 1, w, lane);
  WAITV(8); BAR();
  if (w == 0) { gwait<SLP>(rs, WS_F0 + xcd * 128, ep); lset(lds + L_FL + 100); }
  wo_dma(lds, wo, ntile, 2, w, lane);
  wo_dma(lds, wo, ntile, 3, w, lane);
}

template <bool QT>
DI void p1_gemm(TSP char* lds, rsrc_t rs, int m, int nt, unsigned& ep, const char* epp, int w, int lane) {
  const int i = lane & 15, g = lane >> 4, q = i >> 2, p = lane & 3;
  f32x4 acc[8][2];
#pragma unroll
  for (int a = 0; a < 8; a++) { acc[a][0] = f32x4{0, 0, 0, 0}; acc[a][1] = f32x4{0, 0, 0, 0}; }
  const int aoff = i * 128 + ((g ^ (i >> 1)) * 16);
  const int boff = 16384 + (8 * g + q) * 128 + ((w ^ (((q >> 1) & 1) | ((g & 1) << 1))) * 32) + p * 8;
  s16x8 A[2][2][8]; s16x4 B[2][2][2][2];
  auto rd1 = [&](int buf, const char* slot, int r) {
    const int ks = r / 12, rr = r % 12;
    const int t = rr == 0 ? 0 : rr == 1 ? 1 : rr == 3 ? 2 : rr == 4 ? 3 : -1;
    const int a = rr == 2 ? 0 : rr - 4;
    if (t >= 0) B[buf][ks][t >> 1][t & 1] = trd(slot + boff + (t >> 1) * 8192 + ks * 4096 + (t & 1) * 512);
    else A[buf][ks][a] = lrd(slot + (aoff ^ (ks * 64)) + a * 2048);
  };
  auto mf1 = [&](int buf, int ks, int mt, int hh) {
    const s16x8 bf = cat(B[buf][ks][hh][0], B[buf][ks][hh][1]);
    if (QT) mfa(acc[mt][hh], bf, A[buf][ks][mt]); else mfa(acc[mt][hh], A[buf][ks][mt], bf);
  };
  auto region = [&](int cur, const char* slot) {
#pragma unroll
    for (int mi = 0; mi < 32; mi++) {
      mf1(cur, mi >> 4, (mi & 15) >> 1, mi & 1);
      SCHED();
      if (mi < 24) { rd1(cur ^ 1, slot, mi); SCHED(); }
    }
  };
  ep = load_epoch(epp);
  const bool zw = !QT && blockIdx.x == 16 && w == 3;
  if (zw) { stc1(rs, WS_CNT + lane * 128, 0u); stc1(rs, WS_CNT + (64 + lane) * 128, 0u); }
  __builtin_amdgcn_s_waitcnt(0xC07F);
  BAR();
  TS(1);
#pragma unroll
  for (int r = 0; r < 24; r++) rd1(0, lds, r);
  SCHED();
#pragma unroll 1
  for (int it = 0; it < 3; it++) {
    BAR(); SCHED();
    region(0, lds + ((2 * it + 1) & 3) * 32768);
    BAR(); SCHED();
    region(1, lds + ((2 * it + 2) & 3) * 32768);
    if (zw && it == 0) {
      WAITV(0);
      if (lane < 8) stc(rs, WS_F0 + lane * 128, u32x4{ep, ep, ep, ep});
    }
  }
  BAR(); SCHED();
  region(0, lds + 3 * 32768);
  asm volatile("s_nop 7");
  SCHED();
  auto mmh = [&](int hh) {
#pragma unroll
    for (int mt = 0; mt < 8; mt++)
#pragma unroll
      for (int ks = 0; ks < 2; ks++) {
        const s16x8 bf = cat(B[1][ks][hh][0], B[1][ks][hh][1]);
        acc[mt][hh] = QT ? mma(bf, A[1][ks][mt], acc[mt][hh]) : mma(A[1][ks][mt], bf, acc[mt][hh]);
      }
  };
  mmh(0);
  SCHED();
  TS(2);
  if (QT) {
    mmh(1);
#pragma unroll
    for (int mt = 0; mt < 8; mt++) {
      u32x4 v = {pk(phi(acc[mt][0][0]), phi(acc[mt][0][1])), pk(phi(acc[mt][0][2]), phi(acc[mt][0][3])),
                 pk(phi(acc[mt][1][0]), phi(acc[mt][1][1])), pk(phi(acc[mt][1][2]), phi(acc[mt][1][3]))};
      stc(rs, WS_PQ + (((m * 8 + mt) * 16 + (nt - 8) * 4 + w) * 64 + lane) * 16, v);
    }
    lwait(lds + L_FL + 100, 1);
    WAITV(0);
    arrive(rs, 34 + m, 16, WS_F2Q + m * 128, 1, ep, lane);
  } else {
    mmh(1);
    u32x4 pa[4];
#pragma unroll
    for (int u = 0; u < 4; u++)
      pa[u] = u32x4{pk(phi(acc[2 * u][0][0]), phi(acc[2 * u][0][1])), pk(phi(acc[2 * u][0][2]), phi(acc[2 * u][0][3])),
                    pk(phi(acc[2 * u + 1][0][0]), phi(acc[2 * u + 1][0][1])), pk(phi(acc[2 * u + 1][0][2]), phi(acc[2 * u + 1][0][3]))};
#pragma unroll
    for (int t = 0; t < 16; t++) { SGB(0x008, 1); SGB(0x002, 2); }
    SCHED();
    f32x4 kv = {0, 0, 0, 0}, z = {0, 0, 0, 0};
    const u32x4 ones = {0x3f803f80u, 0x3f803f80u, 0x3f803f80u, 0x3f803f80u};
#pragma unroll
    for (int u = 0; u < 4; u++) {
      u32x4 b = {pk(acc[2 * u][1][0], acc[2 * u][1][1]), pk(acc[2 * u][1][2], acc[2 * u][1][3]),
                 pk(acc[2 * u + 1][1][0], acc[2 * u + 1][1][1]), pk(acc[2 * u + 1][1][2], acc[2 * u + 1][1][3])};
      z = mma(as8(pa[u]), as8(ones), z);
      kv = mma(as8(pa[u]), as8(b), kv);
    }
    const int head = nt * 4 + w;
    u32x4 v = {pk(kv[0], kv[1]), pk(kv[2], kv[3]), pk(z[0], z[1]), pk(z[2], z[3])};
    stc(rs, WS_KVP + ((m * 32 + head) * 64 + lane) * 16, v);
    lwait(lds + L_FL + 100, 1);
    WAITV(0);
    if (ticket(rs, 1 + head, lane) == 15) {
      TS(4);
      u32x4 kl[16];
#pragma unroll
      for (int pp = 0; pp < 16; pp++) kl[pp] = ldc(rs, WS_KVP + ((pp * 32 + head) * 64 + lane) * 16);
      u32x4 r;
#pragma unroll
      for (int d = 0; d < 4; d++) {
        float lo[4], hi[4];
#pragma unroll
        for (int j = 0; j < 4; j++) {
          lo[j] = (bflo(kl[4 * j][d]) + bflo(kl[4 * j + 1][d])) + (bflo(kl[4 * j + 2][d]) + bflo(kl[4 * j + 3][d]));
          hi[j] = (bfhi(kl[4 * j][d]) + bfhi(kl[4 * j + 1][d])) + (bfhi(kl[4 * j + 2][d]) + bfhi(kl[4 * j + 3][d]));
        }
        r[d] = pk((lo[0] + lo[1]) + (lo[2] + lo[3]), (hi[0] + hi[1]) + (hi[2] + hi[3]));
      }
      const int u = (head >> 3) * 4 + (head & 3), hh = (head >> 2) & 1;
      const bool zr = (i & 3) == hh + 2 * (u & 1);
      __builtin_amdgcn_raw_buffer_store_b64(u32x2{r[0], r[1]}, rs, WS_KVR + (u * 64 + lane) * 16 + hh * 8, 0, 16);
      if (zr) stc(rs, WS_KVR + 16384 + ((u >> 1) * 64 + lane) * 16, hh ? u32x4{0u, 0u, r[2], r[3]} : u32x4{r[2], r[3], 0u, 0u});
      TS(5);
      WAITV(0);
      arrive(rs, 33, 32, WS_F2K, 8, ep, lane);
      TS(6);
    }
  }
}

DI float dpp_sum16(float v) {
  v += __builtin_bit_cast(float, __builtin_amdgcn_update_dpp(0, __builtin_bit_cast(int, v), 0x128, 0xf, 0xf, false));
  v += __builtin_bit_cast(float, __builtin_amdgcn_update_dpp(0, __builtin_bit_cast(int, v), 0x124, 0xf, 0xf, false));
  v += __builtin_bit_cast(float, __builtin_amdgcn_update_dpp(0, __builtin_bit_cast(int, v), 0x122, 0xf, 0xf, false));
  v += __builtin_bit_cast(float, __builtin_amdgcn_update_dpp(0, __builtin_bit_cast(int, v), 0x121, 0xf, 0xf, false));
  return v;
}

DI void p1_rows(TSP char* lds, const char* x, const char* gw, u32x4& gwv, rsrc_t rs, int a, unsigned& ep, const char* epp, int w, int lane) {
  const int i = lane & 15, g = lane >> 4;
  char* LX = lds;
  char* LS = lds + 32768;
  const char* LC = lds + 106496;
  const int amt = (a >> 1) & 1;
  {
    const unsigned v = lane * 16;
    const char* src = x + (size_t)(128 * (a >> 2) + perm32(amt, 8 * (a & 1) + w)) * 1024;
    glds4(laddr_of(LX) + w * 4096, src, v, v + 31 * 1024, v + 62 * 1024, v + 93 * 1024);
    glds4(laddr_of(LX) + w * 4096 + 16384, src + 8192, v, v + 31 * 1024, v + 62 * 1024, v + 93 * 1024);
  }
  int soff[4];
#pragma unroll
  for (int r = 0; r < 4; r++) {
    const int k2 = 4 * g + r;
    const int k = i + 32 * k2;
    const int col = (i == 0) ? 16 * k2 : (k2 < 8 ? k : 512 - k);
    soff[r] = col * 128 + ((w ^ (col & 7)) * 16);
    PIN(soff[r]);
  }
  TS(12);
  BAR();
  TS(13);
  const u32x4 b1re = *(const u32x4*)(LC + lane * 16), b1im = *(const u32x4*)(LC + 1024 + lane * 16);
  const u32x4 a3re = *(const u32x4*)(LC + 2048 + lane * 16), a3im = *(const u32x4*)(LC + 3072 + lane * 16);
  const u32x4 a4re = *(const u32x4*)(LC + 4096 + lane * 16), a4im = *(const u32x4*)(LC + 5120 + lane * 16);
  const f32x4 twc = *(const f32x4*)(LC + 6144 + lane * 16), tws = *(const f32x4*)(LC + 7168 + lane * 16);
  ep = load_epoch(epp);
  WAITV(0);
  TS(1);
#pragma unroll 1
  for (int hf = 0; hf < 2; hf++) {
    unsigned o[4][4];
#pragma unroll
    for (int a4 = 0; a4 < 4; a4++) {
      const char* row = LX + ((w + 4 * hf) * 4 + a4) * 1024;
      s16x8 xf = trd2(row + lane * 8, row + 512 + lane * 8);
      const f32x4 z4 = {0, 0, 0, 0};
      f32x4 are = mma(xf, as8(b1re), z4), aim = mma(xf, as8(b1im), z4);
      f32x4 tr = are * twc + aim * tws, ti = aim * twc - are * tws;
      asm("" : "+v"(tr), "+v"(ti));
      u32x4 bb;
#pragma unroll
      for (int r = 0; r < 4; r++) bb[r] = pk(tr[r], ti[r]);
      f32x4 xre = mma(as8(a3re), as8(bb), z4), xim = mma(as8(a3im), as8(bb), z4);
      f32x4 sre = mma(as8(a4re), as8(bb), z4), sim = mma(as8(a4im), as8(bb), z4);
#pragma unroll
      for (int r = 0; r < 4; r++) o[r][a4] = pk((i == 0) ? sre[r] : xre[r], (i == 0) ? sim[r] : xim[r]);
    }
    if (lane == 0) {
      u32x4 v = {o[0][0] >> 16, o[0][1] >> 16, o[0][2] >> 16, o[0][3] >> 16};
      *(u32x4*)(LS + 256 * 128 + (w + 4 * hf) * 16) = v;
#pragma unroll
      for (int a4 = 0; a4 < 4; a4++) o[0][a4] &= 0xffffu;
    }
#pragma unroll
    for (int r = 0; r < 4; r++) {
      u32x4 v = {o[r][0], o[r][1], o[r][2], o[r][3]};
      *(u32x4*)(LS + (soff[r] ^ (hf * 64))) = v;
    }
  }
  TS(14);
  linc(lds + L_FL + 108, lane);
  gwv = *(const u32x4*)(gw + lane * 16);
  lwait(lds + L_FL + 108, 4);
  TS(2);
  const int sl = lane & 7;
  const int sc = w * 8 + (lane >> 3);
  const int xline = (amt * 4 + (a >> 4)) * 1024 + ((a >> 2) & 3) * 256 + (a & 1) * 128;
  u32x4 v0[8];
#pragma unroll
  for (int j = 0; j < 8; j++) v0[j] = *(const u32x4*)(LS + (j * 32 + sc) * 128 + ((sl ^ (sc & 7)) * 16));
#pragma unroll
  for (int j = 0; j < 8; j++) stc(rs, WS_X1T + (j * 32 + sc) * CP + xline + sl * 16, v0[j]);
  if (w == 3 && lane < 8) stc(rs, WS_X1T + 256 * CP + xline + lane * 16, *(const u32x4*)(LS + 256 * 128 + lane * 16));
  TS(15);
}

DI void p1_filt(TSP char* lds, const char* wr, const char* wi, rsrc_t rs, int a, unsigned& ep, const char* epp, int w, int lane) {
  const int i = lane & 15, g = lane >> 4;
  char* LF = lds + 69632;
  {
    const float fi = (float)i;
    char* cp = lds + 106496 + lane * 16 + w * 4;
    float c[2], s[2];
#pragma unroll
    for (int e = 0; e < 2; e++) {
      const float ang = (float)(4 * g + 16 * (w >> 1) + 2 * (w & 1) + e) * (fi * (1.f / 32.f));
      c[e] = fcos(ang);
      s[e] = (i == 0) ? (e ? -1.f : 1.f) : fsin(-ang);
    }
    *(unsigned*)(cp) = pk(c[0], c[1]);
    *(unsigned*)(cp + 1024) = pk(s[0], s[1]);
    const float n2i = (float)(4 * g + w) * fi;
    {
      const float ang = n2i * (1.f / 16.f);
      const float cs = fcos(ang), sn = fsin(ang);
      *(unsigned*)(cp + 2048) = pk(cs, sn);
      *(unsigned*)(cp + 3072) = pk(sn, cs) ^ ((i >= 8) ? 0x80000000u : 0x00008000u);
    }
    {
      const float ang = n2i * (1.f / 32.f);
      const float cs = fcos(ang);
      const float sn = (i == 0) ? ((w & 1) ? -1.f : 1.f) : fsin(-ang);
      const unsigned u = pk(cs, sn);
      const int sh = (i & 1) * 16;
      *(unsigned*)(cp + 4096) = (u & 0xffffu) << sh;
      *(unsigned*)(cp + 5120) = (u >> 16) << sh;
    }
    *(float*)(cp + 6144) = fcos(n2i * (1.f / 512.f));
    *(float*)(cp + 7168) = fsin(n2i * (1.f / 512.f));
  }
  TS(12);
  BAR();
  {
    const unsigned v = lane * 16;
    const size_t off = (size_t)(8 * (a & 7) + 64 * (4 * (a >> 3) + w)) * 514;
    glds4(laddr_of(LF) + w * 4352, wr + off, v, v, v, v);
    glds4(laddr_of(LF) + (w + 4) * 4352, wi + off, v, v, v, v);
    if (lane == 0) {
      asm volatile("s_mov_b32 m0, %0\n\ts_nop 0\n\tglobal_load_lds_dwordx4 %2, %1" :: "s"(laddr_of(LF) + w * 4352 + 4096), "s"(wr + off + 4096), "v"(v) : "memory");
      asm volatile("s_mov_b32 m0, %0\n\ts_nop 0\n\tglobal_load_lds_dwordx4 %2, %1" :: "s"(laddr_of(LF) + (w + 4) * 4352 + 4096), "s"(wi + off + 4096), "v"(v) : "memory");
    }
  }
  ep = load_epoch(epp);
  WAITV(0);
  linc(lds + L_FL + 112, lane);
  lwait(lds + L_FL + 112, 4);
  TS(1);
  const int sl = lane & 7;
  const int wline = ((((a & 7) >> 1) * 2 + ((a >> 3) & 1)) * 64 + (a >> 4) * 16 + (a & 1) * 8) * 16;
#pragma unroll 1
  for (int hf = 0; hf < 2; hf++) {
    const int cb = (lane >> 3) * 16 + w + 128 * hf;
    const char* fp = LF + sl * 514 + cb * 2;
#pragma unroll
    for (int j = 0; j < 4; j++) {
      u32x4 fv;
#pragma unroll
      for (int r = 0; r < 4; r++) fv[r] = (unsigned)*(const unsigned short*)(fp + r * 4352 + j * 8) | ((unsigned)*(const unsigned short*)(fp + (4 + r) * 4352 + j * 8) << 16);
      stc(rs, WS_WT + (cb + 4 * j) * CP + wline + sl * 16, fv);
    }
  }
  if (w == 3 && lane < 8) {
    u32x4 v;
#pragma unroll
    for (int r = 0; r < 4; r++) v[r] = (unsigned)*(const unsigned short*)(LF + sl * 514 + 512 + r * 4352) | ((unsigned)*(const unsigned short*)(LF + sl * 514 + 512 + (4 + r) * 4352) << 16);
    stc(rs, WS_WT + 256 * CP + wline + sl * 16, v);
  }
}
DI void p1_aux_finish(char* lds, rsrc_t rs, int xcd, unsigned ep, int w8, int lane) {
  if (w8 == 4) gwait<SLP>(rs, WS_F0 + xcd * 128, ep);
  WAITV(0);
  BAR();
  if (w8 == 1) arrive(rs, 0, 64, WS_F1, 8, ep, lane);
}

DI void p1_gate(char* lds, u32x4 gwv, rsrc_t rs, int a, int w, int lane) {
  const int i = lane & 15, g = lane >> 4;
  const u32x4 ones = {0x3f803f80u, 0x3f803f80u, 0x3f803f80u, 0x3f803f80u};
#pragma unroll 1
  for (int hf = 0; hf < 2; hf++) {
    float gsum[4];
#pragma unroll
    for (int j = 0; j < 4; j++) {
      u32x4 xv = *(const u32x4*)(lds + ((w + 4 * hf) * 4 + j) * 1024 + lane * 16);
      u32x4 pr;
#pragma unroll
      for (int d = 0; d < 4; d++) pr[d] = pk(bflo(xv[d]) * bflo(gwv[d]), bfhi(xv[d]) * bfhi(gwv[d]));
      gsum[j] = mma(as8(ones), as8(pr), f32x4{0, 0, 0, 0})[0];
    }
    float gs = (g == 0) ? gsum[0] : (g == 1) ? gsum[1] : (g == 2) ? gsum[2] : gsum[3];
    gs = rbf(dpp_sum16(gs));
    const float gg = rbf(__builtin_amdgcn_rcpf(1.f + __builtin_amdgcn_exp2f(gs * -1.44269504088896341f)));
    const int s = 32 * (4 * (a >> 2) + g) + perm32((a >> 1) & 1, 8 * (a & 1) + w + 4 * hf);
    if (i == 0) stc1(rs, WS_GT + (s >> 6) * 256 + (s & 15) * 16 + ((s >> 4) & 3) * 4, pk(gg, 1.f - gg));
  }
}

template <class F> DI void p2_fft(TSP F idle, char* lb, char* fl, char* f1seen, const char* ws, rsrc_t rs, int c, int rep, unsigned ep, int w, int lane) {
  const int i = lane & 15, g = lane >> 4;
  const int k1 = 16 * w + i;
  const float fk1 = (float)k1, fg = (float)g;
  u32x4 fre[4], fim[4], fc[2], fs[2], fn[2];
  {
    const float st = fk1 * (1.f / 64.f);
    const float a0 = fg * 4.f * st;
    const bool odd = i & 1;
    const unsigned m2 = odd ? 0x80008000u : 0u;
    const unsigned mre = (i & 2) ? 0x80008000u : 0u;
    const unsigned mim = ((i + 1) & 2) ? 0x80008000u : 0u;
#pragma unroll
    for (int d = 0; d < 4; d++) {
      const float ang = __builtin_fmaf((float)d, st, a0);
      const float cs = fcos(ang), sn = fsin(ang);
      const unsigned r0 = pk(cs, sn), i0 = pk(sn, cs) ^ 0x8000u;
      const unsigned r1 = (odd ? i0 : r0) ^ mre, i1 = (odd ? r0 : i0) ^ mim;
      fre[0][d] = r0; fim[0][d] = i0;
      fre[1][d] = r1; fim[1][d] = i1;
      fre[2][d] = r0 ^ m2; fim[2][d] = i0 ^ m2;
      fre[3][d] = r1 ^ m2; fim[3][d] = i1 ^ m2;
    }
#pragma unroll
    for (int ks = 0; ks < 4; ks++) { PIN(fre[ks]); PIN(fim[ks]); }
  }
  float c1[2][4], s1[2][4], c2[2][4], s2[2][4];
#pragma unroll
  for (int mt = 0; mt < 2; mt++) {
    const float fa = (float)perm32(mt, i);
    const float st = fa * (1.f / 32.f), a0 = fg * 8.f * st;
    const float st1 = fk1 * (1.f / 2048.f), b0 = (fg * 8.f + (float)(4 * mt)) * st1;
    const float st2 = fa * (1.f / 2048.f), d0 = (fg * 4.f + (float)(16 * w)) * st2;
#pragma unroll
    for (int d = 0; d < 4; d++) {
      const float x0 = __builtin_fmaf((float)(2 * d), st, a0), x1 = __builtin_fmaf((float)(2 * d + 1), st, a0);
      fc[mt][d] = pk(fcos(x0), fcos(x1));
      fs[mt][d] = pk(fsin(x0), fsin(x1));
      fn[mt][d] = fs[mt][d] ^ 0x80008000u;
      const float t1 = __builtin_fmaf((float)d, st1, b0);
      c1[mt][d] = fcos(t1); s1[mt][d] = fsin(t1);
      const float t2 = __builtin_fmaf((float)d, st2, d0);
      c2[mt][d] = fcos(t2); s2[mt][d] = fsin(t2);
      PIN(c1[mt][d]); PIN(s1[mt][d]); PIN(c2[mt][d]); PIN(s2[mt][d]);
    }
    PIN(fc[mt]); PIN(fs[mt]); PIN(fn[mt]);
  }
  idle();
  TS(4);
  if (w == 0) { gwait<SLP>(rs, WS_F1 + rep * 128, ep); lset(f1seen); }
  else lwait(f1seen, 1);
  TS(5);
  asm volatile("s_mov_b32 m0, %0\n\ts_nop 0\n\tglobal_load_lds_dwordx4 %2, %1 sc1\n\tglobal_load_lds_dwordx4 %2, %1 offset:1024 sc1"
               :: "s"(laddr_of(lb) + 8192 + w * 2048), "s"(ws + WS_X1T + (size_t)c * CP + w * 2048), "v"(lane * 16) : "memory");
  u32x4 wf[2];
#pragma unroll
  for (int mt = 0; mt < 2; mt++) wf[mt] = ldc(rs, WS_WT + c * CP + ((w * 2 + mt) * 64 + lane) * 16);
  WAITV(2);
  linc(fl, lane);
  lwait(fl, 4);
  TS(6);
  const f32x4 z4 = {0, 0, 0, 0};
  f32x4 re[2], im[2];
#pragma unroll
  for (int mt = 0; mt < 2; mt++) { re[mt] = z4; im[mt] = z4; }
#pragma unroll
  for (int ks = 0; ks < 4; ks++)
#pragma unroll
    for (int mt = 0; mt < 2; mt++) {
      s16x8 xa = lrd(lb + 8192 + ((mt * 4 + ks) * 64 + lane) * 16);
      re[mt] = mma(xa, as8(fre[ks]), re[mt]); im[mt] = mma(xa, as8(fim[ks]), im[mt]);
    }
  u32x4 pr, pi;
#pragma unroll
  for (int mt = 0; mt < 2; mt++) {
    float tr[4], ti[4];
#pragma unroll
    for (int r = 0; r < 4; r++) { tr[r] = re[mt][r] * c1[mt][r] + im[mt][r] * s1[mt][r]; ti[r] = im[mt][r] * c1[mt][r] - re[mt][r] * s1[mt][r]; }
    pr[2 * mt] = pk(tr[0], tr[1]); pr[2 * mt + 1] = pk(tr[2], tr[3]);
    pi[2 * mt] = pk(ti[0], ti[1]); pi[2 * mt + 1] = pk(ti[2], ti[3]);
  }
#pragma unroll
  for (int mt = 0; mt < 2; mt++) {
    re[mt] = mma(as8(fs[mt]), as8(pi), mma(as8(fc[mt]), as8(pr), z4));
    im[mt] = mma(as8(fn[mt]), as8(pr), mma(as8(fc[mt]), as8(pi), z4));
  }
#pragma unroll
  for (int mt = 0; mt < 2; mt++) {
    float tr[4], ti[4];
#pragma unroll
    for (int r = 0; r < 4; r++) {
      const float a = bflo(wf[mt][r]), b = bfhi(wf[mt][r]);
      tr[r] = re[mt][r] * a - im[mt][r] * b; ti[r] = re[mt][r] * b + im[mt][r] * a;
    }
    pr[2 * mt] = pk(tr[0], tr[1]); pr[2 * mt + 1] = pk(tr[2], tr[3]);
    pi[2 * mt] = pk(ti[0], ti[1]); pi[2 * mt + 1] = pk(ti[2], ti[3]);
  }
#pragma unroll
  for (int nt = 0; nt < 2; nt++) {
    re[nt] = mma(as8(pi), as8(fn[nt]), mma(as8(pr), as8(fc[nt]), z4));
    im[nt] = mma(as8(pi), as8(fc[nt]), mma(as8(pr), as8(fs[nt]), z4));
  }
#pragma unroll
  for (int nt = 0; nt < 2; nt++) {
    u32x4 v;
#pragma unroll
    for (int r = 0; r < 4; r++) v[r] = pk(re[nt][r] * c2[nt][r] - im[nt][r] * s2[nt][r], -re[nt][r] * s2[nt][r] - im[nt][r] * c2[nt][r]);
    *(u32x4*)(lb + ((nt * 4 + w) * 64 + lane) * 16) = v;
  }
  linc(fl + 4, lane);
  lwait(fl + 4, 4);
#pragma unroll
  for (int mt = 0; mt < 2; mt++) { re[mt] = z4; im[mt] = z4; }
#pragma unroll
  for (int ks = 0; ks < 4; ks++)
#pragma unroll
    for (int mt = 0; mt < 2; mt++) {
      s16x8 ya = lrd(lb + ((mt * 4 + ks) * 64 + lane) * 16);
      re[mt] = mma(ya, as8(fre[ks]), re[mt]); im[mt] = mma(ya, as8(fim[ks]), im[mt]);
    }
  u32x4 vr = {pk(re[0][0], re[0][1]), pk(re[0][2], re[0][3]), pk(re[1][0], re[1][1]), pk(re[1][2], re[1][3])};
  u32x4 vi = {pk(im[0][0], im[0][1]), pk(im[0][2], im[0][3]), pk(im[1][0], im[1][1]), pk(im[1][2], im[1][3])};
  const unsigned yt = WS_YT + (32 * k1 + 8 * g) * 2;
  const int rr = (c == 256) ? 1 : (c & 1) * 256 + (c & ~1);
  stc(rs, yt + rr * YP, vr);
  if (c & 255) stc(rs, yt + (rr + 1) * YP, vi);
  TS(7);
}

extern "C" __global__ __launch_bounds__(512, 2) void gm_all(const char* x, const char* wqkv, const char* wr, const char* wi, const char* gw, const char* wo, char* ws, char* out) {
  __shared__ __attribute__((aligned(16))) char lds[163840];
  const int b = blockIdx.x, xcd = b & 7, ii = b >> 3;
  const int w8 = __builtin_amdgcn_readfirstlane(threadIdx.x >> 6), lane = threadIdx.x & 63;
  const int w = w8 & 3;
  TSDECL
  TS(0);
  if (ii < 2 && w8 < 4) {
    unsigned long long pc; int o0, o1;
    asm volatile("s_getpc_b64 %0\n.Lgm_pc%=:\n\ts_mov_b32 %1, gm_all-.Lgm_pc%=\n\ts_mov_b32 %2, gm_end-.Lgm_pc%=" : "=s"(pc), "=s"(o0), "=s"(o1));
    const unsigned off = (ii * 256 + threadIdx.x) * 128;
    if (off < (unsigned)(o1 - o0))
      asm volatile("s_mov_b32 m0, %0\n\ts_nop 0\n\tglobal_load_lds_dword %2, %1" :: "s"(laddr_of(lds) + L_ATT + 8192), "s"(pc + (long long)o0), "v"(off) : "memory");
  }
  unsigned ep;
  const char* epp = ws + WS_E;
  const rsrc_t rs = __builtin_amdgcn_make_buffer_rsrc(ws, 0, WS_END, 0x00020000);
  char* FL = lds + L_FL;
  const int ml = ii & 3, nl = ii >> 2;
  const int mtile = xcd * 4 + ml, ntile = nl;
  const bool aux = ii >= 24;
  {
    const int m = (xcd >> 1) * 4 + ml;
    const int nt = nl < 4 ? (xcd & 1) * 4 + nl : 8 + (xcd & 1) * 2 + (nl - 4);
    if (w8 < 4 || aux) {
      if (w8 == 0) *(LAS unsigned*)(FL + lane * 4) = 0;
      if (w8 >= 4) p1_filt(TSA lds, wr, wi, rs, xcd * 8 + (ii - 24), ep, epp, w, lane);
      else {
        __builtin_amdgcn_s_setprio(2);
        if (aux) {
          u32x4 gwv;
          p1_rows(TSA lds, x, gw, gwv, rs, xcd * 8 + (ii - 24), ep, epp, w, lane);
          p1_gate(lds, gwv, rs, xcd * 8 + (ii - 24), w, lane);
          p1_aux_finish(lds, rs, xcd, ep, w8, lane);
#pragma unroll 1
          for (int j = 0; j < 4; j++) wo_dma(lds, wo, ntile, j, w, lane);
        }
        else if (nl < 4) p1_gemm<false>(TSA lds, rs, m, nt, ep, epp, w, lane);
        else p1_gemm<true>(TSA lds, rs, m, nt, ep, epp, w, lane);
        __builtin_amdgcn_s_setprio(0);
      }
    } else {
      const int col0 = nl >= 4 ? (nt - 8) * 128 : 512 + nt * 64;
      const int col1 = nl >= 4 ? col0 + 64 : col0 + 512;
      p1_prod(lds, x, wqkv, wo, rs, xcd, m, col0, col1, (2 * ml + nl) & ROT1, ntile, ep, epp, w, lane);
    }
  }
  TS(3);
  const int i = lane & 15, g = lane >> 4, q = i >> 2, p = lane & 3;
  const int fsw = (2 * g + (q >> 1)) & 3;
  const int col = (b + 1) & 255;
  if (w8 >= 4 || b == SPECIAL) __builtin_amdgcn_s_setprio(1);
  if (w8 >= 4 || b == SPECIAL) {
    const bool pg = w8 >= 4;
    p2_fft(TSA [&]() { if (pg) { if (aux) p1_aux_finish(lds, rs, xcd, ep, w8, lane); else { WAITV(0); linc(FL, lane); } } }, pg ? lds + L_FFT : lds + L_Y, pg ? FL + 4 : FL + 20, pg ? FL + 16 : FL + 28, ws, rs, pg ? col : 256, xcd, ep, w, lane);
  }
  if (w8 < 4) {
    WAITV(0);
    linc(FL + 80, lane);
    if (aux) linc(FL, lane);
    TS(7);
    const int stg = 4 * mtile + w;
    const int toffA = L_WO + (4 * g + q) * 128 + (fsw * 32) + p * 8;
    auto wfrd = [&](int u, int a) { const char* pp = lds + (toffA ^ (32 * a)) + (u >> 2) * 16384 + (u & 3) * 2048; return trd2(pp, pp + 8192); };
    if (w == 0) { gwait<SLP>(rs, WS_F2Q + (mtile >> 1) * 128, ep); lset(FL + 104); }
    lwait(FL, 4);
    s16x8 wf[4][4];
#pragma unroll
    for (int u = 0; u < 4; u++)
#pragma unroll
      for (int a = 0; a < 4; a++) wf[u][a] = wfrd(u, a);
    linc(FL + 12, lane);
    lwait(FL + 104, 1);
    u32x4 pq[16];
#pragma unroll
    for (int u = 0; u < 16; u++) pq[u] = ldc(rs, WS_PQ + ((stg * 16 + u) * 64 + lane) * 16);
#pragma unroll
    for (int u = 0; u < 4; u++)
#pragma unroll
      for (int a = 0; a < 4; a++) PIN(wf[u][a]);
    lwait(FL + 12, 4);
    if (w == 0) { gwait<SLP>(rs, WS_F2K + xcd * 128, ep); lset(FL + 88); }
    else lwait(FL + 88, 1);
    TS(8);
    asm volatile("s_mov_b32 m0, %0\n\ts_nop 0\n\tglobal_load_lds_dwordx4 %2, %1 sc1\n\tglobal_load_lds_dwordx4 %2, %1 offset:1024 sc1"
                 :: "s"(laddr_of(lds) + L_ATT + w * 2048), "s"(ws + WS_KVR + 16384 + w * 2048), "v"(lane * 16) : "memory");
    glds4c(laddr_of(lds) + w * 4096, ws + WS_KVR + w * 4096, lane * 16, lane * 16, lane * 16, lane * 16);
    WAITV(0);
    linc(FL + 92, lane);
    lwait(FL + 16, 1);
    const unsigned gv = __builtin_amdgcn_raw_buffer_load_b32(rs, WS_GT + mtile * 256 + i * 16 + w * 4, 0, 16);
    lwait(FL + 92, 4);
    TS(13);
    f32x4 acc[4];
#pragma unroll
    for (int a = 0; a < 4; a++) acc[a] = f32x4{0, 0, 0, 0};
    const f32x4 z4 = {0, 0, 0, 0};
    __builtin_amdgcn_s_setprio(2);
    f32x4 rd[8];
#pragma unroll
    for (int v = 0; v < 8; v++) {
      const f32x4 eps = {1e-6f, 1e-6f, 1e-6f, 1e-6f};
      const s16x8 zv = lrd(lds + L_ATT + v * 1024 + lane * 16);
      const f32x4 d0 = mma(zv, as8(pq[2 * v]), eps), d1 = mma(zv, as8(pq[2 * v + 1]), eps);
      rd[v] = f32x4{d0[0], d0[1], d1[2], d1[3]};
    }
    u32x4 kvn = *(const u32x4*)(lds + lane * 16), kvn2 = *(const u32x4*)(lds + 1024 + lane * 16);
    SCHED();
#pragma unroll
    for (int v = 0; v < 8; v++)
#pragma unroll
      for (int r = 0; r < 4; r++) rd[v][r] = __builtin_amdgcn_rcpf(rd[v][r]);
    f32x4 n0 = mma16(as4(u32x2{kvn[0], kvn[1]}), as4(u32x2{pq[0][0], pq[0][1]}), z4);
    f32x4 n1 = mma16(as4(u32x2{kvn[2], kvn[3]}), as4(u32x2{pq[0][2], pq[0][3]}), z4);
    SCHED();
#pragma unroll
    for (int u = 0; u < 16; u++) {
      f32x4 m0 = z4, m1 = z4;
      if (u + 1 < 16) {
        m0 = mma16(as4(u32x2{kvn2[0], kvn2[1]}), as4(u32x2{pq[u + 1][0], pq[u + 1][1]}), z4);
        m1 = mma16(as4(u32x2{kvn2[2], kvn2[3]}), as4(u32x2{pq[u + 1][2], pq[u + 1][3]}), z4);
      }
      if (u + 2 < 16) kvn2 = *(const u32x4*)(lds + (u + 2) * 1024 + lane * 16);
      n0 *= rd[u >> 1][2 * (u & 1)]; n1 *= rd[u >> 1][2 * (u & 1) + 1];
      const u32x4 o = {pk(n0[0], n0[1]), pk(n0[2], n0[3]), pk(n1[0], n1[1]), pk(n1[2], n1[3])};
#pragma unroll
      for (int a = 0; a < 4; a++) {
        acc[a] = mma(wf[u & 3][a], as8(o), acc[a]);
        if (u < 12) wf[u & 3][a] = wfrd(u + 4, a);
      }
      n0 = m0; n1 = m1;
      SCHED();
    }
    TS(9);
    lwait(FL + 48, 4);
    TS(10);
    const float ga = bflo(gv), gb = bfhi(gv);
    unsigned pv[4][2];
#pragma unroll
    for (int a = 0; a < 2; a++) {
      const f32x4 t0 = *(const f32x4*)(lds + L_FFT + ((w * 4 + a) * 64 + lane) * 16);
      const f32x4 t1 = *(const f32x4*)(lds + L_FFT + ((w * 4 + a + 2) * 64 + lane) * 16);
      const f32x4 v0 = acc[a] * ga + (t0 + t1) * gb, v1 = acc[a + 2] * ga + (t0 - t1) * gb;
      pv[a][0] = pk(v0[0], v0[1]); pv[a][1] = pk(v0[2], v0[3]);
      pv[a + 2][0] = pk(v1[0], v1[1]); pv[a + 2][1] = pk(v1[2], v1[3]);
    }
#pragma unroll
    for (int a = 0; a < 2; a++) {
      auto s0 = __builtin_amdgcn_permlane16_swap(pv[2 * a][0], pv[2 * a + 1][0], false, false);
      auto s1 = __builtin_amdgcn_permlane16_swap(pv[2 * a][1], pv[2 * a + 1][1], false, false);
      u32x4 v = {s0[0], s1[0], s0[1], s1[1]};
      char* op = out + (size_t)(16 * stg + i) * 1024 + (256 * a + 32 * ntile + 16 * (g & 1) + 8 * (g >> 1)) * 2;
      asm volatile("global_store_dwordx4 %0, %1, off sc1\n\ts_nop 1" :: "v"(op), "v"(v) : "memory");
    }
    TS(11);
    TSFLUSH();
    return;
  }
  {
    WAITV(0);
    if (b == SPECIAL) lwait(FL + 80, 4);
    const int pos = (col & 1) * 128 + (col >> 1);
    arrive(rs, 50 + w * 4 + (pos >> 6), 64, WS_F3 + (w * 4 + (pos >> 6)) * 128, 1, ep, lane);
    const float sgn = (i & 1) ? -1.f : 1.f;
    const unsigned k0v = (i & 1) ? 0xb5803580u : 0x35803580u;
    const float fn = (float)(32 * ntile + 16 * (w & 1) + i) * (1.f / 512.f);
    const float t2 = 2.f * fcos(fn * 32.f);
    f32x2 c0[4], c1[4];
#pragma unroll
    for (int d = 0; d < 4; d++) {
      const float kf = (float)(4 * g + 128 * (w & 1) + (w >> 1)) + (float)(16 * (d >> 1) + 2 * (d & 1));
      const float a0 = kf * fn, a1 = (kf + 32.f) * fn;
      c0[d] = f32x2{fcos(a0), fsin(a0)} * 0x1p-19f;
      c1[d] = f32x2{fcos(a1), fsin(a1)} * 0x1p-19f;
    }
    u32x4 gf[8];
#pragma unroll
    for (int j = 0; j < 2; j++) {
#pragma unroll
      for (int h = 0; h < 2; h++) {
#pragma unroll
        for (int d = 0; d < 4; d++) {
          gf[4 * j + 2 * h][d] = pk(c0[d][0], c0[d][1]);
          gf[4 * j + 2 * h + 1][d] = pk(c1[d][0], c1[d][1]);
          c0[d] = c1[d] * t2 - c0[d];
        }
#pragma unroll
        for (int d = 0; d < 4; d++) c1[d] = c0[d] * t2 - c1[d];
      }
      gf[4 * j][0] = (w == j && g == 0) ? k0v : gf[4 * j][0];
      if (j == 0) {
        const float sg = (w & 1) ? sgn : 1.f;
#pragma unroll
        for (int d = 0; d < 4; d++) { c0[d] *= sg; c1[d] *= sg; }
      }
    }
#pragma unroll
    for (int k = 0; k < 8; k++) PIN(gf[k]);
    TS(8);
    gwait<SLP>(rs, WS_F3 + ((mtile >> 3) * 4 + w) * 128, ep);
    lwait(FL + 80, 4);
    linc(FL + 96, lane);
    TS(9);
    {
      const unsigned v = (lane >> 3) * YP + ((((lane & 7) >> 1) ^ ((lane >> 4) & 3)) * 32) + (lane & 1) * 16;
      const char* src = ws + WS_YT + (size_t)(w * 128) * YP + mtile * 128;
      const unsigned la = laddr_of(lds) + L_Y + w * 16384;
#pragma unroll
      for (int j = 0; j < 4; j++) glds4c(la + j * 4096, src + (size_t)j * (32 * YP), v, v + (8 * YP - 1024), v + 2 * (8 * YP - 1024), v + 3 * (8 * YP - 1024));
    }
    WAITV(0);
    lset(FL + 32 + w * 4);
    TS(10);
    const int toffB = L_Y + (4 * g + q) * 128 + (fsw * 32) + p * 8;
    f32x4 accb[4];
#pragma unroll
    for (int c = 0; c < 4; c++) accb[c] = f32x4{0, 0, 0, 0};
    s16x8 tb_[2][2][4];
    auto rd = [&](int u) {
      const char* slot = lds + (w ^ (u >> 1)) * 16384 + (u & 1) * 8192;
#pragma unroll
      for (int ks = 0; ks < 2; ks++)
#pragma unroll
        for (int c = 0; c < 4; c++) tb_[u & 1][ks][c] = trd2(slot + (toffB ^ (32 * c)) + ks * 4096, slot + (toffB ^ (32 * c)) + ks * 4096 + 2048);
    };
    auto mm = [&](int u) {
#pragma unroll
      for (int ks = 0; ks < 2; ks++)
#pragma unroll
        for (int c = 0; c < 4; c++) accb[c] = mma(as8(gf[2 * u + ks]), tb_[u & 1][ks][c], accb[c]);
    };
    rd(0);
    SCHED();
#pragma unroll
    for (int u = 1; u < 4; u++) {
      if (u == 2) lwait(FL + 32 + (w ^ 1) * 4, 1);
      rd(u); mm(u - 1);
#pragma unroll
      for (int t = 0; t < 8; t++) { SGB(0x100, 2); SGB(0x008, 1); }
      SCHED();
    }
    mm(3);
    lwait(FL + 96, 4);
    if (b == 0 && w == 1 && lane == 0) stc1(rs, WS_E, ep);
#pragma unroll
    for (int c = 0; c < 4; c++) *(f32x4*)(lds + L_FFT + ((c * 4 + w) * 64 + lane) * 16) = accb[c];
    linc(FL + 48, lane);
    TS(11);
    TSFLUSH();
  }
}

extern "C" __global__ void gm_end() {}

extern "C" void kernel_launch(void* const* d_in, const int* in_sizes, int n_in, void* d_out, int out_size, void* d_ws, size_t ws_size, hipStream_t stream) {
  gm_all<<<256, 512, 0, stream>>>((const char*)d_in[0], (const char*)d_in[1], (const char*)d_in[4], (const char*)d_in[5], (const char*)d_in[6],
                                  (const char*)d_in[2], (char*)d_ws, (char*)d_out);
}
```

```cpp
#include <hip/hip_runtime.h>
#include <stdint.h>

typedef short s16x4 __attribute__((ext_vector_type(4)));
typedef short s16x8 __attribute__((ext_vector_type(8)));
typedef float f32x4 __attribute__((ext_vector_type(4)));
typedef float f32x2 __attribute__((ext_vector_type(2)));
typedef unsigned u32x4 __attribute__((ext_vector_type(4)));
typedef unsigned u32x2 __attribute__((ext_vector_type(2)));
typedef __bf16 bfx2 __attribute__((ext_vector_type(2)));
#define LAS __attribute__((address_space(3)))
#define DI __device__ __forceinline__
#define ROT1 6
#define CSTR 4352
#define PITC 8320
#define PITY 4224
#define SLP 2

static constexpr unsigned CP = PITC, YP = PITY;
static constexpr unsigned WS_X1T = 0;
static constexpr unsigned WS_WT  = WS_X1T + (9u << 18);
static constexpr unsigned WS_PQ  = WS_WT + (9u << 18);
static constexpr unsigned WS_KVP = WS_PQ + (2u << 20);
static constexpr unsigned WS_GT  = WS_KVP + (512u << 10);
static constexpr unsigned WS_KVR = WS_GT + (16u << 10);
static constexpr unsigned WS_YT  = WS_KVR + (32u << 10);
static constexpr unsigned WS_E   = WS_YT + (9u << 18);
static constexpr unsigned WS_CNT = WS_E + 4096;
static constexpr unsigned CS = CSTR;
static constexpr unsigned WS_F0  = WS_CNT + 128 * CS;
static constexpr unsigned WS_F1  = WS_F0 + 8 * 128;
static constexpr unsigned WS_F2K = WS_F1 + 8 * 128;
static constexpr unsigned WS_F2Q = WS_F2K + 8 * 128;
static constexpr unsigned WS_F3  = WS_F2Q + 16 * 128;
static constexpr unsigned WS_F5  = WS_F3 + 16 * 128;
static constexpr unsigned WS_END = WS_F5 + 8 * 128;
static constexpr unsigned SENT = 0xffffffffu;
static constexpr int SPECIAL = 255;

static constexpr int L_WO = 0, L_Y = 65536, L_FFT = 131072, L_ATT = 147456, L_FL = 163840 - 256;

DI unsigned pk(float lo, float hi) { f32x2 p = {lo, hi}; return __builtin_bit_cast(unsigned, __builtin_convertvector(p, bfx2)); }
DI float bflo(unsigned u) { return __builtin_bit_cast(float, u << 16); }
DI float bfhi(unsigned u) { return __builtin_bit_cast(float, u & 0xffff0000u); }
DI float rbf(float v) { return bflo(pk(v, 0.f)); }
DI s16x8 as8(u32x4 v) { return __builtin_bit_cast(s16x8, v); }
DI s16x4 as4(u32x2 v) { return __builtin_bit_cast(s16x4, v); }
DI f32x4 mma(s16x8 a, s16x8 b, f32x4 c) { return __builtin_amdgcn_mfma_f32_16x16x32_bf16(a, b, c, 0, 0, 0); }
DI f32x4 mma16(s16x4 a, s16x4 b, f32x4 c) { return __builtin_amdgcn_mfma_f32_16x16x16bf16_1k(a, b, c, 0, 0, 0); }
DI void mfa(f32x4& c, s16x8 a, s16x8 b) { asm volatile("v_mfma_f32_16x16x32_bf16 %0, %1, %2, %0" : "+v"(c) : "v"(a), "v"(b)); }
DI s16x4 trd(const char* l) { return __builtin_amdgcn_ds_read_tr16_b64_v4i16((LAS s16x4*)l); }
DI s16x8 cat(s16x4 a, s16x4 b) { return __builtin_shufflevector(a, b, 0, 1, 2, 3, 4, 5, 6, 7); }
DI s16x8 trd2(const char* l0, const char* l1) { return cat(trd(l0), trd(l1)); }
DI s16x8 lrd(const char* l) { return *(const s16x8*)l; }
DI float fsin(float rev) { return __builtin_amdgcn_sinf(rev); }
DI float fcos(float rev) { return __builtin_amdgcn_cosf(rev); }
DI unsigned laddr_of(const char* l) { return (unsigned)(size_t)(const LAS char*)l; }
DI void glds4(unsigned laddr, const char* sb, unsigned v0, unsigned v1, unsigned v2, unsigned v3) {
  asm volatile("s_mov_b32 m0, %0\n\ts_nop 0\n\t"
               "global_load_lds_dwordx4 %2, %1\n\t"
               "global_load_lds_dwordx4 %3, %1 offset:1024\n\t"
               "global_load_lds_dwordx4 %4, %1 offset:2048\n\t"
               "global_load_lds_dwordx4 %5, %1 offset:3072"
               :: "s"(laddr), "s"(sb), "v"(v0), "v"(v1), "v"(v2), "v"(v3) : "memory");
}
DI void glds4c(unsigned laddr, const char* sb, unsigned v0, unsigned v1, unsigned v2, unsigned v3) {
  asm volatile("s_mov_b32 m0, %0\n\ts_nop 0\n\t"
               "global_load_lds_dwordx4 %2, %1 sc1\n\t"
               "global_load_lds_dwordx4 %3, %1 offset:1024 sc1\n\t"
               "global_load_lds_dwordx4 %4, %1 offset:2048 sc1\n\t"
               "global_load_lds_dwordx4 %5, %1 offset:3072 sc1"
               :: "s"(laddr), "s"(sb), "v"(v0), "v"(v1), "v"(v2), "v"(v3) : "memory");
}
DI int perm32(int t, int i) { return 8 * (i >> 2) + 4 * t + (i & 3); }
DI float phi(float t) {
  float e = __builtin_amdgcn_exp2f(t * 1.44269504088896341f);
  e = __builtin_amdgcn_fmed3f(e, 0.f, 1.f);
  return fmaxf(t + 1e-6f, 1e-6f) + e;
}
#define TSP
#define TSA
#define TSDECL
#define TS(slot)
#define TSFLUSH()
#define SCHED() __builtin_amdgcn_sched_barrier(0)
#define SGB(mask, n) __builtin_amdgcn_sched_group_barrier(mask, n, 0)
#define PIN(x) asm volatile("" : "+v"(x))
#define WAITV(n) asm volatile("s_waitcnt vmcnt(" #n ")" ::: "memory")
#define BAR() asm volatile("s_waitcnt lgkmcnt(0)\n\ts_barrier" ::: "memory")
#define FENCE() asm volatile("" ::: "memory")

typedef __amdgpu_buffer_rsrc_t rsrc_t;
DI u32x4 ldc(rsrc_t r, unsigned off) { return __builtin_amdgcn_raw_buffer_load_b128(r, off, 0, 16); }
DI void stc(rsrc_t r, unsigned off, u32x4 v) { __builtin_amdgcn_raw_buffer_store_b128(v, r, off, 0, 16); }
DI void stc1(rsrc_t r, unsigned off, unsigned v) { __builtin_amdgcn_raw_buffer_store_b32(v, r, off, 0, 16); }
template <int SL> DI void gwait(rsrc_t r, unsigned off, unsigned ep) {
#pragma unroll 1
  for (int sp = 0; sp < 200000; sp++) {
    FENCE();
    const u32x4 f = ldc(r, off);
    if (__all(f[0] == ep && f[1] == ep && f[2] == ep && f[3] == ep)) break;
    __builtin_amdgcn_s_sleep(SL);
  }
  FENCE();
}
DI unsigned ticket(rsrc_t r, int c, int lane) {
  unsigned old = 0;
  if (lane == 0) old = __builtin_amdgcn_raw_ptr_buffer_atomic_add_i32(1, r, WS_CNT + c * CS, 0, 16);
  old = __builtin_amdgcn_readfirstlane(old);
  FENCE();
  return old;
}
DI void arrive(rsrc_t r, int c, unsigned n, unsigned flag, int nrep, unsigned ep, int lane) {
  if (ticket(r, c, lane) == n - 1 && lane < nrep) stc(r, flag + lane * 128, u32x4{ep, ep, ep, ep});
}
DI void linc(char* p, int lane) { if (lane == 0) asm volatile("ds_add_u32 %0, %1" :: "v"(laddr_of(p)), "v"(1u) : "memory"); }
DI void lset(char* p) { asm volatile("ds_write_b32 %0, %1" :: "v"(laddr_of(p)), "v"(1u) : "memory"); }
DI void lwait(const char* p, unsigned want) {
#pragma unroll 1
  for (int sp = 0; sp < 4000000; sp++) {
    if ((unsigned)__builtin_amdgcn_readfirstlane(*(const volatile LAS unsigned*)p) >= want) break;
    __builtin_amdgcn_s_sleep(1);
  }
  FENCE();
}

DI void epoch_dma(char* lds, const char* p, int lane) {
  if (lane == 0) asm volatile("s_mov_b32 m0, %0\n\ts_nop 0\n\tglobal_load_lds_dword %2, %1" :: "s"(laddr_of(lds) + L_FL - 16), "s"(p), "v"(0u) : "memory");
}
DI unsigned epoch_get(const char* lds) { return (unsigned)__builtin_amdgcn_readfirstlane(*(const volatile LAS unsigned*)(lds + L_FL - 16)) + 1u; }
DI bool has_sent(u32x4 v) { return v[0] == SENT || v[1] == SENT || v[2] == SENT || v[3] == SENT; }
DI unsigned ticket_issue(rsrc_t r, int c, int lane) {
  unsigned old = 0;
  if (lane == 0) old = __builtin_amdgcn_raw_ptr_buffer_atomic_add_i32(1, r, WS_CNT + c * CS, 0, 16);
  return old;
}
DI void ticket_done(rsrc_t r, unsigned old, unsigned n, unsigned flag, int nrep, unsigned ep, int lane) {
  if ((unsigned)__builtin_amdgcn_readfirstlane(old) == n - 1 && lane < nrep) stc(r, flag + lane * 128, u32x4{ep, ep, ep, ep});
}

DI void wo_dma(char* lds, const char* wo, int ntile, int j, int w, int lane) {
  const int blk = ((lane & 7) >> 1) ^ ((lane >> 4) & 3);
  const unsigned v = (lane >> 3) * 1024 + (blk & 1) * 32 + (blk >> 1) * 512 + (lane & 1) * 16;
  glds4(laddr_of(lds) + L_WO + j * 16384 + w * 4096, wo + (size_t)(j * 128 + w * 32) * 1024 + ntile * 64, v, v + 7168, v + 2 * 7168, v + 3 * 7168);
}

DI void p1_prod(char* lds, const char* x, const char* wqkv, const char* wo, int ntile, int m, int col0, int col1, int rot, int w, int lane) {
  unsigned voffA[4], voffB[4];
#pragma unroll
  for (int j = 0; j < 4; j++) {
    const int par = j & 1;
    voffA[j] = (lane >> 3) * 1024 + (((lane & 7) ^ ((4 * par + (lane >> 4)) & 7)) * 16) + j * (8192 - 1024);
    voffB[j] = (lane >> 3) * 3072 + ((((lane & 7) >> 1) ^ (((lane >> 4) & 1) | (par << 1))) * 32) + (lane & 1) * 16 + j * (24576 - 1024);
  }
  const unsigned lbase = laddr_of(lds) + w * 4096;
  const char* xa = x + (size_t)(m * 128 + w * 32) * 1024;
  const char* wb = wqkv + (size_t)((w & 1) * 32) * 3072 + ((w >> 1) ? col1 : col0) * 2;
  auto issue = [&](int idx) {
    const int ks = (idx + rot) & 7;
    const unsigned slot = lbase + (idx & 3) * 32768;
    glds4(slot, xa + ks * 128, voffA[0], voffA[1], voffA[2], voffA[3]);
    glds4(slot + 16384, wb + (size_t)(ks * 64) * 3072, voffB[0], voffB[1], voffB[2], voffB[3]);
  };
  issue(0); issue(1);
  WAITV(8); BAR();
  issue(2); issue(3);
#pragma unroll 1
  for (int j = 1; j < 5; j++) { WAITV(16); BAR(); issue(j + 3); }
  WAITV(16); BAR();
  wo_dma(lds, wo, ntile, 0, w, lane);
  WAITV(12); BAR();
  wo_dma(lds, wo, ntile, 1, w, lane);
  WAITV(8); BAR();
}

template <bool QT>
DI void p1_gemm(TSP char* lds, rsrc_t rs, int m, int nt, unsigned& ep, const char* epp, int w, int lane) {
  const int i = lane & 15, g = lane >> 4, q = i >> 2, p = lane & 3;
  f32x4 acc[8][2];
#pragma unroll
  for (int a = 0; a < 8; a++) { acc[a][0] = f32x4{0, 0, 0, 0}; acc[a][1] = f32x4{0, 0, 0, 0}; }
  const int aoff = i * 128 + ((g ^ (i >> 1)) * 16);
  const int boff = 16384 + (8 * g + q) * 128 + ((w ^ (((q >> 1) & 1) | ((g & 1) << 1))) * 32) + p * 8;
  s16x8 A[2][2][8]; s16x4 B[2][2][2][2];
  auto rd1 = [&](int buf, const char* slot, int r) {
    const int ks = r / 12, rr = r % 12;
    const int t = rr == 0 ? 0 : rr == 1 ? 1 : rr == 3 ? 2 : rr == 4 ? 3 : -1;
    const int a = rr == 2 ? 0 : rr - 4;
    if (t >= 0) B[buf][ks][t >> 1][t & 1] = trd(slot + boff + (t >> 1) * 8192 + ks * 4096 + (t & 1) * 512);
    else A[buf][ks][a] = lrd(slot + (aoff ^ (ks * 64)) + a * 2048);
  };
  auto mf1 = [&](int buf, int ks, int mt, int hh) {
    const s16x8 bf = cat(B[buf][ks][hh][0], B[buf][ks][hh][1]);
    if (QT) mfa(acc[mt][hh], bf, A[buf][ks][mt]); else mfa(acc[mt][hh], A[buf][ks][mt], bf);
  };
  auto region = [&](int cur, const char* slot) {
#pragma unroll
    for (int mi = 0; mi < 32; mi++) {
      mf1(cur, mi >> 4, (mi & 15) >> 1, mi & 1);
      SCHED();
      if (mi < 24) { rd1(cur ^ 1, slot, mi); SCHED(); }
    }
  };
  if (w == 0) epoch_dma(lds, epp, lane);
  if (QT && blockIdx.x == 128 && w == 0) {
    const u32x4 sent4 = {SENT, SENT, SENT, SENT};
    stc1(rs, WS_CNT + lane * CS, 0u);
    if (lane < 4) stc1(rs, WS_CNT + (64 + lane) * CS, 0u);
#pragma unroll 1
    for (int j = 0; j < 24; j++) stc(rs, WS_KVR + (j * 64 + lane) * 16, sent4);
    WAITV(0);
    const unsigned e = epoch_get(lds);
    if (lane < 8) stc(rs, WS_F0 + lane * 128, u32x4{e, e, e, e});
  }
  __builtin_amdgcn_s_waitcnt(0xC07F);
  BAR();
  TS(1);
#pragma unroll
  for (int r = 0; r < 24; r++) rd1(0, lds, r);
  SCHED();
#pragma unroll 1
  for (int it = 0; it < 3; it++) {
    BAR(); SCHED();
    region(0, lds + ((2 * it + 1) & 3) * 32768);
    if (it == 1) WAITV(0);
    BAR(); SCHED();
    region(1, lds + ((2 * it + 2) & 3) * 32768);
  }
  ep = epoch_get(lds);
  BAR(); SCHED();
  region(0, lds + 3 * 32768);
  asm volatile("s_nop 7");
  SCHED();
  auto mmh = [&](int hh) {
#pragma unroll
    for (int mt = 0; mt < 8; mt++)
#pragma unroll
      for (int ks = 0; ks < 2; ks++) {
        const s16x8 bf = cat(B[1][ks][hh][0], B[1][ks][hh][1]);
        acc[mt][hh] = QT ? mma(bf, A[1][ks][mt], acc[mt][hh]) : mma(A[1][ks][mt], bf, acc[mt][hh]);
      }
  };
  mmh(0);
  SCHED();
  TS(2);
  if (QT) {
    mmh(1);
#pragma unroll
    for (int mt = 0; mt < 8; mt++) {
      u32x4 v = {pk(phi(acc[mt][0][0]), phi(acc[mt][0][1])), pk(phi(acc[mt][0][2]), phi(acc[mt][0][3])),
                 pk(phi(acc[mt][1][0]), phi(acc[mt][1][1])), pk(phi(acc[mt][1][2]), phi(acc[mt][1][3]))};
      stc(rs, WS_PQ + (((m * 8 + mt) * 16 + (nt - 8) * 4 + w) * 64 + lane) * 16, v);
    }
    lwait(lds + L_FL + 100, 1);
    WAITV(0);
    arrive(rs, 34 + m, 16, WS_F2Q + m * 128, 1, ep, lane);
  } else {
    mmh(1);
    u32x4 pa[4];
#pragma unroll
    for (int u = 0; u < 4; u++)
      pa[u] = u32x4{pk(phi(acc[2 * u][0][0]), phi(acc[2 * u][0][1])), pk(phi(acc[2 * u][0][2]), phi(acc[2 * u][0][3])),
                    pk(phi(acc[2 * u + 1][0][0]), phi(acc[2 * u + 1][0][1])), pk(phi(acc[2 * u + 1][0][2]), phi(acc[2 * u + 1][0][3]))};
#pragma unroll
    for (int t = 0; t < 16; t++) { SGB(0x008, 1); SGB(0x002, 2); }
    SCHED();
    f32x4 kv = {0, 0, 0, 0}, z = {0, 0, 0, 0};
    const u32x4 ones = {0x3f803f80u, 0x3f803f80u, 0x3f803f80u, 0x3f803f80u};
#pragma unroll
    for (int u = 0; u < 4; u++) {
      u32x4 b = {pk(acc[2 * u][1][0], acc[2 * u][1][1]), pk(acc[2 * u][1][2], acc[2 * u][1][3]),
                 pk(acc[2 * u + 1][1][0], acc[2 * u + 1][1][1]), pk(acc[2 * u + 1][1][2], acc[2 * u + 1][1][3])};
      z = mma(as8(pa[u]), as8(ones), z);
      kv = mma(as8(pa[u]), as8(b), kv);
    }
    const int head = nt * 4 + w;
    u32x4 v = {pk(kv[0], kv[1]), pk(kv[2], kv[3]), pk(z[0], z[1]), pk(z[2], z[3])};
    stc(rs, WS_KVP + ((m * 32 + head) * 64 + lane) * 16, v);
    lwait(lds + L_FL + 100, 1);
    WAITV(0);
    if (ticket(rs, 1 + head, lane) == 15) {
      TS(4);
      u32x4 kl[16];
#pragma unroll
      for (int pp = 0; pp < 16; pp++) kl[pp] = ldc(rs, WS_KVP + ((pp * 32 + head) * 64 + lane) * 16);
      FENCE();
      const unsigned tk = ticket_issue(rs, 33, lane);
      u32x4 r;
#pragma unroll
      for (int d = 0; d < 4; d++) {
        float lo[4], hi[4];
#pragma unroll
        for (int j = 0; j < 4; j++) {
          lo[j] = (bflo(kl[4 * j][d]) + bflo(kl[4 * j + 1][d])) + (bflo(kl[4 * j + 2][d]) + bflo(kl[4 * j + 3][d]));
          hi[j] = (bfhi(kl[4 * j][d]) + bfhi(kl[4 * j + 1][d])) + (bfhi(kl[4 * j + 2][d]) + bfhi(kl[4 * j + 3][d]));
        }
        r[d] = pk((lo[0] + lo[1]) + (lo[2] + lo[3]), (hi[0] + hi[1]) + (hi[2] + hi[3]));
      }
      ticket_done(rs, tk, 32, WS_F2K, 8, ep, lane);
      const int u = (head >> 3) * 4 + (head & 3), hh = (head >> 2) & 1;
      const bool zr = (i & 3) == hh + 2 * (u & 1);
      __builtin_amdgcn_raw_buffer_store_b64(u32x2{r[0], r[1]}, rs, WS_KVR + (u * 64 + lane) * 16 + hh * 8, 0, 16);
      if (zr) stc(rs, WS_KVR + 16384 + ((u >> 1) * 64 + lane) * 16, hh ? u32x4{0u, 0u, r[2], r[3]} : u32x4{r[2], r[3], 0u, 0u});
      TS(5);
    }
  }
}

DI float dpp_sum16(float v) {
  v += __builtin_bit_cast(float, __builtin_amdgcn_update_dpp(0, __builtin_bit_cast(int, v), 0x128, 0xf, 0xf, false));
  v += __builtin_bit_cast(float, __builtin_amdgcn_update_dpp(0, __builtin_bit_cast(int, v), 0x124, 0xf, 0xf, false));
  v += __builtin_bit_cast(float, __builtin_amdgcn_update_dpp(0, __builtin_bit_cast(int, v), 0x122, 0xf, 0xf, false));
  v += __builtin_bit_cast(float, __builtin_amdgcn_update_dpp(0, __builtin_bit_cast(int, v), 0x121, 0xf, 0xf, false));
  return v;
}

DI void p1_rows(TSP char* lds, const char* x, const char* gw, u32x4& gwv, rsrc_t rs, int a, int w, int lane) {
  const int i = lane & 15, g = lane >> 4;
  char* LX = lds;
  char* LS = lds + 32768;
  const char* LC = lds + 106496;
  const int amt = (a >> 1) & 1;
  {
    const unsigned v = lane * 16;
    const char* src = x + (size_t)(128 * (a >> 2) + perm32(amt, 8 * (a & 1) + w)) * 1024;
    glds4(laddr_of(LX) + w * 4096, src, v, v + 31 * 1024, v + 62 * 1024, v + 93 * 1024);
    glds4(laddr_of(LX) + w * 4096 + 16384, src + 8192, v, v + 31 * 1024, v + 62 * 1024, v + 93 * 1024);
  }
  int soff[4];
#pragma unroll
  for (int r = 0; r < 4; r++) {
    const int k2 = 4 * g + r;
    const int k = i + 32 * k2;
    const int col = (i == 0) ? 16 * k2 : (k2 < 8 ? k : 512 - k);
    soff[r] = col * 128 + ((w ^ (col & 7)) * 16);
    PIN(soff[r]);
  }
  TS(12);
  BAR();
  TS(13);
  const u32x4 b1re = *(const u32x4*)(LC + lane * 16), b1im = *(const u32x4*)(LC + 1024 + lane * 16);
  const u32x4 a3re = *(const u32x4*)(LC + 2048 + lane * 16), a3im = *(const u32x4*)(LC + 3072 + lane * 16);
  const u32x4 a4re = *(const u32x4*)(LC + 4096 + lane * 16), a4im = *(const u32x4*)(LC + 5120 + lane * 16);
  const f32x4 twc = *(const f32x4*)(LC + 6144 + lane * 16), tws = *(const f32x4*)(LC + 7168 + lane * 16);
  WAITV(0);
  TS(1);
#pragma unroll 1
  for (int hf = 0; hf < 2; hf++) {
    unsigned o[4][4];
#pragma unroll
    for (int a4 = 0; a4 < 4; a4++) {
      const char* row = LX + ((w + 4 * hf) * 4 + a4) * 1024;
      s16x8 xf = trd2(row + lane * 8, row + 512 + lane * 8);
      const f32x4 z4 = {0, 0, 0, 0};
      f32x4 are = mma(xf, as8(b1re), z4), aim = mma(xf, as8(b1im), z4);
      f32x4 tr = are * twc + aim * tws, ti = aim * twc - are * tws;
      asm("" : "+v"(tr), "+v"(ti));
      u32x4 bb;
#pragma unroll
      for (int r = 0; r < 4; r++) bb[r] = pk(tr[r], ti[r]);
      f32x4 xre = mma(as8(a3re), as8(bb), z4), xim = mma(as8(a3im), as8(bb), z4);
      f32x4 sre = mma(as8(a4re), as8(bb), z4), sim = mma(as8(a4im), as8(bb), z4);
#pragma unroll
      for (int r = 0; r < 4; r++) o[r][a4] = pk((i == 0) ? sre[r] : xre[r], (i == 0) ? sim[r] : xim[r]);
    }
    if (lane == 0) {
      u32x4 v = {o[0][0] >> 16, o[0][1] >> 16, o[0][2] >> 16, o[0][3] >> 16};
      *(u32x4*)(LS + 256 * 128 + (w + 4 * hf) * 16) = v;
#pragma unroll
      for (int a4 = 0; a4 < 4; a4++) o[0][a4] &= 0xffffu;
    }
#pragma unroll
    for (int r = 0; r < 4; r++) {
      u32x4 v = {o[r][0], o[r][1], o[r][2], o[r][3]};
      *(u32x4*)(LS + (soff[r] ^ (hf * 64))) = v;
    }
  }
  TS(14);
  linc(lds + L_FL + 108, lane);
  gwv = *(const u32x4*)(gw + lane * 16);
  lwait(lds + L_FL + 108, 4);
  TS(2);
  const int sl = lane & 7;
  const int sc = w * 8 + (lane >> 3);
  const int xline = (amt * 4 + (a >> 4)) * 1024 + ((a >> 2) & 3) * 256 + (a & 1) * 128;
  u32x4 v0[8];
#pragma unroll
  for (int j = 0; j < 8; j++) v0[j] = *(const u32x4*)(LS + (j * 32 + sc) * 128 + ((sl ^ (sc & 7)) * 16));
#pragma unroll
  for (int j = 0; j < 8; j++) stc(rs, WS_X1T + (j * 32 + sc) * CP + xline + sl * 16, v0[j]);
  if (w == 3 && lane < 8) stc(rs, WS_X1T + 256 * CP + xline + lane * 16, *(const u32x4*)(LS + 256 * 128 + lane * 16));
  TS(15);
}

DI void p1_filt(TSP char* lds, const char* wo, const char* wr, const char* wi, rsrc_t rs, int a, const char* epp, int w, int lane) {
  const int i = lane & 15, g = lane >> 4;
  char* LF = lds + 69632;
  if (w == 0) epoch_dma(lds, epp, lane);
  {
    const float fi = (float)i;
    char* cp = lds + 106496 + lane * 16 + w * 4;
    float c[2], s[2];
#pragma unroll
    for (int e = 0; e < 2; e++) {
      const float ang = (float)(4 * g + 16 * (w >> 1) + 2 * (w & 1) + e) * (fi * (1.f / 32.f));
      c[e] = fcos(ang);
      s[e] = (i == 0) ? (e ? -1.f : 1.f) : fsin(-ang);
    }
    *(unsigned*)(cp) = pk(c[0], c[1]);
    *(unsigned*)(cp + 1024) = pk(s[0], s[1]);
    const float n2i = (float)(4 * g + w) * fi;
    {
      const float ang = n2i * (1.f / 16.f);
      const float cs = fcos(ang), sn = fsin(ang);
      *(unsigned*)(cp + 2048) = pk(cs, sn);
      *(unsigned*)(cp + 3072) = pk(sn, cs) ^ ((i >= 8) ? 0x80000000u : 0x00008000u);
    }
    {
      const float ang = n2i * (1.f / 32.f);
      const float cs = fcos(ang);
      const float sn = (i == 0) ? ((w & 1) ? -1.f : 1.f) : fsin(-ang);
      const unsigned u = pk(cs, sn);
      const int sh = (i & 1) * 16;
      *(unsigned*)(cp + 4096) = (u & 0xffffu) << sh;
      *(unsigned*)(cp + 5120) = (u >> 16) << sh;
    }
    *(float*)(cp + 6144) = fcos(n2i * (1.f / 512.f));
    *(float*)(cp + 7168) = fsin(n2i * (1.f / 512.f));
  }
  TS(12);
  BAR();
  {
    const unsigned v = lane * 16;
    const size_t off = (size_t)(8 * (a & 7) + 64 * (4 * (a >> 3) + w)) * 514;
    glds4(laddr_of(LF) + w * 4352, wr + off, v, v, v, v);
    glds4(laddr_of(LF) + (w + 4) * 4352, wi + off, v, v, v, v);
    if (lane == 0) {
      asm volatile("s_mov_b32 m0, %0\n\ts_nop 0\n\tglobal_load_lds_dwordx4 %2, %1" :: "s"(laddr_of(LF) + w * 4352 + 4096), "s"(wr + off + 4096), "v"(v) : "memory");
      asm volatile("s_mov_b32 m0, %0\n\ts_nop 0\n\tglobal_load_lds_dwordx4 %2, %1" :: "s"(laddr_of(LF) + (w + 4) * 4352 + 4096), "s"(wi + off + 4096), "v"(v) : "memory");
    }
  }
  const int sl = lane & 7;
  asm volatile("s_mov_b32 m0, %0\n\ts_nop 0\n\tglobal_load_lds_dword %2, %1\n\tglobal_load_lds_dword %3, %1"
               :: "s"(laddr_of(lds) + L_ATT + 8192), "s"(wo), "v"((unsigned)(((a & 7) * 4 + w) * 64 + lane) * 128u), "v"((unsigned)(((a & 7) * 4 + w) * 64 + lane + 2048) * 128u) : "memory");
  WAITV(2);
  linc(lds + L_FL + 112, lane);
  lwait(lds + L_FL + 112, 4);
  TS(1);
  const int wline = ((((a & 7) >> 1) * 2 + ((a >> 3) & 1)) * 64 + (a >> 4) * 16 + (a & 1) * 8) * 16;
#pragma unroll 1
  for (int hf = 0; hf < 2; hf++) {
    const int cb = (lane >> 3) * 16 + w + 128 * hf;
    const char* fp = LF + sl * 514 + cb * 2;
#pragma unroll
    for (int j = 0; j < 4; j++) {
      u32x4 fv;
#pragma unroll
      for (int r = 0; r < 4; r++) fv[r] = (unsigned)*(const unsigned short*)(fp + r * 4352 + j * 8) | ((unsigned)*(const unsigned short*)(fp + (4 + r) * 4352 + j * 8) << 16);
      stc(rs, WS_WT + (cb + 4 * j) * CP + wline + sl * 16, fv);
    }
  }
  if (w == 3 && lane < 8) {
    u32x4 v;
#pragma unroll
    for (int r = 0; r < 4; r++) v[r] = (unsigned)*(const unsigned short*)(LF + sl * 514 + 512 + r * 4352) | ((unsigned)*(const unsigned short*)(LF + sl * 514 + 512 + (4 + r) * 4352) << 16);
    stc(rs, WS_WT + 256 * CP + wline + sl * 16, v);
  }
}

DI void p1_gate(char* lds, u32x4 gwv, rsrc_t rs, int a, int w, int lane) {
  const int i = lane & 15, g = lane >> 4;
  const u32x4 ones = {0x3f803f80u, 0x3f803f80u, 0x3f803f80u, 0x3f803f80u};
#pragma unroll 1
  for (int hf = 0; hf < 2; hf++) {
    float gsum[4];
#pragma unroll
    for (int j = 0; j < 4; j++) {
      u32x4 xv = *(const u32x4*)(lds + ((w + 4 * hf) * 4 + j) * 1024 + lane * 16);
      u32x4 pr;
#pragma unroll
      for (int d = 0; d < 4; d++) pr[d] = pk(bflo(xv[d]) * bflo(gwv[d]), bfhi(xv[d]) * bfhi(gwv[d]));
      gsum[j] = mma(as8(ones), as8(pr), f32x4{0, 0, 0, 0})[0];
    }
    float gs = (g == 0) ? gsum[0] : (g == 1) ? gsum[1] : (g == 2) ? gsum[2] : gsum[3];
    gs = rbf(dpp_sum16(gs));
    const float gg = rbf(__builtin_amdgcn_rcpf(1.f + __builtin_amdgcn_exp2f(gs * -1.44269504088896341f)));
    const int s = 32 * (4 * (a >> 2) + g) + perm32((a >> 1) & 1, 8 * (a & 1) + w + 4 * hf);
    if (i == 0) stc1(rs, WS_GT + (s >> 6) * 256 + (s & 15) * 16 + ((s >> 4) & 3) * 4, pk(gg, 1.f - gg));
  }
}

template <class F> DI void p2_fft(TSP F idle, char* lb, char* fl, char* f1seen, const char* ws, rsrc_t rs, int c, int cnt, int rep, unsigned& ep, int w, int lane) {
  const int i = lane & 15, g = lane >> 4;
  const int k1 = 16 * w + i;
  const float fk1 = (float)k1, fg = (float)g;
  u32x4 fre[4], fim[4], fc[2], fs[2], fn[2];
  {
    const float st = fk1 * (1.f / 64.f);
    const float a0 = fg * 4.f * st;
    const bool odd = i & 1;
    const unsigned m2 = odd ? 0x80008000u : 0u;
    const unsigned mre = (i & 2) ? 0x80008000u : 0u;
    const unsigned mim = ((i + 1) & 2) ? 0x80008000u : 0u;
#pragma unroll
    for (int d = 0; d < 4; d++) {
      const float ang = __builtin_fmaf((float)d, st, a0);
      const float cs = fcos(ang), sn = fsin(ang);
      const unsigned r0 = pk(cs, sn), i0 = pk(sn, cs) ^ 0x8000u;
      const unsigned r1 = (odd ? i0 : r0) ^ mre, i1 = (odd ? r0 : i0) ^ mim;
      fre[0][d] = r0; fim[0][d] = i0;
      fre[1][d] = r1; fim[1][d] = i1;
      fre[2][d] = r0 ^ m2; fim[2][d] = i0 ^ m2;
      fre[3][d] = r1 ^ m2; fim[3][d] = i1 ^ m2;
    }
#pragma unroll
    for (int ks = 0; ks < 4; ks++) { PIN(fre[ks]); PIN(fim[ks]); }
  }
  idle(0);
  float c1[2][4], s1[2][4], c2[2][4], s2[2][4];
#pragma unroll
  for (int mt = 0; mt < 2; mt++) {
    const float fa = (float)perm32(mt, i);
    const float st = fa * (1.f / 32.f), a0 = fg * 8.f * st;
    const float st1 = fk1 * (1.f / 2048.f), b0 = (fg * 8.f + (float)(4 * mt)) * st1;
    const float st2 = fa * (1.f / 2048.f), d0 = (fg * 4.f + (float)(16 * w)) * st2;
#pragma unroll
    for (int d = 0; d < 4; d++) {
      const float x0 = __builtin_fmaf((float)(2 * d), st, a0), x1 = __builtin_fmaf((float)(2 * d + 1), st, a0);
      fc[mt][d] = pk(fcos(x0), fcos(x1));
      fs[mt][d] = pk(fsin(x0), fsin(x1));
      fn[mt][d] = fs[mt][d] ^ 0x80008000u;
      const float t1 = __builtin_fmaf((float)d, st1, b0);
      c1[mt][d] = fcos(t1); s1[mt][d] = fsin(t1);
      const float t2 = __builtin_fmaf((float)d, st2, d0);
      c2[mt][d] = fcos(t2); s2[mt][d] = fsin(t2);
      PIN(c1[mt][d]); PIN(s1[mt][d]); PIN(c2[mt][d]); PIN(s2[mt][d]);
    }
    PIN(fc[mt]); PIN(fs[mt]); PIN(fn[mt]);
    idle(1 + mt);
  }
  const unsigned yt = WS_YT + (32 * k1 + 8 * g) * 2 + ((c == 256) ? 1 : (c & 1) * 256 + (c & ~1)) * YP;
  stc(rs, yt, u32x4{SENT, SENT, SENT, SENT});
  if (c & 255) stc(rs, yt + YP, u32x4{SENT, SENT, SENT, SENT});
  idle(3);
  TS(4);
  if (w == 0) { gwait<SLP>(rs, WS_F1 + rep * 128, ep); lset(f1seen); }
  else lwait(f1seen, 1);
  TS(5);
  asm volatile("s_mov_b32 m0, %0\n\ts_nop 0\n\tglobal_load_lds_dwordx4 %2, %1 sc1\n\tglobal_load_lds_dwordx4 %2, %1 offset:1024 sc1"
               :: "s"(laddr_of(lb) + 8192 + w * 2048), "s"(ws + WS_X1T + (size_t)c * CP + w * 2048), "v"(lane * 16) : "memory");
  u32x4 wf[2];
#pragma unroll
  for (int mt = 0; mt < 2; mt++) wf[mt] = ldc(rs, WS_WT + c * CP + ((w * 2 + mt) * 64 + lane) * 16);
  WAITV(2);
  linc(fl, lane);
  const unsigned tk = ticket_issue(rs, 50 + w * 4 + cnt, lane);
  lwait(fl, 4);
  TS(6);
  const f32x4 z4 = {0, 0, 0, 0};
  f32x4 re[2], im[2];
#pragma unroll
  for (int mt = 0; mt < 2; mt++) { re[mt] = z4; im[mt] = z4; }
#pragma unroll
  for (int ks = 0; ks < 4; ks++)
#pragma unroll
    for (int mt = 0; mt < 2; mt++) {
      s16x8 xa = lrd(lb + 8192 + ((mt * 4 + ks) * 64 + lane) * 16);
      re[mt] = mma(xa, as8(fre[ks]), re[mt]); im[mt] = mma(xa, as8(fim[ks]), im[mt]);
    }
  u32x4 pr, pi;
#pragma unroll
  for (int mt = 0; mt < 2; mt++) {
    float tr[4], ti[4];
#pragma unroll
    for (int r = 0; r < 4; r++) { tr[r] = re[mt][r] * c1[mt][r] + im[mt][r] * s1[mt][r]; ti[r] = im[mt][r] * c1[mt][r] - re[mt][r] * s1[mt][r]; }
    pr[2 * mt] = pk(tr[0], tr[1]); pr[2 * mt + 1] = pk(tr[2], tr[3]);
    pi[2 * mt] = pk(ti[0], ti[1]); pi[2 * mt + 1] = pk(ti[2], ti[3]);
  }
#pragma unroll
  for (int mt = 0; mt < 2; mt++) {
    re[mt] = mma(as8(fs[mt]), as8(pi), mma(as8(fc[mt]), as8(pr), z4));
    im[mt] = mma(as8(fn[mt]), as8(pr), mma(as8(fc[mt]), as8(pi), z4));
  }
#pragma unroll
  for (int mt = 0; mt < 2; mt++) {
    float tr[4], ti[4];
#pragma unroll
    for (int r = 0; r < 4; r++) {
      const float a = bflo(wf[mt][r]), b = bfhi(wf[mt][r]);
      tr[r] = re[mt][r] * a - im[mt][r] * b; ti[r] = re[mt][r] * b + im[mt][r] * a;
    }
    pr[2 * mt] = pk(tr[0], tr[1]); pr[2 * mt + 1] = pk(tr[2], tr[3]);
    pi[2 * mt] = pk(ti[0], ti[1]); pi[2 * mt + 1] = pk(ti[2], ti[3]);
  }
#pragma unroll
  for (int nt = 0; nt < 2; nt++) {
    re[nt] = mma(as8(pi), as8(fn[nt]), mma(as8(pr), as8(fc[nt]), z4));
    im[nt] = mma(as8(pi), as8(fc[nt]), mma(as8(pr), as8(fs[nt]), z4));
  }
#pragma unroll
  for (int nt = 0; nt < 2; nt++) {
    u32x4 v;
#pragma unroll
    for (int r = 0; r < 4; r++) v[r] = pk(re[nt][r] * c2[nt][r] - im[nt][r] * s2[nt][r], -re[nt][r] * s2[nt][r] - im[nt][r] * c2[nt][r]);
    *(u32x4*)(lb + ((nt * 4 + w) * 64 + lane) * 16) = v;
  }
  linc(fl + 4, lane);
  lwait(fl + 4, 4);
#pragma unroll
  for (int mt = 0; mt < 2; mt++) { re[mt] = z4; im[mt] = z4; }
#pragma unroll
  for (int ks = 0; ks < 4; ks++)
#pragma unroll
    for (int mt = 0; mt < 2; mt++) {
      s16x8 ya = lrd(lb + ((mt * 4 + ks) * 64 + lane) * 16);
      re[mt] = mma(ya, as8(fre[ks]), re[mt]); im[mt] = mma(ya, as8(fim[ks]), im[mt]);
    }
  u32x4 vr = {pk(re[0][0], re[0][1]), pk(re[0][2], re[0][3]), pk(re[1][0], re[1][1]), pk(re[1][2], re[1][3])};
  u32x4 vi = {pk(im[0][0], im[0][1]), pk(im[0][2], im[0][3]), pk(im[1][0], im[1][1]), pk(im[1][2], im[1][3])};
  const bool last = (unsigned)__builtin_amdgcn_readfirstlane(tk) == (cnt == 0 ? 64u : 63u);
  stc(rs, yt, vr);
  if (c & 255) stc(rs, yt + YP, vi);
  if (last && lane == 0) stc(rs, WS_F3 + (w * 4 + cnt) * 128, u32x4{ep, ep, ep, ep});
  TS(7);
}

extern "C" __global__ __launch_bounds__(512, 2) void gm_all(const char* x, const char* wqkv, const char* wr, const char* wi, const char* gw, const char* wo, char* ws, char* out) {
  __shared__ __attribute__((aligned(16))) char lds[163840];
  const int b = blockIdx.x, xcd = b & 7, ii = b >> 3;
  const int w8 = __builtin_amdgcn_readfirstlane(threadIdx.x >> 6), lane = threadIdx.x & 63;
  const int w = w8 & 3;
  TSDECL
  TS(0);
  if (ii < 2 && w8 < 4) {
    unsigned long long pc; int o0, o1;
    asm volatile("s_getpc_b64 %0\n.Lgm_pc%=:\n\ts_mov_b32 %1, gm_all-.Lgm_pc%=\n\ts_mov_b32 %2, gm_end-.Lgm_pc%=" : "=s"(pc), "=s"(o0), "=s"(o1));
    const unsigned off = (ii * 256 + threadIdx.x) * 128;
    if (off < (unsigned)(o1 - o0))
      asm volatile("s_mov_b32 m0, %0\n\ts_nop 0\n\tglobal_load_lds_dword %2, %1" :: "s"(laddr_of(lds) + L_ATT + 8192), "s"(pc + (long long)o0), "v"(off) : "memory");
  }
  unsigned ep;
  const char* epp = ws + WS_E;
  const rsrc_t rs = __builtin_amdgcn_make_buffer_rsrc(ws, 0, WS_END, 0x00020000);
  char* FL = lds + L_FL;
  const int ml = ii & 3, nl = ii >> 2;
  const int mtile = xcd * 4 + ml, ntile = nl;
  const bool aux = ii >= 24;
  {
    const int m = (xcd >> 1) * 4 + ml;
    const int nt = nl < 4 ? (xcd & 1) * 4 + nl : 8 + (xcd & 1) * 2 + (nl - 4);
    if (w8 < 4 || aux) {
      if (w8 == 0) *(LAS unsigned*)(FL + lane * 4) = 0;
      if (w8 >= 4) p1_filt(TSA lds, wo, wr, wi, rs, xcd * 8 + (ii - 24), epp, w, lane);
      else {
        __builtin_amdgcn_s_setprio(2);
        if (aux) {
          u32x4 gwv;
          p1_rows(TSA lds, x, gw, gwv, rs, xcd * 8 + (ii - 24), w, lane);
          p1_gate(lds, gwv, rs, xcd * 8 + (ii - 24), w, lane);
          WAITV(0);
          BAR();
          ep = epoch_get(lds);
          if (w == 1) arrive(rs, 0, 64, WS_F1, 8, ep, lane);
#pragma unroll 1
          for (int j = 0; j < 4; j++) wo_dma(lds, wo, ntile, j, w, lane);
        }
        else if (nl < 4) p1_gemm<false>(TSA lds, rs, m, nt, ep, epp, w, lane);
        else p1_gemm<true>(TSA lds, rs, m, nt, ep, epp, w, lane);
        __builtin_amdgcn_s_setprio(0);
      }
    } else {
      const int col0 = nl >= 4 ? (nt - 8) * 128 : 512 + nt * 64;
      const int col1 = nl >= 4 ? col0 + 64 : col0 + 512;
      p1_prod(lds, x, wqkv, wo, ntile, m, col0, col1, (2 * ml + nl) & ROT1, w, lane);
    }
  }
  TS(3);
  const int i = lane & 15, g = lane >> 4, q = i >> 2, p = lane & 3;
  const int fsw = (2 * g + (q >> 1)) & 3;
  if (w8 >= 4 || b == SPECIAL) {
    const bool pg = w8 >= 4;
    if (pg) {
      ep = epoch_get(lds);
      if (!aux) {
        if (w == 0) { gwait<SLP>(rs, WS_F0 + xcd * 128, ep); lset(FL + 100); }
        wo_dma(lds, wo, ntile, 2, w, lane); wo_dma(lds, wo, ntile, 3, w, lane);
      }
    }
    __builtin_amdgcn_s_setprio(1);
    auto idle = [&](int st) {
      if (st == 2 && pg && aux) { if (w == 0) gwait<SLP>(rs, WS_F0 + xcd * 128, ep); WAITV(0); BAR(); }
      if (st == 3 && !aux) { WAITV(0); linc(FL, lane); }
    };
    p2_fft(TSA idle, pg ? lds + L_FFT : lds + L_Y, pg ? FL + 4 : FL + 20, pg ? FL + 16 : FL + 28, ws, rs, pg ? b : 256, pg ? (b & 1) * 2 + (b >> 7) : 0, xcd, ep, w, lane);
  }
  if (w8 < 4) {
    linc(FL + 80, lane);
    if (aux) { if (b != SPECIAL) WAITV(0); linc(FL, lane); }
    TS(7);
    const int stg = 4 * mtile + w;
    bool kok = false;
    if (w == 0) {
#pragma unroll 1
      for (int sp = 0; sp < 200000; sp++) {
        FENCE();
        const bool ok = ldc(rs, (lane & 1) ? WS_F2K + xcd * 128 : WS_F2Q + (mtile >> 1) * 128)[0] == ep;
        const unsigned long long mk = __ballot(ok);
        kok = mk & 2;
        if (mk & 1) break;
        __builtin_amdgcn_s_sleep(SLP);
      }
      FENCE();
      if (kok) lset(FL + 88);
      lset(FL + 104);
    } else lwait(FL + 104, 1);
    u32x4 pq[4][4];
#pragma unroll
    for (int jj = 0; jj < 4; jj++)
#pragma unroll
      for (int u = 0; u < 4; u++) pq[jj][u] = ldc(rs, WS_PQ + (((4 * mtile + ((w + 1 + jj) & 3)) * 16 + 4 * w + u) * 64 + lane) * 16);
    lwait(FL, 4);
    s16x8 wf[4][4];
    {
      const int toffA = L_WO + w * 16384 + (4 * g + q) * 128 + (fsw * 32) + p * 8;
#pragma unroll
      for (int u = 0; u < 4; u++)
#pragma unroll
        for (int a = 0; a < 4; a++) { const char* pp = lds + (toffA ^ (32 * a)) + u * 2048; wf[u][a] = trd2(pp, pp + 8192); }
    }
    if (w == 0) { if (!kok) { gwait<SLP>(rs, WS_F2K + xcd * 128, ep); lset(FL + 88); } }
    else lwait(FL + 88, 1);
    TS(8);
    u32x4 kr[6];
#pragma unroll 1
    for (int sp = 0; sp < 20000; sp++) {
      FENCE();
      bool bad = false;
#pragma unroll
      for (int j = 0; j < 6; j++) kr[j] = ldc(rs, WS_KVR + ((j < 4 ? 4 * w + j : 12 + 2 * w + j) * 64 + lane) * 16);
#pragma unroll
      for (int j = 0; j < 6; j++) bad |= has_sent(kr[j]);
      if (!__any(bad)) break;
    }
    lwait(FL + 16, 1);
    const unsigned gv = __builtin_amdgcn_raw_buffer_load_b32(rs, WS_GT + mtile * 256 + i * 16 + w * 4, 0, 16);
    TS(13);
    const f32x4 z4 = {0, 0, 0, 0};
    const f32x4 eps = {1e-6f, 1e-6f, 1e-6f, 1e-6f};
    f32x4 acc[4];
    f32x4 nm[2][4][2], dn[2][4];
    auto nd = [&](int jj) {
#pragma unroll
      for (int u = 0; u < 4; u++) {
        dn[jj & 1][u] = mma(as8(kr[4 + (u >> 1)]), as8(pq[jj][u]), eps);
        nm[jj & 1][u][0] = mma16(as4(u32x2{kr[u][0], kr[u][1]}), as4(u32x2{pq[jj][u][0], pq[jj][u][1]}), z4);
        nm[jj & 1][u][1] = mma16(as4(u32x2{kr[u][2], kr[u][3]}), as4(u32x2{pq[jj][u][2], pq[jj][u][3]}), z4);
      }
    };
    u32x4 of[2][4];
    auto sc = [&](int jj) {
#pragma unroll
      for (int u = 0; u < 4; u++) {
        const f32x4 n0 = nm[jj & 1][u][0] * __builtin_amdgcn_rcpf(dn[jj & 1][u][2 * (u & 1)]);
        const f32x4 n1 = nm[jj & 1][u][1] * __builtin_amdgcn_rcpf(dn[jj & 1][u][2 * (u & 1) + 1]);
        of[jj & 1][u] = u32x4{pk(n0[0], n0[1]), pk(n0[2], n0[3]), pk(n1[0], n1[1]), pk(n1[2], n1[3])};
      }
    };
    auto pj = [&](int jj) {
#pragma unroll
      for (int u = 0; u < 4; u++)
#pragma unroll
        for (int a = 0; a < 4; a++) acc[a] = mma(wf[u][a], as8(of[jj & 1][u]), u ? acc[a] : z4);
      if (jj < 3) {
#pragma unroll
        for (int a = 0; a < 4; a++) *(f32x4*)(lds + L_WO + w * 16384 + (jj * 4 + a) * 1024 + lane * 16) = acc[a];
      }
    };
    nd(0); SCHED();
    nd(1); sc(0);
#pragma unroll
    for (int t = 0; t < 12; t++) { SGB(0x008, 1); SGB(0x002, 3); }
    SCHED();
#pragma unroll
    for (int jj = 0; jj < 3; jj++) {
      pj(jj); sc(jj + 1);
#pragma unroll
      for (int t = 0; t < 16; t++) { SGB(0x008, 1); SGB(0x002, 3); }
      SCHED();
      if (jj < 2) { nd(jj + 2); SCHED(); }
    }
    linc(FL + 92, lane);
    pj(3);
    TS(9);
    lwait(FL + 92, 4);
#pragma unroll
    for (int ww = 1; ww < 4; ww++)
#pragma unroll
      for (int a = 0; a < 4; a++) acc[a] += *(const f32x4*)(lds + L_WO + ((w + ww) & 3) * 16384 + ((3 - ww) * 4 + a) * 1024 + lane * 16);
    lwait(FL + 48, 4);
    TS(10);
    const float ga = bflo(gv), gb = bfhi(gv);
    unsigned pv[4][2];
#pragma unroll
    for (int a = 0; a < 2; a++) {
      const f32x4 t0 = *(const f32x4*)(lds + L_FFT + ((w * 4 + a) * 64 + lane) * 16);
      const f32x4 t1 = *(const f32x4*)(lds + L_FFT + ((w * 4 + a + 2) * 64 + lane) * 16);
      const f32x4 v0 = acc[a] * ga + (t0 + t1) * gb, v1 = acc[a + 2] * ga + (t0 - t1) * gb;
      pv[a][0] = pk(v0[0], v0[1]); pv[a][1] = pk(v0[2], v0[3]);
      pv[a + 2][0] = pk(v1[0], v1[1]); pv[a + 2][1] = pk(v1[2], v1[3]);
    }
#pragma unroll
    for (int a = 0; a < 2; a++) {
      auto s0 = __builtin_amdgcn_permlane16_swap(pv[2 * a][0], pv[2 * a + 1][0], false, false);
      auto s1 = __builtin_amdgcn_permlane16_swap(pv[2 * a][1], pv[2 * a + 1][1], false, false);
      u32x4 v = {s0[0], s1[0], s0[1], s1[1]};
      char* op = out + (size_t)(16 * stg + i) * 1024 + (256 * a + 32 * ntile + 16 * (g & 1) + 8 * (g >> 1)) * 2;
      asm volatile("global_store_dwordx4 %0, %1, off sc1\n\ts_nop 1" :: "v"(op), "v"(v) : "memory");
    }
    TS(11);
    TSFLUSH();
    return;
  }
  {
    const float sgn = (i & 1) ? -1.f : 1.f;
    const unsigned k0v = (i & 1) ? 0xb5803580u : 0x35803580u;
    const float fn = (float)(32 * ntile + 16 * (w & 1) + i) * (1.f / 512.f);
    const float t2 = 2.f * fcos(fn * 32.f);
    f32x2 c0[4], c1[4];
#pragma unroll
    for (int d = 0; d < 4; d++) {
      const float kf = (float)(4 * g + 128 * (w & 1) + (w >> 1)) + (float)(16 * (d >> 1) + 2 * (d & 1));
      const float a0 = kf * fn, a1 = (kf + 32.f) * fn;
      c0[d] = f32x2{fcos(a0), fsin(a0)} * 0x1p-19f;
      c1[d] = f32x2{fcos(a1), fsin(a1)} * 0x1p-19f;
    }
    u32x4 gf[8];
#pragma unroll
    for (int j = 0; j < 2; j++) {
#pragma unroll
      for (int h = 0; h < 2; h++) {
#pragma unroll
        for (int d = 0; d < 4; d++) {
          gf[4 * j + 2 * h][d] = pk(c0[d][0], c0[d][1]);
          gf[4 * j + 2 * h + 1][d] = pk(c1[d][0], c1[d][1]);
          c0[d] = c1[d] * t2 - c0[d];
        }
#pragma unroll
        for (int d = 0; d < 4; d++) c1[d] = c0[d] * t2 - c1[d];
      }
      gf[4 * j][0] = (w == j && g == 0) ? k0v : gf[4 * j][0];
      if (j == 0) {
        const float sg = (w & 1) ? sgn : 1.f;
#pragma unroll
        for (int d = 0; d < 4; d++) { c0[d] *= sg; c1[d] *= sg; }
      }
    }
#pragma unroll
    for (int k = 0; k < 8; k++) PIN(gf[k]);
    TS(8);
    gwait<SLP>(rs, WS_F3 + ((mtile >> 3) * 4 + w) * 128, ep);
    lwait(FL + 80, 4);
    linc(FL + 96, lane);
    TS(9);
    const int toffB = L_Y + (4 * g + q) * 128 + (fsw * 32) + p * 8;
    f32x4 accb[4];
    s16x8 tb_[2][2][4];
    auto rd = [&](int u) {
      const char* slot = lds + (w ^ (u >> 1)) * 16384 + (u & 1) * 8192;
#pragma unroll
      for (int ks = 0; ks < 2; ks++)
#pragma unroll
        for (int c = 0; c < 4; c++) tb_[u & 1][ks][c] = trd2(slot + (toffB ^ (32 * c)) + ks * 4096, slot + (toffB ^ (32 * c)) + ks * 4096 + 2048);
    };
    auto mm = [&](int u) {
#pragma unroll
      for (int ks = 0; ks < 2; ks++)
#pragma unroll
        for (int c = 0; c < 4; c++) accb[c] = mma(as8(gf[2 * u + ks]), tb_[u & 1][ks][c], accb[c]);
    };
#pragma unroll 1
    for (int sp = 0; sp < 20000; sp++) {
      const unsigned v = (lane >> 3) * YP + ((((lane & 7) >> 1) ^ ((lane >> 4) & 3)) * 32) + (lane & 1) * 16;
      const char* src = ws + WS_YT + (size_t)(w * 128) * YP + mtile * 128;
      const unsigned la = laddr_of(lds) + L_Y + w * 16384;
#pragma unroll
      for (int j = 0; j < 4; j++) glds4c(la + j * 4096, src + (size_t)j * (32 * YP), v, v + (8 * YP - 1024), v + 2 * (8 * YP - 1024), v + 3 * (8 * YP - 1024));
#pragma unroll
      for (int c = 0; c < 4; c++) accb[c] = f32x4{0, 0, 0, 0};
      WAITV(8);
      rd(0);
      WAITV(0);
      TS(10);
      rd(1); mm(0);
#pragma unroll
      for (int t = 0; t < 8; t++) { SGB(0x100, 2); SGB(0x008, 1); }
      SCHED();
      mm(1);
      const f32x4 s4 = (accb[0] + accb[1]) + (accb[2] + accb[3]);
      const float s1 = (s4[0] + s4[1]) + (s4[2] + s4[3]);
      if (!__any(s1 != s1)) break;
      bool bad = false;
#pragma unroll 1
      for (int j = 0; j < 16; j++) bad |= has_sent(*(const u32x4*)(lds + L_Y + w * 16384 + j * 1024 + lane * 16));
      if (!__any(bad)) break;
    }
    lset(FL + 32 + w * 4);
    lwait(FL + 32 + (w ^ 1) * 4, 1);
    rd(2);
    SCHED();
    rd(3); mm(2);
#pragma unroll
    for (int t = 0; t < 8; t++) { SGB(0x100, 2); SGB(0x008, 1); }
    SCHED();
    mm(3);
    lwait(FL + 96, 4);
    if (b == 0 && w == 1 && lane == 0) stc1(rs, WS_E, ep);
#pragma unroll
    for (int c = 0; c < 4; c++) *(f32x4*)(lds + L_FFT + ((c * 4 + w) * 64 + lane) * 16) = accb[c];
    linc(FL + 48, lane);
    TS(11);
    TSFLUSH();
  }
}

extern "C" __global__ void gm_end() {}

extern "C" void kernel_launch(void* const* d_in, const int* in_sizes, int n_in, void* d_out, int out_size, void* d_ws, size_t ws_size, hipStream_t stream) {
  gm_all<<<256, 512, 0, stream>>>((const char*)d_in[0], (const char*)d_in[1], (const char*)d_in[4], (const char*)d_in[5], (const char*)d_in[6],
                                  (const char*)d_in[2], (char*)d_ws, (char*)d_out);
}
```

```cpp
#define STAMPS
#define PROBE_K 1
#define PROBE_CLS 2
#define PROBE_S 1
#define PROBE_SKIP 999
#include <hip/hip_runtime.h>
#include <stdint.h>

typedef short s16x4 __attribute__((ext_vector_type(4)));
typedef short s16x8 __attribute__((ext_vector_type(8)));
typedef float f32x4 __attribute__((ext_vector_type(4)));
typedef float f32x2 __attribute__((ext_vector_type(2)));
typedef unsigned u32x4 __attribute__((ext_vector_type(4)));
typedef unsigned u32x2 __attribute__((ext_vector_type(2)));
typedef __bf16 bfx2 __attribute__((ext_vector_type(2)));
#define LAS __attribute__((address_space(3)))
#define DI __device__ __forceinline__
#define ROT1 6
#define CSTR 4352
#define PITC 8320
#define PITY 4224
#define SLP 2

static constexpr unsigned CP = PITC, YP = PITY;
static constexpr unsigned WS_X1T = 0;
static constexpr unsigned WS_WT  = WS_X1T + (9u << 18);
static constexpr unsigned WS_PQ  = WS_WT + (9u << 18);
static constexpr unsigned WS_KVP = WS_PQ + (2u << 20);
static constexpr unsigned WS_GT  = WS_KVP + (512u << 10);
static constexpr unsigned WS_KVR = WS_GT + (16u << 10);
static constexpr unsigned WS_YT  = WS_KVR + (32u << 10);
static constexpr unsigned WS_E   = WS_YT + (9u << 18);
static constexpr unsigned WS_CNT = WS_E + 4096;
static constexpr unsigned CS = CSTR;
static constexpr unsigned WS_F0  = WS_CNT + 128 * CS;
static constexpr unsigned WS_F1  = WS_F0 + 8 * 128;
static constexpr unsigned WS_F2K = WS_F1 + 8 * 128;
static constexpr unsigned WS_F2Q = WS_F2K + 8 * 128;
static constexpr unsigned WS_F3  = WS_F2Q + 16 * 128;
static constexpr unsigned WS_F5  = WS_F3 + 16 * 128;
static constexpr unsigned WS_END = WS_F5 + 8 * 128;
static constexpr unsigned SENT = 0xffffffffu;
static constexpr int SPECIAL = 255;

static constexpr int L_WO = 0, L_Y = 65536, L_FFT = 131072, L_ATT = 147456, L_FL = 163840 - 256;

DI unsigned pk(float lo, float hi) { f32x2 p = {lo, hi}; return __builtin_bit_cast(unsigned, __builtin_convertvector(p, bfx2)); }
DI float bflo(unsigned u) { return __builtin_bit_cast(float, u << 16); }
DI float bfhi(unsigned u) { return __builtin_bit_cast(float, u & 0xffff0000u); }
DI float rbf(float v) { return bflo(pk(v, 0.f)); }
DI s16x8 as8(u32x4 v) { return __builtin_bit_cast(s16x8, v); }
DI s16x4 as4(u32x2 v) { return __builtin_bit_cast(s16x4, v); }
DI f32x4 mma(s16x8 a, s16x8 b, f32x4 c) { return __builtin_amdgcn_mfma_f32_16x16x32_bf16(a, b, c, 0, 0, 0); }
DI f32x4 mma16(s16x4 a, s16x4 b, f32x4 c) { return __builtin_amdgcn_mfma_f32_16x16x16bf16_1k(a, b, c, 0, 0, 0); }
DI void mfa(f32x4& c, s16x8 a, s16x8 b) { asm volatile("v_mfma_f32_16x16x32_bf16 %0, %1, %2, %0" : "+v"(c) : "v"(a), "v"(b)); }
DI s16x4 trd(const char* l) { return __builtin_amdgcn_ds_read_tr16_b64_v4i16((LAS s16x4*)l); }
DI s16x8 cat(s16x4 a, s16x4 b) { return __builtin_shufflevector(a, b, 0, 1, 2, 3, 4, 5, 6, 7); }
DI s16x8 trd2(const char* l0, const char* l1) { return cat(trd(l0), trd(l1)); }
DI s16x8 lrd(const char* l) { return *(const s16x8*)l; }
DI float fsin(float rev) { return __builtin_amdgcn_sinf(rev); }
DI float fcos(float rev) { return __builtin_amdgcn_cosf(rev); }
DI unsigned laddr_of(const char* l) { return (unsigned)(size_t)(const LAS char*)l; }
DI void glds4(unsigned laddr, const char* sb, unsigned v0, unsigned v1, unsigned v2, unsigned v3) {
  asm volatile("s_mov_b32 m0, %0\n\ts_nop 0\n\t"
               "global_load_lds_dwordx4 %2, %1\n\t"
               "global_load_lds_dwordx4 %3, %1 offset:1024\n\t"
               "global_load_lds_dwordx4 %4, %1 offset:2048\n\t"
               "global_load_lds_dwordx4 %5, %1 offset:3072"
               :: "s"(laddr), "s"(sb), "v"(v0), "v"(v1), "v"(v2), "v"(v3) : "memory");
}
DI void glds4c(unsigned laddr, const char* sb, unsigned v0, unsigned v1, unsigned v2, unsigned v3) {
  asm volatile("s_mov_b32 m0, %0\n\ts_nop 0\n\t"
               "global_load_lds_dwordx4 %2, %1 sc1\n\t"
               "global_load_lds_dwordx4 %3, %1 offset:1024 sc1\n\t"
               "global_load_lds_dwordx4 %4, %1 offset:2048 sc1\n\t"
               "global_load_lds_dwordx4 %5, %1 offset:3072 sc1"
               :: "s"(laddr), "s"(sb), "v"(v0), "v"(v1), "v"(v2), "v"(v3) : "memory");
}
DI int perm32(int t, int i) { return 8 * (i >> 2) + 4 * t + (i & 3); }
DI float phi(float t) {
  float e = __builtin_amdgcn_exp2f(t * 1.44269504088896341f);
  e = __builtin_amdgcn_fmed3f(e, 0.f, 1.f);
  return fmaxf(t + 1e-6f, 1e-6f) + e;
}
struct TsT { unsigned long long r[16]; };
#define TSP TsT& ts_,
#define TSA ts_,
#define TSDECL TsT ts_; for (int q_ = 0; q_ < 16; q_++) ts_.r[q_] = 0;
#define TS(slot) do { ts_.r[slot] = __builtin_amdgcn_s_memrealtime(); } while (0)
#define TSFLUSH() do { const int ii_ = blockIdx.x >> 3; const int cls_ = ii_ >= 24 ? 2 : ((ii_ >> 2) < 4 ? 0 : 1); \
  if ((int)(threadIdx.x >> 8) == PROBE_K && (PROBE_CLS == 9 || cls_ == PROBE_CLS) && blockIdx.x != PROBE_SKIP && ts_.r[PROBE_S] != 0) { const unsigned long long tgt_ = ts_.r[PROBE_S] + 3000ull; \
    for (int sp_ = 0; sp_ < 100000 && __builtin_amdgcn_s_memrealtime() < tgt_; sp_++) __builtin_amdgcn_s_sleep(1); } } while (0)
#define SCHED() __builtin_amdgcn_sched_barrier(0)
#define SGB(mask, n) __builtin_amdgcn_sched_group_barrier(mask, n, 0)
#define PIN(x) asm volatile("" : "+v"(x))
#define WAITV(n) asm volatile("s_waitcnt vmcnt(" #n ")" ::: "memory")
#define BAR() asm volatile("s_waitcnt lgkmcnt(0)\n\ts_barrier" ::: "memory")
#define FENCE() asm volatile("" ::: "memory")

typedef __amdgpu_buffer_rsrc_t rsrc_t;
DI u32x4 ldc(rsrc_t r, unsigned off) { return __builtin_amdgcn_raw_buffer_load_b128(r, off, 0, 16); }
DI void stc(rsrc_t r, unsigned off, u32x4 v) { __builtin_amdgcn_raw_buffer_store_b128(v, r, off, 0, 16); }
DI void stc1(rsrc_t r, unsigned off, unsigned v) { __builtin_amdgcn_raw_buffer_store_b32(v, r, off, 0, 16); }
template <int SL> DI void gwait(rsrc_t r, unsigned off, unsigned ep) {
#pragma unroll 1
  for (int sp = 0; sp < 200000; sp++) {
    FENCE();
    const u32x4 f = ldc(r, off);
    if (__all(f[0] == ep && f[1] == ep && f[2] == ep && f[3] == ep)) break;
    __builtin_amdgcn_s_sleep(SL);
  }
  FENCE();
}
DI unsigned ticket(rsrc_t r, int c, int lane) {
  unsigned old = 0;
  if (lane == 0) old = __builtin_amdgcn_raw_ptr_buffer_atomic_add_i32(1, r, WS_CNT + c * CS, 0, 16);
  old = __builtin_amdgcn_readfirstlane(old);
  FENCE();
  return old;
}
DI void arrive(rsrc_t r, int c, unsigned n, unsigned flag, int nrep, unsigned ep, int lane) {
  if (ticket(r, c, lane) == n - 1 && lane < nrep) stc(r, flag + lane * 128, u32x4{ep, ep, ep, ep});
}
DI void linc(char* p, int lane) { if (lane == 0) asm volatile("ds_add_u32 %0, %1" :: "v"(laddr_of(p)), "v"(1u) : "memory"); }
DI void lset(char* p) { asm volatile("ds_write_b32 %0, %1" :: "v"(laddr_of(p)), "v"(1u) : "memory"); }
DI void lwait(const char* p, unsigned want) {
#pragma unroll 1
  for (int sp = 0; sp < 4000000; sp++) {
    if ((unsigned)__builtin_amdgcn_readfirstlane(*(const volatile LAS unsigned*)p) >= want) break;
    __builtin_amdgcn_s_sleep(1);
  }
  FENCE();
}

DI void epoch_dma(char* lds, const char* p, int lane) {
  if (lane == 0) asm volatile("s_mov_b32 m0, %0\n\ts_nop 0\n\tglobal_load_lds_dword %2, %1" :: "s"(laddr_of(lds) + L_FL - 16), "s"(p), "v"(0u) : "memory");
}
DI unsigned epoch_get(const char* lds) { return (unsigned)__builtin_amdgcn_readfirstlane(*(const volatile LAS unsigned*)(lds + L_FL - 16)) + 1u; }
DI bool has_sent(u32x4 v) { return v[0] == SENT || v[1] == SENT || v[2] == SENT || v[3] == SENT; }
DI unsigned ticket_issue(rsrc_t r, int c, int lane) {
  unsigned old = 0;
  if (lane == 0) old = __builtin_amdgcn_raw_ptr_buffer_atomic_add_i32(1, r, WS_CNT + c * CS, 0, 16);
  return old;
}
DI void ticket_done(rsrc_t r, unsigned old, unsigned n, unsigned flag, int nrep, unsigned ep, int lane) {
  if ((unsigned)__builtin_amdgcn_readfirstlane(old) == n - 1 && lane < nrep) stc(r, flag + lane * 128, u32x4{ep, ep, ep, ep});
}

DI void wo_dma(char* lds, const char* wo, int ntile, int j, int w, int lane) {
  const int blk = ((lane & 7) >> 1) ^ ((lane >> 4) & 3);
  const unsigned v = (lane >> 3) * 1024 + (blk & 1) * 32 + (blk >> 1) * 512 + (lane & 1) * 16;
  glds4(laddr_of(lds) + L_WO + j * 16384 + w * 4096, wo + (size_t)(j * 128 + w * 32) * 1024 + ntile * 64, v, v + 7168, v + 2 * 7168, v + 3 * 7168);
}

DI void p1_prod(char* lds, const char* x, const char* wqkv, const char* wo, int ntile, int m, int col0, int col1, int rot, int w, int lane) {
  unsigned voffA[4], voffB[4];
#pragma unroll
  for (int j = 0; j < 4; j++) {
    const int par = j & 1;
    voffA[j] = (lane >> 3) * 1024 + (((lane & 7) ^ ((4 * par + (lane >> 4)) & 7)) * 16) + j * (8192 - 1024);
    voffB[j] = (lane >> 3) * 3072 + ((((lane & 7) >> 1) ^ (((lane >> 4) & 1) | (par << 1))) * 32) + (lane & 1) * 16 + j * (24576 - 1024);
  }
  const unsigned lbase = laddr_of(lds) + w * 4096;
  const char* xa = x + (size_t)(m * 128 + w * 32) * 1024;
  const char* wb = wqkv + (size_t)((w & 1) * 32) * 3072 + ((w >> 1) ? col1 : col0) * 2;
  auto issue = [&](int idx) {
    const int ks = (idx + rot) & 7;
    const unsigned slot = lbase + (idx & 3) * 32768;
    glds4(slot, xa + ks * 128, voffA[0], voffA[1], voffA[2], voffA[3]);
    glds4(slot + 16384, wb + (size_t)(ks * 64) * 3072, voffB[0], voffB[1], voffB[2], voffB[3]);
  };
  issue(0); issue(1);
  WAITV(8); BAR();
  issue(2); issue(3);
#pragma unroll 1
  for (int j = 1; j < 5; j++) { WAITV(16); BAR(); issue(j + 3); }
  WAITV(16); BAR();
  wo_dma(lds, wo, ntile, 0, w, lane);
  WAITV(12); BAR();
  wo_dma(lds, wo, ntile, 1, w, lane);
  WAITV(8); BAR();
}

template <bool QT>
DI void p1_gemm(TSP char* lds, rsrc_t rs, int m, int nt, unsigned& ep, const char* epp, int w, int lane) {
  const int i = lane & 15, g = lane >> 4, q = i >> 2, p = lane & 3;
  f32x4 acc[8][2];
#pragma unroll
  for (int a = 0; a < 8; a++) { acc[a][0] = f32x4{0, 0, 0, 0}; acc[a][1] = f32x4{0, 0, 0, 0}; }
  const int aoff = i * 128 + ((g ^ (i >> 1)) * 16);
  const int boff = 16384 + (8 * g + q) * 128 + ((w ^ (((q >> 1) & 1) | ((g & 1) << 1))) * 32) + p * 8;
  s16x8 A[2][2][8]; s16x4 B[2][2][2][2];
  auto rd1 = [&](int buf, const char* slot, int r) {
    const int ks = r / 12, rr = r % 12;
    const int t = rr == 0 ? 0 : rr == 1 ? 1 : rr == 3 ? 2 : rr == 4 ? 3 : -1;
    const int a = rr == 2 ? 0 : rr - 4;
    if (t >= 0) B[buf][ks][t >> 1][t & 1] = trd(slot + boff + (t >> 1) * 8192 + ks * 4096 + (t & 1) * 512);
    else A[buf][ks][a] = lrd(slot + (aoff ^ (ks * 64)) + a * 2048);
  };
  auto mf1 = [&](int buf, int ks, int mt, int hh) {
    const s16x8 bf = cat(B[buf][ks][hh][0], B[buf][ks][hh][1]);
    if (QT) mfa(acc[mt][hh], bf, A[buf][ks][mt]); else mfa(acc[mt][hh], A[buf][ks][mt], bf);
  };
  auto region = [&](int cur, const char* slot) {
#pragma unroll
    for (int mi = 0; mi < 32; mi++) {
      mf1(cur, mi >> 4, (mi & 15) >> 1, mi & 1);
      SCHED();
      if (mi < 24) { rd1(cur ^ 1, slot, mi); SCHED(); }
    }
  };
  if (w == 0) epoch_dma(lds, epp, lane);
  if (QT && blockIdx.x == 128 && w == 0) {
    const u32x4 sent4 = {SENT, SENT, SENT, SENT};
    stc1(rs, WS_CNT + lane * CS, 0u);
    if (lane < 4) stc1(rs, WS_CNT + (64 + lane) * CS, 0u);
#pragma unroll 1
    for (int j = 0; j < 24; j++) stc(rs, WS_KVR + (j * 64 + lane) * 16, sent4);
    WAITV(0);
    const unsigned e = epoch_get(lds);
    if (lane < 8) stc(rs, WS_F0 + lane * 128, u32x4{e, e, e, e});
  }
  __builtin_amdgcn_s_waitcnt(0xC07F);
  BAR();
  TS(1);
#pragma unroll
  for (int r = 0; r < 24; r++) rd1(0, lds, r);
  SCHED();
#pragma unroll 1
  for (int it = 0; it < 3; it++) {
    BAR(); SCHED();
    region(0, lds + ((2 * it + 1) & 3) * 32768);
    if (it == 1) WAITV(0);
    BAR(); SCHED();
    region(1, lds + ((2 * it + 2) & 3) * 32768);
  }
  ep = epoch_get(lds);
  BAR(); SCHED();
  region(0, lds + 3 * 32768);
  asm volatile("s_nop 7");
  SCHED();
  auto mmh = [&](int hh) {
#pragma unroll
    for (int mt = 0; mt < 8; mt++)
#pragma unroll
      for (int ks = 0; ks < 2; ks++) {
        const s16x8 bf = cat(B[1][ks][hh][0], B[1][ks][hh][1]);
        acc[mt][hh] = QT ? mma(bf, A[1][ks][mt], acc[mt][hh]) : mma(A[1][ks][mt], bf, acc[mt][hh]);
      }
  };
  mmh(0);
  SCHED();
  TS(2);
  if (QT) {
    mmh(1);
#pragma unroll
    for (int mt = 0; mt < 8; mt++) {
      u32x4 v = {pk(phi(acc[mt][0][0]), phi(acc[mt][0][1])), pk(phi(acc[mt][0][2]), phi(acc[mt][0][3])),
                 pk(phi(acc[mt][1][0]), phi(acc[mt][1][1])), pk(phi(acc[mt][1][2]), phi(acc[mt][1][3]))};
      stc(rs, WS_PQ + (((m * 8 + mt) * 16 + (nt - 8) * 4 + w) * 64 + lane) * 16, v);
    }
    lwait(lds + L_FL + 100, 1);
    WAITV(0);
    arrive(rs, 34 + m, 16, WS_F2Q + m * 128, 1, ep, lane);
  } else {
    mmh(1);
    u32x4 pa[4];
#pragma unroll
    for (int u = 0; u < 4; u++)
      pa[u] = u32x4{pk(phi(acc[2 * u][0][0]), phi(acc[2 * u][0][1])), pk(phi(acc[2 * u][0][2]), phi(acc[2 * u][0][3])),
                    pk(phi(acc[2 * u + 1][0][0]), phi(acc[2 * u + 1][0][1])), pk(phi(acc[2 * u + 1][0][2]), phi(acc[2 * u + 1][0][3]))};
#pragma unroll
    for (int t = 0; t < 16; t++) { SGB(0x008, 1); SGB(0x002, 2); }
    SCHED();
    f32x4 kv = {0, 0, 0, 0}, z = {0, 0, 0, 0};
    const u32x4 ones = {0x3f803f80u, 0x3f803f80u, 0x3f803f80u, 0x3f803f80u};
#pragma unroll
    for (int u = 0; u < 4; u++) {
      u32x4 b = {pk(acc[2 * u][1][0], acc[2 * u][1][1]), pk(acc[2 * u][1][2], acc[2 * u][1][3]),
                 pk(acc[2 * u + 1][1][0], acc[2 * u + 1][1][1]), pk(acc[2 * u + 1][1][2], acc[2 * u + 1][1][3])};
      z = mma(as8(pa[u]), as8(ones), z);
      kv = mma(as8(pa[u]), as8(b), kv);
    }
    const int head = nt * 4 + w;
    u32x4 v = {pk(kv[0], kv[1]), pk(kv[2], kv[3]), pk(z[0], z[1]), pk(z[2], z[3])};
    stc(rs, WS_KVP + ((m * 32 + head) * 64 + lane) * 16, v);
    lwait(lds + L_FL + 100, 1);
    WAITV(0);
    if (ticket(rs, 1 + head, lane) == 15) {
      TS(4);
      u32x4 kl[16];
#pragma unroll
      for (int pp = 0; pp < 16; pp++) kl[pp] = ldc(rs, WS_KVP + ((pp * 32 + head) * 64 + lane) * 16);
      FENCE();
      const unsigned tk = ticket_issue(rs, 33, lane);
      u32x4 r;
#pragma unroll
      for (int d = 0; d < 4; d++) {
        float lo[4], hi[4];
#pragma unroll
        for (int j = 0; j < 4; j++) {
          lo[j] = (bflo(kl[4 * j][d]) + bflo(kl[4 * j + 1][d])) + (bflo(kl[4 * j + 2][d]) + bflo(kl[4 * j + 3][d]));
          hi[j] = (bfhi(kl[4 * j][d]) + bfhi(kl[4 * j + 1][d])) + (bfhi(kl[4 * j + 2][d]) + bfhi(kl[4 * j + 3][d]));
        }
        r[d] = pk((lo[0] + lo[1]) + (lo[2] + lo[3]), (hi[0] + hi[1]) + (hi[2] + hi[3]));
      }
      ticket_done(rs, tk, 32, WS_F2K, 8, ep, lane);
      const int u = (head >> 3) * 4 + (head & 3), hh = (head >> 2) & 1;
      const bool zr = (i & 3) == hh + 2 * (u & 1);
      __builtin_amdgcn_raw_buffer_store_b64(u32x2{r[0], r[1]}, rs, WS_KVR + (u * 64 + lane) * 16 + hh * 8, 0, 16);
      if (zr) stc(rs, WS_KVR + 16384 + ((u >> 1) * 64 + lane) * 16, hh ? u32x4{0u, 0u, r[2], r[3]} : u32x4{r[2], r[3], 0u, 0u});
      TS(5);
    }
  }
}

DI float dpp_sum16(float v) {
  v += __builtin_bit_cast(float, __builtin_amdgcn_update_dpp(0, __builtin_bit_cast(int, v), 0x128, 0xf, 0xf, false));
  v += __builtin_bit_cast(float, __builtin_amdgcn_update_dpp(0, __builtin_bit_cast(int, v), 0x124, 0xf, 0xf, false));
  v += __builtin_bit_cast(float, __builtin_amdgcn_update_dpp(0, __builtin_bit_cast(int, v), 0x122, 0xf, 0xf, false));
  v += __builtin_bit_cast(float, __builtin_amdgcn_update_dpp(0, __builtin_bit_cast(int, v), 0x121, 0xf, 0xf, false));
  return v;
}

DI void p1_rows(TSP char* lds, const char* x, const char* gw, u32x4& gwv, rsrc_t rs, int a, int w, int lane) {
  const int i = lane & 15, g = lane >> 4;
  char* LX = lds;
  char* LS = lds + 32768;
  const char* LC = lds + 106496;
  const int amt = (a >> 1) & 1;
  {
    const unsigned v = lane * 16;
    const char* src = x + (size_t)(128 * (a >> 2) + perm32(amt, 8 * (a & 1) + w)) * 1024;
    glds4(laddr_of(LX) + w * 4096, src, v, v + 31 * 1024, v + 62 * 1024, v + 93 * 1024);
    glds4(laddr_of(LX) + w * 4096 + 16384, src + 8192, v, v + 31 * 1024, v + 62 * 1024, v + 93 * 1024);
  }
  int soff[4];
#pragma unroll
  for (int r = 0; r < 4; r++) {
    const int k2 = 4 * g + r;
    const int k = i + 32 * k2;
    const int col = (i == 0) ? 16 * k2 : (k2 < 8 ? k : 512 - k);
    soff[r] = col * 128 + ((w ^ (col & 7)) * 16);
    PIN(soff[r]);
  }
  TS(12);
  BAR();
  TS(13);
  const u32x4 b1re = *(const u32x4*)(LC + lane * 16), b1im = *(const u32x4*)(LC + 1024 + lane * 16);
  const u32x4 a3re = *(const u32x4*)(LC + 2048 + lane * 16), a3im = *(const u32x4*)(LC + 3072 + lane * 16);
  const u32x4 a4re = *(const u32x4*)(LC + 4096 + lane * 16), a4im = *(const u32x4*)(LC + 5120 + lane * 16);
  const f32x4 twc = *(const f32x4*)(LC + 6144 + lane * 16), tws = *(const f32x4*)(LC + 7168 + lane * 16);
  WAITV(0);
  TS(1);
#pragma unroll 1
  for (int hf = 0; hf < 2; hf++) {
    unsigned o[4][4];
#pragma unroll
    for (int a4 = 0; a4 < 4; a4++) {
      const char* row = LX + ((w + 4 * hf) * 4 + a4) * 1024;
      s16x8 xf = trd2(row + lane * 8, row + 512 + lane * 8);
      const f32x4 z4 = {0, 0, 0, 0};
      f32x4 are = mma(xf, as8(b1re), z4), aim = mma(xf, as8(b1im), z4);
      f32x4 tr = are * twc + aim * tws, ti = aim * twc - are * tws;
      asm("" : "+v"(tr), "+v"(ti));
      u32x4 bb;
#pragma unroll
      for (int r = 0; r < 4; r++) bb[r] = pk(tr[r], ti[r]);
      f32x4 xre = mma(as8(a3re), as8(bb), z4), xim = mma(as8(a3im), as8(bb), z4);
      f32x4 sre = mma(as8(a4re), as8(bb), z4), sim = mma(as8(a4im), as8(bb), z4);
#pragma unroll
      for (int r = 0; r < 4; r++) o[r][a4] = pk((i == 0) ? sre[r] : xre[r], (i == 0) ? sim[r] : xim[r]);
    }
    if (lane == 0) {
      u32x4 v = {o[0][0] >> 16, o[0][1] >> 16, o[0][2] >> 16, o[0][3] >> 16};
      *(u32x4*)(LS + 256 * 128 + (w + 4 * hf) * 16) = v;
#pragma unroll
      for (int a4 = 0; a4 < 4; a4++) o[0][a4] &= 0xffffu;
    }
#pragma unroll
    for (int r = 0; r < 4; r++) {
      u32x4 v = {o[r][0], o[r][1], o[r][2], o[r][3]};
      *(u32x4*)(LS + (soff[r] ^ (hf * 64))) = v;
    }
  }
  TS(14);
  linc(lds + L_FL + 108, lane);
  gwv = *(const u32x4*)(gw + lane * 16);
  lwait(lds + L_FL + 108, 4);
  TS(2);
  const int sl = lane & 7;
  const int sc = w * 8 + (lane >> 3);
  const int xline = (amt * 4 + (a >> 4)) * 1024 + ((a >> 2) & 3) * 256 + (a & 1) * 128;
  u32x4 v0[8];
#pragma unroll
  for (int j = 0; j < 8; j++) v0[j] = *(const u32x4*)(LS + (j * 32 + sc) * 128 + ((sl ^ (sc & 7)) * 16));
#pragma unroll
  for (int j = 0; j < 8; j++) stc(rs, WS_X1T + (j * 32 + sc) * CP + xline + sl * 16, v0[j]);
  if (w == 3 && lane < 8) stc(rs, WS_X1T + 256 * CP + xline + lane * 16, *(const u32x4*)(LS + 256 * 128 + lane * 16));
  TS(15);
}

DI void p1_filt(TSP char* lds, const char* wo, const char* wr, const char* wi, rsrc_t rs, int a, const char* epp, int w, int lane) {
  const int i = lane & 15, g = lane >> 4;
  char* LF = lds + 69632;
  if (w == 0) epoch_dma(lds, epp, lane);
  {
    const float fi = (float)i;
    char* cp = lds + 106496 + lane * 16 + w * 4;
    float c[2], s[2];
#pragma unroll
    for (int e = 0; e < 2; e++) {
      const float ang = (float)(4 * g + 16 * (w >> 1) + 2 * (w & 1) + e) * (fi * (1.f / 32.f));
      c[e] = fcos(ang);
      s[e] = (i == 0) ? (e ? -1.f : 1.f) : fsin(-ang);
    }
    *(unsigned*)(cp) = pk(c[0], c[1]);
    *(unsigned*)(cp + 1024) = pk(s[0], s[1]);
    const float n2i = (float)(4 * g + w) * fi;
    {
      const float ang = n2i * (1.f / 16.f);
      const float cs = fcos(ang), sn = fsin(ang);
      *(unsigned*)(cp + 2048) = pk(cs, sn);
      *(unsigned*)(cp + 3072) = pk(sn, cs) ^ ((i >= 8) ? 0x80000000u : 0x00008000u);
    }
    {
      const float ang = n2i * (1.f / 32.f);
      const float cs = fcos(ang);
      const float sn = (i == 0) ? ((w & 1) ? -1.f : 1.f) : fsin(-ang);
      const unsigned u = pk(cs, sn);
      const int sh = (i & 1) * 16;
      *(unsigned*)(cp + 4096) = (u & 0xffffu) << sh;
      *(unsigned*)(cp + 5120) = (u >> 16) << sh;
    }
    *(float*)(cp + 6144) = fcos(n2i * (1.f / 512.f));
    *(float*)(cp + 7168) = fsin(n2i * (1.f / 512.f));
  }
  TS(12);
  BAR();
  {
    const unsigned v = lane * 16;
    const size_t off = (size_t)(8 * (a & 7) + 64 * (4 * (a >> 3) + w)) * 514;
    glds4(laddr_of(LF) + w * 4352, wr + off, v, v, v, v);
    glds4(laddr_of(LF) + (w + 4) * 4352, wi + off, v, v, v, v);
    if (lane == 0) {
      asm volatile("s_mov_b32 m0, %0\n\ts_nop 0\n\tglobal_load_lds_dwordx4 %2, %1" :: "s"(laddr_of(LF) + w * 4352 + 4096), "s"(wr + off + 4096), "v"(v) : "memory");
      asm volatile("s_mov_b32 m0, %0\n\ts_nop 0\n\tglobal_load_lds_dwordx4 %2, %1" :: "s"(laddr_of(LF) + (w + 4) * 4352 + 4096), "s"(wi + off + 4096), "v"(v) : "memory");
    }
  }
  const int sl = lane & 7;
  asm volatile("s_mov_b32 m0, %0\n\ts_nop 0\n\tglobal_load_lds_dword %2, %1\n\tglobal_load_lds_dword %3, %1"
               :: "s"(laddr_of(lds) + L_ATT + 8192), "s"(wo), "v"((unsigned)(((a & 7) * 4 + w) * 64 + lane) * 128u), "v"((unsigned)(((a & 7) * 4 + w) * 64 + lane + 2048) * 128u) : "memory");
  WAITV(2);
  linc(lds + L_FL + 112, lane);
  lwait(lds + L_FL + 112, 4);
  TS(1);
  const int wline = ((((a & 7) >> 1) * 2 + ((a >> 3) & 1)) * 64 + (a >> 4) * 16 + (a & 1) * 8) * 16;
#pragma unroll 1
  for (int hf = 0; hf < 2; hf++) {
    const int cb = (lane >> 3) * 16 + w + 128 * hf;
    const char* fp = LF + sl * 514 + cb * 2;
#pragma unroll
    for (int j = 0; j < 4; j++) {
      u32x4 fv;
#pragma unroll
      for (int r = 0; r < 4; r++) fv[r] = (unsigned)*(const unsigned short*)(fp + r * 4352 + j * 8) | ((unsigned)*(const unsigned short*)(fp + (4 + r) * 4352 + j * 8) << 16);
      stc(rs, WS_WT + (cb + 4 * j) * CP + wline + sl * 16, fv);
    }
  }
  if (w == 3 && lane < 8) {
    u32x4 v;
#pragma unroll
    for (int r = 0; r < 4; r++) v[r] = (unsigned)*(const unsigned short*)(LF + sl * 514 + 512 + r * 4352) | ((unsigned)*(const unsigned short*)(LF + sl * 514 + 512 + (4 + r) * 4352) << 16);
    stc(rs, WS_WT + 256 * CP + wline + sl * 16, v);
  }
}

DI void p1_gate(char* lds, u32x4 gwv, rsrc_t rs, int a, int w, int lane) {
  const int i = lane & 15, g = lane >> 4;
  const u32x4 ones = {0x3f803f80u, 0x3f803f80u, 0x3f803f80u, 0x3f803f80u};
#pragma unroll 1
  for (int hf = 0; hf < 2; hf++) {
    float gsum[4];
#pragma unroll
    for (int j = 0; j < 4; j++) {
      u32x4 xv = *(const u32x4*)(lds + ((w + 4 * hf) * 4 + j) * 1024 + lane * 16);
      u32x4 pr;
#pragma unroll
      for (int d = 0; d < 4; d++) pr[d] = pk(bflo(xv[d]) * bflo(gwv[d]), bfhi(xv[d]) * bfhi(gwv[d]));
      gsum[j] = mma(as8(ones), as8(pr), f32x4{0, 0, 0, 0})[0];
    }
    float gs = (g == 0) ? gsum[0] : (g == 1) ? gsum[1] : (g == 2) ? gsum[2] : gsum[3];
    gs = rbf(dpp_sum16(gs));
    const float gg = rbf(__builtin_amdgcn_rcpf(1.f + __builtin_amdgcn_exp2f(gs * -1.44269504088896341f)));
    const int s = 32 * (4 * (a >> 2) + g) + perm32((a >> 1) & 1, 8 * (a & 1) + w + 4 * hf);
    if (i == 0) stc1(rs, WS_GT + (s >> 6) * 256 + (s & 15) * 16 + ((s >> 4) & 3) * 4, pk(gg, 1.f - gg));
  }
}

template <class F> DI void p2_fft(TSP F idle, char* lb, char* fl, char* f1seen, const char* ws, rsrc_t rs, int c, int cnt, int rep, unsigned& ep, int w, int lane) {
  const int i = lane & 15, g = lane >> 4;
  const int k1 = 16 * w + i;
  const float fk1 = (float)k1, fg = (float)g;
  u32x4 fre[4], fim[4], fc[2], fs[2], fn[2];
  {
    const float st = fk1 * (1.f / 64.f);
    const float a0 = fg * 4.f * st;
    const bool odd = i & 1;
    const unsigned m2 = odd ? 0x80008000u : 0u;
    const unsigned mre = (i & 2) ? 0x80008000u : 0u;
    const unsigned mim = ((i + 1) & 2) ? 0x80008000u : 0u;
#pragma unroll
    for (int d = 0; d < 4; d++) {
      const float ang = __builtin_fmaf((float)d, st, a0);
      const float cs = fcos(ang), sn = fsin(ang);
      const unsigned r0 = pk(cs, sn), i0 = pk(sn, cs) ^ 0x8000u;
      const unsigned r1 = (odd ? i0 : r0) ^ mre, i1 = (odd ? r0 : i0) ^ mim;
      fre[0][d] = r0; fim[0][d] = i0;
      fre[1][d] = r1; fim[1][d] = i1;
      fre[2][d] = r0 ^ m2; fim[2][d] = i0 ^ m2;
      fre[3][d] = r1 ^ m2; fim[3][d] = i1 ^ m2;
    }
#pragma unroll
    for (int ks = 0; ks < 4; ks++) { PIN(fre[ks]); PIN(fim[ks]); }
  }
  idle(0);
  float c1[2][4], s1[2][4], c2[2][4], s2[2][4];
#pragma unroll
  for (int mt = 0; mt < 2; mt++) {
    const float fa = (float)perm32(mt, i);
    const float st = fa * (1.f / 32.f), a0 = fg * 8.f * st;
    const float st1 = fk1 * (1.f / 2048.f), b0 = (fg * 8.f + (float)(4 * mt)) * st1;
    const float st2 = fa * (1.f / 2048.f), d0 = (fg * 4.f + (float)(16 * w)) * st2;
#pragma unroll
    for (int d = 0; d < 4; d++) {
      const float x0 = __builtin_fmaf((float)(2 * d), st, a0), x1 = __builtin_fmaf((float)(2 * d + 1), st, a0);
      fc[mt][d] = pk(fcos(x0), fcos(x1));
      fs[mt][d] = pk(fsin(x0), fsin(x1));
      fn[mt][d] = fs[mt][d] ^ 0x80008000u;
      const float t1 = __builtin_fmaf((float)d, st1, b0);
      c1[mt][d] = fcos(t1); s1[mt][d] = fsin(t1);
      const float t2 = __builtin_fmaf((float)d, st2, d0);
      c2[mt][d] = fcos(t2); s2[mt][d] = fsin(t2);
      PIN(c1[mt][d]); PIN(s1[mt][d]); PIN(c2[mt][d]); PIN(s2[mt][d]);
    }
    PIN(fc[mt]); PIN(fs[mt]); PIN(fn[mt]);
    idle(1 + mt);
  }
  const unsigned yt = WS_YT + (32 * k1 + 8 * g) * 2 + ((c == 256) ? 1 : (c & 1) * 256 + (c & ~1)) * YP;
  stc(rs, yt, u32x4{SENT, SENT, SENT, SENT});
  if (c & 255) stc(rs, yt + YP, u32x4{SENT, SENT, SENT, SENT});
  idle(3);
  TS(4);
  if (w == 0) { gwait<SLP>(rs, WS_F1 + rep * 128, ep); lset(f1seen); }
  else lwait(f1seen, 1);
  TS(5);
  asm volatile("s_mov_b32 m0, %0\n\ts_nop 0\n\tglobal_load_lds_dwordx4 %2, %1 sc1\n\tglobal_load_lds_dwordx4 %2, %1 offset:1024 sc1"
               :: "s"(laddr_of(lb) + 8192 + w * 2048), "s"(ws + WS_X1T + (size_t)c * CP + w * 2048), "v"(lane * 16) : "memory");
  u32x4 wf[2];
#pragma unroll
  for (int mt = 0; mt < 2; mt++) wf[mt] = ldc(rs, WS_WT + c * CP + ((w * 2 + mt) * 64 + lane) * 16);
  WAITV(2);
  linc(fl, lane);
  const unsigned tk = ticket_issue(rs, 50 + w * 4 + cnt, lane);
  lwait(fl, 4);
  TS(6);
  const f32x4 z4 = {0, 0, 0, 0};
  f32x4 re[2], im[2];
#pragma unroll
  for (int mt = 0; mt < 2; mt++) { re[mt] = z4; im[mt] = z4; }
#pragma unroll
  for (int ks = 0; ks < 4; ks++)
#pragma unroll
    for (int mt = 0; mt < 2; mt++) {
      s16x8 xa = lrd(lb + 8192 + ((mt * 4 + ks) * 64 + lane) * 16);
      re[mt] = mma(xa, as8(fre[ks]), re[mt]); im[mt] = mma(xa, as8(fim[ks]), im[mt]);
    }
  u32x4 pr, pi;
#pragma unroll
  for (int mt = 0; mt < 2; mt++) {
    float tr[4], ti[4];
#pragma unroll
    for (int r = 0; r < 4; r++) { tr[r] = re[mt][r] * c1[mt][r] + im[mt][r] * s1[mt][r]; ti[r] = im[mt][r] * c1[mt][r] - re[mt][r] * s1[mt][r]; }
    pr[2 * mt] = pk(tr[0], tr[1]); pr[2 * mt + 1] = pk(tr[2], tr[3]);
    pi[2 * mt] = pk(ti[0], ti[1]); pi[2 * mt + 1] = pk(ti[2], ti[3]);
  }
#pragma unroll
  for (int mt = 0; mt < 2; mt++) {
    re[mt] = mma(as8(fs[mt]), as8(pi), mma(as8(fc[mt]), as8(pr), z4));
    im[mt] = mma(as8(fn[mt]), as8(pr), mma(as8(fc[mt]), as8(pi), z4));
  }
#pragma unroll
  for (int mt = 0; mt < 2; mt++) {
    float tr[4], ti[4];
#pragma unroll
    for (int r = 0; r < 4; r++) {
      const float a = bflo(wf[mt][r]), b = bfhi(wf[mt][r]);
      tr[r] = re[mt][r] * a - im[mt][r] * b; ti[r] = re[mt][r] * b + im[mt][r] * a;
    }
    pr[2 * mt] = pk(tr[0], tr[1]); pr[2 * mt + 1] = pk(tr[2], tr[3]);
    pi[2 * mt] = pk(ti[0], ti[1]); pi[2 * mt + 1] = pk(ti[2], ti[3]);
  }
#pragma unroll
  for (int nt = 0; nt < 2; nt++) {
    re[nt] = mma(as8(pi), as8(fn[nt]), mma(as8(pr), as8(fc[nt]), z4));
    im[nt] = mma(as8(pi), as8(fc[nt]), mma(as8(pr), as8(fs[nt]), z4));
  }
#pragma unroll
  for (int nt = 0; nt < 2; nt++) {
    u32x4 v;
#pragma unroll
    for (int r = 0; r < 4; r++) v[r] = pk(re[nt][r] * c2[nt][r] - im[nt][r] * s2[nt][r], -re[nt][r] * s2[nt][r] - im[nt][r] * c2[nt][r]);
    *(u32x4*)(lb + ((nt * 4 + w) * 64 + lane) * 16) = v;
  }
  linc(fl + 4, lane);
  lwait(fl + 4, 4);
#pragma unroll
  for (int mt = 0; mt < 2; mt++) { re[mt] = z4; im[mt] = z4; }
#pragma unroll
  for (int ks = 0; ks < 4; ks++)
#pragma unroll
    for (int mt = 0; mt < 2; mt++) {
      s16x8 ya = lrd(lb + ((mt * 4 + ks) * 64 + lane) * 16);
      re[mt] = mma(ya, as8(fre[ks]), re[mt]); im[mt] = mma(ya, as8(fim[ks]), im[mt]);
    }
  u32x4 vr = {pk(re[0][0], re[0][1]), pk(re[0][2], re[0][3]), pk(re[1][0], re[1][1]), pk(re[1][2], re[1][3])};
  u32x4 vi = {pk(im[0][0], im[0][1]), pk(im[0][2], im[0][3]), pk(im[1][0], im[1][1]), pk(im[1][2], im[1][3])};
  const bool last = (unsigned)__builtin_amdgcn_readfirstlane(tk) == (cnt == 0 ? 64u : 63u);
  stc(rs, yt, vr);
  if (c & 255) stc(rs, yt + YP, vi);
  if (last && lane == 0) stc(rs, WS_F3 + (w * 4 + cnt) * 128, u32x4{ep, ep, ep, ep});
  TS(7);
}

extern "C" __global__ __launch_bounds__(512, 2) void gm_all(const char* x, const char* wqkv, const char* wr, const char* wi, const char* gw, const char* wo, char* ws, char* out) {
  __shared__ __attribute__((aligned(16))) char lds[163840];
  const int b = blockIdx.x, xcd = b & 7, ii = b >> 3;
  const int w8 = __builtin_amdgcn_readfirstlane(threadIdx.x >> 6), lane = threadIdx.x & 63;
  const int w = w8 & 3;
  TSDECL
  TS(0);
  if (ii < 2 && w8 < 4) {
    unsigned long long pc; int o0, o1;
    asm volatile("s_getpc_b64 %0\n.Lgm_pc%=:\n\ts_mov_b32 %1, gm_all-.Lgm_pc%=\n\ts_mov_b32 %2, gm_end-.Lgm_pc%=" : "=s"(pc), "=s"(o0), "=s"(o1));
    const unsigned off = (ii * 256 + threadIdx.x) * 128;
    if (off < (unsigned)(o1 - o0))
      asm volatile("s_mov_b32 m0, %0\n\ts_nop 0\n\tglobal_load_lds_dword %2, %1" :: "s"(laddr_of(lds) + L_ATT + 8192), "s"(pc + (long long)o0), "v"(off) : "memory");
  }
  unsigned ep;
  const char* epp = ws + WS_E;
  const rsrc_t rs = __builtin_amdgcn_make_buffer_rsrc(ws, 0, WS_END, 0x00020000);
  char* FL = lds + L_FL;
  const int ml = ii & 3, nl = ii >> 2;
  const int mtile = xcd * 4 + ml, ntile = nl;
  const bool aux = ii >= 24;
  {
    const int m = (xcd >> 1) * 4 + ml;
    const int nt = nl < 4 ? (xcd & 1) * 4 + nl : 8 + (xcd & 1) * 2 + (nl - 4);
    if (w8 < 4 || aux) {
      if (w8 == 0) *(LAS unsigned*)(FL + lane * 4) = 0;
      if (w8 >= 4) p1_filt(TSA lds, wo, wr, wi, rs, xcd * 8 + (ii - 24), epp, w, lane);
      else {
        __builtin_amdgcn_s_setprio(2);
        if (aux) {
          u32x4 gwv;
          p1_rows(TSA lds, x, gw, gwv, rs, xcd * 8 + (ii - 24), w, lane);
          p1_gate(lds, gwv, rs, xcd * 8 + (ii - 24), w, lane);
          WAITV(0);
          BAR();
          ep = epoch_get(lds);
          if (w == 1) arrive(rs, 0, 64, WS_F1, 8, ep, lane);
#pragma unroll 1
          for (int j = 0; j < 4; j++) wo_dma(lds, wo, ntile, j, w, lane);
        }
        else if (nl < 4) p1_gemm<false>(TSA lds, rs, m, nt, ep, epp, w, lane);
        else p1_gemm<true>(TSA lds, rs, m, nt, ep, epp, w, lane);
        __builtin_amdgcn_s_setprio(0);
      }
    } else {
      const int col0 = nl >= 4 ? (nt - 8) * 128 : 512 + nt * 64;
      const int col1 = nl >= 4 ? col0 + 64 : col0 + 512;
      p1_prod(lds, x, wqkv, wo, ntile, m, col0, col1, (2 * ml + nl) & ROT1, w, lane);
    }
  }
  TS(3);
  const int i = lane & 15, g = lane >> 4, q = i >> 2, p = lane & 3;
  const int fsw = (2 * g + (q >> 1)) & 3;
  if (w8 >= 4 || b == SPECIAL) {
    const bool pg = w8 >= 4;
    if (pg) {
      ep = epoch_get(lds);
      if (!aux) {
        if (w == 0) { gwait<SLP>(rs, WS_F0 + xcd * 128, ep); lset(FL + 100); }
        wo_dma(lds, wo, ntile, 2, w, lane); wo_dma(lds, wo, ntile, 3, w, lane);
      }
    }
    __builtin_amdgcn_s_setprio(1);
    auto idle = [&](int st) {
      if (st == 2 && pg && aux) { if (w == 0) gwait<SLP>(rs, WS_F0 + xcd * 128, ep); WAITV(0); BAR(); }
      if (st == 3 && !aux) { WAITV(0); linc(FL, lane); }
    };
    p2_fft(TSA idle, pg ? lds + L_FFT : lds + L_Y, pg ? FL + 4 : FL + 20, pg ? FL + 16 : FL + 28, ws, rs, pg ? b : 256, pg ? (b & 1) * 2 + (b >> 7) : 0, xcd, ep, w, lane);
  }
  if (w8 < 4) {
    linc(FL + 80, lane);
    if (aux) { if (b != SPECIAL) WAITV(0); linc(FL, lane); }
    TS(7);
    const int stg = 4 * mtile + w;
    bool kok = false;
    if (w == 0) {
#pragma unroll 1
      for (int sp = 0; sp < 200000; sp++) {
        FENCE();
        const bool ok = ldc(rs, (lane & 1) ? WS_F2K + xcd * 128 : WS_F2Q + (mtile >> 1) * 128)[0] == ep;
        const unsigned long long mk = __ballot(ok);
        kok = mk & 2;
        if (mk & 1) break;
        __builtin_amdgcn_s_sleep(SLP);
      }
      FENCE();
      if (kok) lset(FL + 88);
      lset(FL + 104);
    } else lwait(FL + 104, 1);
    u32x4 pq[4][4];
#pragma unroll
    for (int jj = 0; jj < 4; jj++)
#pragma unroll
      for (int u = 0; u < 4; u++) pq[jj][u] = ldc(rs, WS_PQ + (((4 * mtile + ((w + 1 + jj) & 3)) * 16 + 4 * w + u) * 64 + lane) * 16);
    lwait(FL, 4);
    s16x8 wf[4][4];
    {
      const int toffA = L_WO + w * 16384 + (4 * g + q) * 128 + (fsw * 32) + p * 8;
#pragma unroll
      for (int u = 0; u < 4; u++)
#pragma unroll
        for (int a = 0; a < 4; a++) { const char* pp = lds + (toffA ^ (32 * a)) + u * 2048; wf[u][a] = trd2(pp, pp + 8192); }
    }
    if (w == 0) { if (!kok) { gwait<SLP>(rs, WS_F2K + xcd * 128, ep); lset(FL + 88); } }
    else lwait(FL + 88, 1);
    TS(8);
    u32x4 kr[6];
#pragma unroll 1
    for (int sp = 0; sp < 20000; sp++) {
      FENCE();
      bool bad = false;
#pragma unroll
      for (int j = 0; j < 6; j++) kr[j] = ldc(rs, WS_KVR + ((j < 4 ? 4 * w + j : 12 + 2 * w + j) * 64 + lane) * 16);
#pragma unroll
      for (int j = 0; j < 6; j++) bad |= has_sent(kr[j]);
      if (!__any(bad)) break;
    }
    lwait(FL + 16, 1);
    const unsigned gv = __builtin_amdgcn_raw_buffer_load_b32(rs, WS_GT + mtile * 256 + i * 16 + w * 4, 0, 16);
    TS(13);
    const f32x4 z4 = {0, 0, 0, 0};
    const f32x4 eps = {1e-6f, 1e-6f, 1e-6f, 1e-6f};
    f32x4 acc[4];
    f32x4 nm[2][4][2], dn[2][4];
    auto nd = [&](int jj) {
#pragma unroll
      for (int u = 0; u < 4; u++) {
        dn[jj & 1][u] = mma(as8(kr[4 + (u >> 1)]), as8(pq[jj][u]), eps);
        nm[jj & 1][u][0] = mma16(as4(u32x2{kr[u][0], kr[u][1]}), as4(u32x2{pq[jj][u][0], pq[jj][u][1]}), z4);
        nm[jj & 1][u][1] = mma16(as4(u32x2{kr[u][2], kr[u][3]}), as4(u32x2{pq[jj][u][2], pq[jj][u][3]}), z4);
      }
    };
    u32x4 of[2][4];
    auto sc = [&](int jj) {
#pragma unroll
      for (int u = 0; u < 4; u++) {
        const f32x4 n0 = nm[jj & 1][u][0] * __builtin_amdgcn_rcpf(dn[jj & 1][u][2 * (u & 1)]);
        const f32x4 n1 = nm[jj & 1][u][1] * __builtin_amdgcn_rcpf(dn[jj & 1][u][2 * (u & 1) + 1]);
        of[jj & 1][u] = u32x4{pk(n0[0], n0[1]), pk(n0[2], n0[3]), pk(n1[0], n1[1]), pk(n1[2], n1[3])};
      }
    };
    auto pj = [&](int jj) {
#pragma unroll
      for (int u = 0; u < 4; u++)
#pragma unroll
        for (int a = 0; a < 4; a++) acc[a] = mma(wf[u][a], as8(of[jj & 1][u]), u ? acc[a] : z4);
      if (jj < 3) {
#pragma unroll
        for (int a = 0; a < 4; a++) *(f32x4*)(lds + L_WO + w * 16384 + (jj * 4 + a) * 1024 + lane * 16) = acc[a];
      }
    };
    nd(0); SCHED();
    nd(1); sc(0);
#pragma unroll
    for (int t = 0; t < 12; t++) { SGB(0x008, 1); SGB(0x002, 3); }
    SCHED();
#pragma unroll
    for (int jj = 0; jj < 3; jj++) {
      pj(jj); sc(jj + 1);
#pragma unroll
      for (int t = 0; t < 16; t++) { SGB(0x008, 1); SGB(0x002, 3); }
      SCHED();
      if (jj < 2) { nd(jj + 2); SCHED(); }
    }
    linc(FL + 92, lane);
    pj(3);
    TS(9);
    lwait(FL + 92, 4);
#pragma unroll
    for (int ww = 1; ww < 4; ww++)
#pragma unroll
      for (int a = 0; a < 4; a++) acc[a] += *(const f32x4*)(lds + L_WO + ((w + ww) & 3) * 16384 + ((3 - ww) * 4 + a) * 1024 + lane * 16);
    lwait(FL + 48, 4);
    TS(10);
    const float ga = bflo(gv), gb = bfhi(gv);
    unsigned pv[4][2];
#pragma unroll
    for (int a = 0; a < 2; a++) {
      const f32x4 t0 = *(const f32x4*)(lds + L_FFT + ((w * 4 + a) * 64 + lane) * 16);
      const f32x4 t1 = *(const f32x4*)(lds + L_FFT + ((w * 4 + a + 2) * 64 + lane) * 16);
      const f32x4 v0 = acc[a] * ga + (t0 + t1) * gb, v1 = acc[a + 2] * ga + (t0 - t1) * gb;
      pv[a][0] = pk(v0[0], v0[1]); pv[a][1] = pk(v0[2], v0[3]);
      pv[a + 2][0] = pk(v1[0], v1[1]); pv[a + 2][1] = pk(v1[2], v1[3]);
    }
#pragma unroll
    for (int a = 0; a < 2; a++) {
      auto s0 = __builtin_amdgcn_permlane16_swap(pv[2 * a][0], pv[2 * a + 1][0], false, false);
      auto s1 = __builtin_amdgcn_permlane16_swap(pv[2 * a][1], pv[2 * a + 1][1], false, false);
      u32x4 v = {s0[0], s1[0], s0[1], s1[1]};
      char* op = out + (size_t)(16 * stg + i) * 1024 + (256 * a + 32 * ntile + 16 * (g & 1) + 8 * (g >> 1)) * 2;
      asm volatile("global_store_dwordx4 %0, %1, off sc1\n\ts_nop 1" :: "v"(op), "v"(v) : "memory");
    }
    TS(11);
    TSFLUSH();
    return;
  }
  {
    const float sgn = (i & 1) ? -1.f : 1.f;
    const unsigned k0v = (i & 1) ? 0xb5803580u : 0x35803580u;
    const float fn = (float)(32 * ntile + 16 * (w & 1) + i) * (1.f / 512.f);
    const float t2 = 2.f * fcos(fn * 32.f);
    f32x2 c0[4], c1[4];
#pragma unroll
    for (int d = 0; d < 4; d++) {
      const float kf = (float)(4 * g + 128 * (w & 1) + (w >> 1)) + (float)(16 * (d >> 1) + 2 * (d & 1));
      const float a0 = kf * fn, a1 = (kf + 32.f) * fn;
      c0[d] = f32x2{fcos(a0), fsin(a0)} * 0x1p-19f;
      c1[d] = f32x2{fcos(a1), fsin(a1)} * 0x1p-19f;
    }
    u32x4 gf[8];
#pragma unroll
    for (int j = 0; j < 2; j++) {
#pragma unroll
      for (int h = 0; h < 2; h++) {
#pragma unroll
        for (int d = 0; d < 4; d++) {
          gf[4 * j + 2 * h][d] = pk(c0[d][0], c0[d][1]);
          gf[4 * j + 2 * h + 1][d] = pk(c1[d][0], c1[d][1]);
          c0[d] = c1[d] * t2 - c0[d];
        }
#pragma unroll
        for (int d = 0; d < 4; d++) c1[d] = c0[d] * t2 - c1[d];
      }
      gf[4 * j][0] = (w == j && g == 0) ? k0v : gf[4 * j][0];
      if (j == 0) {
        const float sg = (w & 1) ? sgn : 1.f;
#pragma unroll
        for (int d = 0; d < 4; d++) { c0[d] *= sg; c1[d] *= sg; }
      }
    }
#pragma unroll
    for (int k = 0; k < 8; k++) PIN(gf[k]);
    TS(8);
    gwait<SLP>(rs, WS_F3 + ((mtile >> 3) * 4 + w) * 128, ep);
    lwait(FL + 80, 4);
    linc(FL + 96, lane);
    TS(9);
    const int toffB = L_Y + (4 * g + q) * 128 + (fsw * 32) + p * 8;
    f32x4 accb[4];
    s16x8 tb_[2][2][4];
    auto rd = [&](int u) {
      const char* slot = lds + (w ^ (u >> 1)) * 16384 + (u & 1) * 8192;
#pragma unroll
      for (int ks = 0; ks < 2; ks++)
#pragma unroll
        for (int c = 0; c < 4; c++) tb_[u & 1][ks][c] = trd2(slot + (toffB ^ (32 * c)) + ks * 4096, slot + (toffB ^ (32 * c)) + ks * 4096 + 2048);
    };
    auto mm = [&](int u) {
#pragma unroll
      for (int ks = 0; ks < 2; ks++)
#pragma unroll
        for (int c = 0; c < 4; c++) accb[c] = mma(as8(gf[2 * u + ks]), tb_[u & 1][ks][c], accb[c]);
    };
#pragma unroll 1
    for (int sp = 0; sp < 20000; sp++) {
      const unsigned v = (lane >> 3) * YP + ((((lane & 7) >> 1) ^ ((lane >> 4) & 3)) * 32) + (lane & 1) * 16;
      const char* src = ws + WS_YT + (size_t)(w * 128) * YP + mtile * 128;
      const unsigned la = laddr_of(lds) + L_Y + w * 16384;
#pragma unroll
      for (int j = 0; j < 4; j++) glds4c(la + j * 4096, src + (size_t)j * (32 * YP), v, v + (8 * YP - 1024), v + 2 * (8 * YP - 1024), v + 3 * (8 * YP - 1024));
#pragma unroll
      for (int c = 0; c < 4; c++) accb[c] = f32x4{0, 0, 0, 0};
      WAITV(8);
      rd(0);
      WAITV(0);
      TS(10);
      rd(1); mm(0);
#pragma unroll
      for (int t = 0; t < 8; t++) { SGB(0x100, 2); SGB(0x008, 1); }
      SCHED();
      mm(1);
      const f32x4 s4 = (accb[0] + accb[1]) + (accb[2] + accb[3]);
      const float s1 = (s4[0] + s4[1]) + (s4[2] + s4[3]);
      if (!__any(s1 != s1)) break;
      bool bad = false;
#pragma unroll 1
      for (int j = 0; j < 16; j++) bad |= has_sent(*(const u32x4*)(lds + L_Y + w * 16384 + j * 1024 + lane * 16));
      if (!__any(bad)) break;
    }
    lset(FL + 32 + w * 4);
    lwait(FL + 32 + (w ^ 1) * 4, 1);
    rd(2);
    SCHED();
    rd(3); mm(2);
#pragma unroll
    for (int t = 0; t < 8; t++) { SGB(0x100, 2); SGB(0x008, 1); }
    SCHED();
    mm(3);
    lwait(FL + 96, 4);
    if (b == 0 && w == 1 && lane == 0) stc1(rs, WS_E, ep);
#pragma unroll
    for (int c = 0; c < 4; c++) *(f32x4*)(lds + L_FFT + ((c * 4 + w) * 64 + lane) * 16) = accb[c];
    linc(FL + 48, lane);
    TS(11);
    TSFLUSH();
  }
}

extern "C" __global__ void gm_end() {}

extern "C" void kernel_launch(void* const* d_in, const int* in_sizes, int n_in, void* d_out, int out_size, void* d_ws, size_t ws_size, hipStream_t stream) {
  gm_all<<<256, 512, 0, stream>>>((const char*)d_in[0], (const char*)d_in[1], (const char*)d_in[4], (const char*)d_in[5], (const char*)d_in[6],
                                  (const char*)d_in[2], (char*)d_ws, (char*)d_out);
}
```

```cpp
#include <hip/hip_runtime.h>
#include <stdint.h>

typedef short s16x4 __attribute__((ext_vector_type(4)));
typedef short s16x8 __attribute__((ext_vector_type(8)));
typedef float f32x4 __attribute__((ext_vector_type(4)));
typedef float f32x2 __attribute__((ext_vector_type(2)));
typedef unsigned u32x4 __attribute__((ext_vector_type(4)));
typedef unsigned u32x2 __attribute__((ext_vector_type(2)));
typedef __bf16 bfx2 __attribute__((ext_vector_type(2)));
#define LAS __attribute__((address_space(3)))
#define DI __device__ __forceinline__
#define ROT1 6
#define WE 0
#define QPF 0
#define AST 1
#define CSTR 4352
#define PITC 8320
#define PITY 4224
#define SLP 2

static constexpr unsigned CP = PITC, YP = PITY;
static constexpr unsigned WS_X1T = 0;
static constexpr unsigned WS_WT  = WS_X1T + (9u << 18);
static constexpr unsigned WS_PQ  = WS_WT + (9u << 18);
static constexpr unsigned WS_KVP = WS_PQ + (2u << 20);
static constexpr unsigned WS_GT  = WS_KVP + (512u << 10);
static constexpr unsigned WS_KVR = WS_GT + (16u << 10);
static constexpr unsigned WS_YT  = WS_KVR + (32u << 10);
static constexpr unsigned WS_E   = WS_YT + (9u << 18);
static constexpr unsigned WS_CNT = WS_E + 4096;
static constexpr unsigned CS = CSTR;
static constexpr unsigned WS_F0  = WS_CNT + 128 * CS;
static constexpr unsigned WS_F1  = WS_F0 + 8 * 128;
static constexpr unsigned WS_F2K = WS_F1 + 8 * 128;
static constexpr unsigned WS_F2Q = WS_F2K + 8 * 128;
static constexpr unsigned WS_F3  = WS_F2Q + 16 * 128;
static constexpr unsigned WS_F5  = WS_F3 + 16 * 128;
static constexpr unsigned WS_END = WS_F5 + 8 * 128;
static constexpr unsigned SENT = 0xffffffffu;
static constexpr int SPECIAL = 255;

static constexpr int L_WO = 0, L_Y = 65536, L_FFT = 131072, L_ATT = 147456, L_FL = 163840 - 256;

DI unsigned pk(float lo, float hi) { f32x2 p = {lo, hi}; return __builtin_bit_cast(unsigned, __builtin_convertvector(p, bfx2)); }
DI float bflo(unsigned u) { return __builtin_bit_cast(float, u << 16); }
DI float bfhi(unsigned u) { return __builtin_bit_cast(float, u & 0xffff0000u); }
DI float rbf(float v) { return bflo(pk(v, 0.f)); }
DI s16x8 as8(u32x4 v) { return __builtin_bit_cast(s16x8, v); }
DI s16x4 as4(u32x2 v) { return __builtin_bit_cast(s16x4, v); }
DI f32x4 mma(s16x8 a, s16x8 b, f32x4 c) { return __builtin_amdgcn_mfma_f32_16x16x32_bf16(a, b, c, 0, 0, 0); }
DI f32x4 mma16(s16x4 a, s16x4 b, f32x4 c) { return __builtin_amdgcn_mfma_f32_16x16x16bf16_1k(a, b, c, 0, 0, 0); }
DI void mfa(f32x4& c, s16x8 a, s16x8 b) { asm volatile("v_mfma_f32_16x16x32_bf16 %0, %1, %2, %0" : "+v"(c) : "v"(a), "v"(b)); }
DI s16x4 trd(const char* l) { return __builtin_amdgcn_ds_read_tr16_b64_v4i16((LAS s16x4*)l); }
DI s16x8 cat(s16x4 a, s16x4 b) { return __builtin_shufflevector(a, b, 0, 1, 2, 3, 4, 5, 6, 7); }
DI s16x8 trd2(const char* l0, const char* l1) { return cat(trd(l0), trd(l1)); }
DI s16x8 lrd(const char* l) { return *(const s16x8*)l; }
DI float fsin(float rev) { return __builtin_amdgcn_sinf(rev); }
DI float fcos(float rev) { return __builtin_amdgcn_cosf(rev); }
DI unsigned laddr_of(const char* l) { return (unsigned)(size_t)(const LAS char*)l; }
DI void glds4(unsigned laddr, const char* sb, unsigned v0, unsigned v1, unsigned v2, unsigned v3) {
  asm volatile("s_mov_b32 m0, %0\n\ts_nop 0\n\t"
               "global_load_lds_dwordx4 %2, %1\n\t"
               "global_load_lds_dwordx4 %3, %1 offset:1024\n\t"
               "global_load_lds_dwordx4 %4, %1 offset:2048\n\t"
               "global_load_lds_dwordx4 %5, %1 offset:3072"
               :: "s"(laddr), "s"(sb), "v"(v0), "v"(v1), "v"(v2), "v"(v3) : "memory");
}
DI void glds4c(unsigned laddr, const char* sb, unsigned v0, unsigned v1, unsigned v2, unsigned v3) {
  asm volatile("s_mov_b32 m0, %0\n\ts_nop 0\n\t"
               "global_load_lds_dwordx4 %2, %1 sc1\n\t"
               "global_load_lds_dwordx4 %3, %1 offset:1024 sc1\n\t"
               "global_load_lds_dwordx4 %4, %1 offset:2048 sc1\n\t"
               "global_load_lds_dwordx4 %5, %1 offset:3072 sc1"
               :: "s"(laddr), "s"(sb), "v"(v0), "v"(v1), "v"(v2), "v"(v3) : "memory");
}
DI int perm32(int t, int i) { return 8 * (i >> 2) + 4 * t + (i & 3); }
DI float phi(float t) {
  float e = __builtin_amdgcn_exp2f(t * 1.44269504088896341f);
  e = __builtin_amdgcn_fmed3f(e, 0.f, 1.f);
  return fmaxf(t + 1e-6f, 1e-6f) + e;
}
#define TSP
#define TSA
#define TSDECL
#define TS(slot)
#define TSFLUSH()
#define SCHED() __builtin_amdgcn_sched_barrier(0)
#define SGB(mask, n) __builtin_amdgcn_sched_group_barrier(mask, n, 0)
#define PIN(x) asm volatile("" : "+v"(x))
#define WAITV(n) asm volatile("s_waitcnt vmcnt(" #n ")" ::: "memory")
#define BAR() asm volatile("s_waitcnt lgkmcnt(0)\n\ts_barrier" ::: "memory")
#define FENCE() asm volatile("" ::: "memory")

typedef __amdgpu_buffer_rsrc_t rsrc_t;
DI u32x4 ldc(rsrc_t r, unsigned off) { return __builtin_amdgcn_raw_buffer_load_b128(r, off, 0, 16); }
DI void stc(rsrc_t r, unsigned off, u32x4 v) { __builtin_amdgcn_raw_buffer_store_b128(v, r, off, 0, 16); }
DI void stc1(rsrc_t r, unsigned off, unsigned v) { __builtin_amdgcn_raw_buffer_store_b32(v, r, off, 0, 16); }
template <int SL> DI void gwait(rsrc_t r, unsigned off, unsigned ep) {
#pragma unroll 1
  for (int sp = 0; sp < 200000; sp++) {
    FENCE();
    const u32x4 f = ldc(r, off);
    if (__all(f[0] == ep && f[1] == ep && f[2] == ep && f[3] == ep)) break;
    __builtin_amdgcn_s_sleep(SL);
  }
  FENCE();
}
DI unsigned ticket(rsrc_t r, int c, int lane) {
  unsigned old = 0;
  if (lane == 0) old = __builtin_amdgcn_raw_ptr_buffer_atomic_add_i32(1, r, WS_CNT + c * CS, 0, 16);
  old = __builtin_amdgcn_readfirstlane(old);
  FENCE();
  return old;
}
DI void arrive(rsrc_t r, int c, unsigned n, unsigned flag, int nrep, unsigned ep, int lane) {
  if (ticket(r, c, lane) == n - 1 && lane < nrep) stc(r, flag + lane * 128, u32x4{ep, ep, ep, ep});
}
DI void linc(char* p, int lane) { if (lane == 0) asm volatile("ds_add_u32 %0, %1" :: "v"(laddr_of(p)), "v"(1u) : "memory"); }
DI void lset(char* p) { asm volatile("ds_write_b32 %0, %1" :: "v"(laddr_of(p)), "v"(1u) : "memory"); }
DI void lwait(const char* p, unsigned want) {
#pragma unroll 1
  for (int sp = 0; sp < 4000000; sp++) {
    if ((unsigned)__builtin_amdgcn_readfirstlane(*(const volatile LAS unsigned*)p) >= want) break;
    __builtin_amdgcn_s_sleep(1);
  }
  FENCE();
}

DI void epoch_dma(char* lds, const char* p, int lane) {
  if (lane == 0) asm volatile("s_mov_b32 m0, %0\n\ts_nop 0\n\tglobal_load_lds_dword %2, %1" :: "s"(laddr_of(lds) + L_FL - 16), "s"(p), "v"(0u) : "memory");
}
DI unsigned epoch_get(const char* lds) { return (unsigned)__builtin_amdgcn_readfirstlane(*(const volatile LAS unsigned*)(lds + L_FL - 16)) + 1u; }
DI bool has_sent(u32x4 v) { return v[0] == SENT || v[1] == SENT || v[2] == SENT || v[3] == SENT; }
DI unsigned ticket_issue(rsrc_t r, int c, int lane) {
  unsigned old = 0;
  if (lane == 0) old = __builtin_amdgcn_raw_ptr_buffer_atomic_add_i32(1, r, WS_CNT + c * CS, 0, 16);
  return old;
}
DI void ticket_done(rsrc_t r, unsigned old, unsigned n, unsigned flag, int nrep, unsigned ep, int lane) {
  if ((unsigned)__builtin_amdgcn_readfirstlane(old) == n - 1 && lane < nrep) stc(r, flag + lane * 128, u32x4{ep, ep, ep, ep});
}

DI void wo_dma(char* lds, const char* wo, int ntile, int j, int w, int lane) {
  const int blk = ((lane & 7) >> 1) ^ ((lane >> 4) & 3);
  const unsigned v = (lane >> 3) * 1024 + (blk & 1) * 32 + (blk >> 1) * 512 + (lane & 1) * 16;
  glds4(laddr_of(lds) + L_WO + j * 16384 + w * 4096, wo + (size_t)(j * 128 + w * 32) * 1024 + ntile * 64, v, v + 7168, v + 2 * 7168, v + 3 * 7168);
}

DI void p1_prod(char* lds, const char* x, const char* wqkv, const char* wo, int ntile, int m, int col0, int col1, int rot, int w, int lane) {
  unsigned voffA[4], voffB[4];
#pragma unroll
  for (int j = 0; j < 4; j++) {
    const int par = j & 1;
    voffA[j] = (lane >> 3) * 1024 + (((lane & 7) ^ ((4 * par + (lane >> 4)) & 7)) * 16) + j * (8192 - 1024);
    voffB[j] = (lane >> 3) * 3072 + ((((lane & 7) >> 1) ^ (((lane >> 4) & 1) | (par << 1))) * 32) + (lane & 1) * 16 + j * (24576 - 1024);
  }
  const unsigned lbase = laddr_of(lds) + w * 4096;
  const char* xa = x + (size_t)(m * 128 + w * 32) * 1024;
  const char* wb = wqkv + (size_t)((w & 1) * 32) * 3072 + ((w >> 1) ? col1 : col0) * 2;
  auto issue = [&](int idx) {
    const int ks = (idx + rot) & 7;
    const unsigned slot = lbase + (idx & 3) * 32768;
    glds4(slot, xa + ks * 128, voffA[0], voffA[1], voffA[2], voffA[3]);
    glds4(slot + 16384, wb + (size_t)(ks * 64) * 3072, voffB[0], voffB[1], voffB[2], voffB[3]);
  };
  issue(0); issue(1);
  WAITV(8); BAR();
  issue(2); issue(3);
#pragma unroll 1
  for (int j = 1; j < 5; j++) { WAITV(16); BAR(); issue(j + 3); }
  WAITV(16); BAR();
  wo_dma(lds, wo, ntile, 0, w, lane);
  WAITV(12); BAR();
  wo_dma(lds, wo, ntile, 1, w, lane);
  WAITV(8); BAR();
}

template <bool QT>
DI void p1_gemm(TSP char* lds, const char* wr, const char* wi, rsrc_t rs, int m, int nt, unsigned& ep, const char* epp, int w, int lane) {
  const int i = lane & 15, g = lane >> 4, q = i >> 2, p = lane & 3;
  f32x4 acc[8][2];
#pragma unroll
  for (int a = 0; a < 8; a++) { acc[a][0] = f32x4{0, 0, 0, 0}; acc[a][1] = f32x4{0, 0, 0, 0}; }
  const int aoff = i * 128 + ((g ^ (i >> 1)) * 16);
  const int boff = 16384 + (8 * g + q) * 128 + ((w ^ (((q >> 1) & 1) | ((g & 1) << 1))) * 32) + p * 8;
  s16x8 A[2][2][8]; s16x4 B[2][2][2][2];
  auto rd1 = [&](int buf, const char* slot, int r) {
    const int ks = r / 12, rr = r % 12;
    const int t = rr == 0 ? 0 : rr == 1 ? 1 : rr == 3 ? 2 : rr == 4 ? 3 : -1;
    const int a = rr == 2 ? 0 : rr - 4;
    if (t >= 0) B[buf][ks][t >> 1][t & 1] = trd(slot + boff + (t >> 1) * 8192 + ks * 4096 + (t & 1) * 512);
    else A[buf][ks][a] = lrd(slot + (aoff ^ (ks * 64)) + a * 2048);
  };
  auto mf1 = [&](int buf, int ks, int mt, int hh) {
    const s16x8 bf = cat(B[buf][ks][hh][0], B[buf][ks][hh][1]);
    if (QT) mfa(acc[mt][hh], bf, A[buf][ks][mt]); else mfa(acc[mt][hh], A[buf][ks][mt], bf);
  };
  auto region = [&](int cur, const char* slot) {
#pragma unroll
    for (int mi = 0; mi < 32; mi++) {
      mf1(cur, mi >> 4, (mi & 15) >> 1, mi & 1);
      SCHED();
      if (mi < 24) { rd1(cur ^ 1, slot, mi); SCHED(); }
    }
  };
  if (QT && QPF) {
    const unsigned ln = (((blockIdx.x >> 3) & 7) * 2 + (w & 1)) * 64 + lane;
    asm volatile("s_mov_b32 m0, %0\n\ts_nop 0\n\tglobal_load_lds_dword %2, %1" :: "s"(laddr_of(lds) + L_ATT + 8192), "s"(((w >> 1) ? wi : wr) + (size_t)(blockIdx.x & 7) * 131584), "v"(ln * 128u) : "memory");
  }
  if (w == 0) epoch_dma(lds, epp, lane);
  if (QT && blockIdx.x == 128 && w == 0) {
    const u32x4 sent4 = {SENT, SENT, SENT, SENT};
    stc1(rs, WS_CNT + lane * CS, 0u);
    if (lane < 4) stc1(rs, WS_CNT + (64 + lane) * CS, 0u);
#pragma unroll 1
    for (int j = 0; j < 24; j++) stc(rs, WS_KVR + (j * 64 + lane) * 16, sent4);
    WAITV(0);
    const unsigned e = epoch_get(lds);
    if (lane < 8) stc(rs, WS_F0 + lane * 128, u32x4{e, e, e, e});
  }
  __builtin_amdgcn_s_waitcnt(0xC07F);
  BAR();
  TS(1);
#pragma unroll
  for (int r = 0; r < 24; r++) rd1(0, lds, r);
  SCHED();
#pragma unroll 1
  for (int it = 0; it < 3; it++) {
    BAR(); SCHED();
    region(0, lds + ((2 * it + 1) & 3) * 32768);
    if (it == 1) WAITV(0);
    BAR(); SCHED();
    region(1, lds + ((2 * it + 2) & 3) * 32768);
  }
  ep = epoch_get(lds);
  BAR(); SCHED();
  region(0, lds + 3 * 32768);
  asm volatile("s_nop 7");
  SCHED();
  auto mmh = [&](int hh) {
#pragma unroll
    for (int mt = 0; mt < 8; mt++)
#pragma unroll
      for (int ks = 0; ks < 2; ks++) {
        const s16x8 bf = cat(B[1][ks][hh][0], B[1][ks][hh][1]);
        acc[mt][hh] = QT ? mma(bf, A[1][ks][mt], acc[mt][hh]) : mma(A[1][ks][mt], bf, acc[mt][hh]);
      }
  };
  mmh(0);
  SCHED();
  TS(2);
  if (QT) {
    mmh(1);
#pragma unroll
    for (int mt = 0; mt < 8; mt++) {
      u32x4 v = {pk(phi(acc[mt][0][0]), phi(acc[mt][0][1])), pk(phi(acc[mt][0][2]), phi(acc[mt][0][3])),
                 pk(phi(acc[mt][1][0]), phi(acc[mt][1][1])), pk(phi(acc[mt][1][2]), phi(acc[mt][1][3]))};
      stc(rs, WS_PQ + (((m * 8 + mt) * 16 + (nt - 8) * 4 + w) * 64 + lane) * 16, v);
    }
    lwait(lds + L_FL + 100, 1);
    WAITV(0);
    arrive(rs, 34 + m, 16, WS_F2Q + m * 128, 1, ep, lane);
  } else {
    mmh(1);
    u32x4 pa[4];
#pragma unroll
    for (int u = 0; u < 4; u++)
      pa[u] = u32x4{pk(phi(acc[2 * u][0][0]), phi(acc[2 * u][0][1])), pk(phi(acc[2 * u][0][2]), phi(acc[2 * u][0][3])),
                    pk(phi(acc[2 * u + 1][0][0]), phi(acc[2 * u + 1][0][1])), pk(phi(acc[2 * u + 1][0][2]), phi(acc[2 * u + 1][0][3]))};
#pragma unroll
    for (int t = 0; t < 16; t++) { SGB(0x008, 1); SGB(0x002, 2); }
    SCHED();
    f32x4 kv = {0, 0, 0, 0}, z = {0, 0, 0, 0};
    const u32x4 ones = {0x3f803f80u, 0x3f803f80u, 0x3f803f80u, 0x3f803f80u};
#pragma unroll
    for (int u = 0; u < 4; u++) {
      u32x4 b = {pk(acc[2 * u][1][0], acc[2 * u][1][1]), pk(acc[2 * u][1][2], acc[2 * u][1][3]),
                 pk(acc[2 * u + 1][1][0], acc[2 * u + 1][1][1]), pk(acc[2 * u + 1][1][2], acc[2 * u + 1][1][3])};
      z = mma(as8(pa[u]), as8(ones), z);
      kv = mma(as8(pa[u]), as8(b), kv);
    }
    const int head = nt * 4 + w;
    u32x4 v = {pk(kv[0], kv[1]), pk(kv[2], kv[3]), pk(z[0], z[1]), pk(z[2], z[3])};
    stc(rs, WS_KVP + ((m * 32 + head) * 64 + lane) * 16, v);
    lwait(lds + L_FL + 100, 1);
    WAITV(0);
    if (ticket(rs, 1 + head, lane) == 15) {
      TS(4);
      u32x4 kl[16];
#pragma unroll
      for (int pp = 0; pp < 16; pp++) kl[pp] = ldc(rs, WS_KVP + ((pp * 32 + head) * 64 + lane) * 16);
      FENCE();
      const unsigned tk = ticket_issue(rs, 33, lane);
      u32x4 r;
#pragma unroll
      for (int d = 0; d < 4; d++) {
        float lo[4], hi[4];
#pragma unroll
        for (int j = 0; j < 4; j++) {
          lo[j] = (bflo(kl[4 * j][d]) + bflo(kl[4 * j + 1][d])) + (bflo(kl[4 * j + 2][d]) + bflo(kl[4 * j + 3][d]));
          hi[j] = (bfhi(kl[4 * j][d]) + bfhi(kl[4 * j + 1][d])) + (bfhi(kl[4 * j + 2][d]) + bfhi(kl[4 * j + 3][d]));
        }
        r[d] = pk((lo[0] + lo[1]) + (lo[2] + lo[3]), (hi[0] + hi[1]) + (hi[2] + hi[3]));
      }
      ticket_done(rs, tk, 32, WS_F2K, 8, ep, lane);
      const int u = (head >> 3) * 4 + (head & 3), hh = (head >> 2) & 1;
      const bool zr = (i & 3) == hh + 2 * (u & 1);
      __builtin_amdgcn_raw_buffer_store_b64(u32x2{r[0], r[1]}, rs, WS_KVR + (u * 64 + lane) * 16 + hh * 8, 0, 16);
      if (zr) stc(rs, WS_KVR + 16384 + ((u >> 1) * 64 + lane) * 16, hh ? u32x4{0u, 0u, r[2], r[3]} : u32x4{r[2], r[3], 0u, 0u});
      TS(5);
    }
  }
}

DI float dpp_sum16(float v) {
  v += __builtin_bit_cast(float, __builtin_amdgcn_update_dpp(0, __builtin_bit_cast(int, v), 0x128, 0xf, 0xf, false));
  v += __builtin_bit_cast(float, __builtin_amdgcn_update_dpp(0, __builtin_bit_cast(int, v), 0x124, 0xf, 0xf, false));
  v += __builtin_bit_cast(float, __builtin_amdgcn_update_dpp(0, __builtin_bit_cast(int, v), 0x122, 0xf, 0xf, false));
  v += __builtin_bit_cast(float, __builtin_amdgcn_update_dpp(0, __builtin_bit_cast(int, v), 0x121, 0xf, 0xf, false));
  return v;
}

DI void p1_rows(TSP char* lds, const char* x, const char* gw, const char* wo, const char* wr, const char* wi, rsrc_t rs, int a, const char* epp, int w, int lane) {
  const int i = lane & 15, g = lane >> 4;
  char* LX = lds;
  char* LS = lds + 32768;
  char* LC = lds + 106496;
  const int amt = (a >> 1) & 1;
  const int n2s = perm32(amt, 8 * (a & 1) + w);
  {
    const unsigned v = lane * 16;
    glds4(laddr_of(LX) + w * 4096, x + (size_t)(128 * (a >> 2) + n2s) * 1024, v, v + 31 * 1024, v + 62 * 1024, v + 93 * 1024);
  }
  char* LF = lds + 69632;
  auto wdma = [&]() {
    const unsigned v = lane * 16;
    const char* fsrc = ((w >> 2) ? wi : wr) + (size_t)(8 * (a & 7) + 64 * (4 * (a >> 3) + (w & 3))) * 514;
    glds4(laddr_of(LF) + w * 4352, fsrc, v, v, v, v);
    if (lane == 0) asm volatile("s_mov_b32 m0, %0\n\ts_nop 0\n\tglobal_load_lds_dwordx4 %2, %1" :: "s"(laddr_of(LF) + w * 4352 + 4096), "s"(fsrc + 4096), "v"(v) : "memory");
  };
  if (WE) wdma();
  {
    const float fi = (float)i;
    const int d = w & 3;
    char* cp = LC + lane * 16 + d * 4;
    const float n2i = (float)(4 * g + d) * fi;
    if (w < 4) {
      float c[2], s[2];
#pragma unroll
      for (int e = 0; e < 2; e++) {
        const float ang = (float)(4 * g + 16 * (d >> 1) + 2 * (d & 1) + e) * (fi * (1.f / 32.f));
        c[e] = fcos(ang);
        s[e] = (i == 0) ? (e ? -1.f : 1.f) : fsin(-ang);
      }
      *(unsigned*)(cp) = pk(c[0], c[1]);
      *(unsigned*)(cp + 1024) = pk(s[0], s[1]);
      *(float*)(cp + 6144) = fcos(n2i * (1.f / 512.f));
      *(float*)(cp + 7168) = fsin(n2i * (1.f / 512.f));
    } else {
      {
        const float ang = n2i * (1.f / 16.f);
        const float cs = fcos(ang), sn = fsin(ang);
        *(unsigned*)(cp + 2048) = pk(cs, sn);
        *(unsigned*)(cp + 3072) = pk(sn, cs) ^ ((i >= 8) ? 0x80000000u : 0x00008000u);
      }
      {
        const float ang = n2i * (1.f / 32.f);
        const float cs = fcos(ang);
        const float sn = (i == 0) ? ((d & 1) ? -1.f : 1.f) : fsin(-ang);
        const unsigned u = pk(cs, sn);
        const int sh = (i & 1) * 16;
        *(unsigned*)(cp + 4096) = (u & 0xffffu) << sh;
        *(unsigned*)(cp + 5120) = (u >> 16) << sh;
      }
    }
  }
  int soff[4];
#pragma unroll
  for (int r = 0; r < 4; r++) {
    const int k2 = 4 * g + r;
    const int k = i + 32 * k2;
    const int col = (i == 0) ? 16 * k2 : (k2 < 8 ? k : 512 - k);
    soff[r] = col * 128 + ((w ^ (col & 7)) * 16);
    PIN(soff[r]);
  }
  TS(12);
  BAR();
  if (!WE) wdma();
  epoch_dma(lds, epp, lane);
  const u32x4 gwv = *(const u32x4*)(gw + lane * 16);
  const u32x4 b1re = *(const u32x4*)(LC + lane * 16), b1im = *(const u32x4*)(LC + 1024 + lane * 16);
  const u32x4 a3re = *(const u32x4*)(LC + 2048 + lane * 16), a3im = *(const u32x4*)(LC + 3072 + lane * 16);
  const u32x4 a4re = *(const u32x4*)(LC + 4096 + lane * 16), a4im = *(const u32x4*)(LC + 5120 + lane * 16);
  const f32x4 twc = *(const f32x4*)(LC + 6144 + lane * 16), tws = *(const f32x4*)(LC + 7168 + lane * 16);
  WAITV(7);
  TS(1);
  {
    unsigned o[4][4];
#pragma unroll
    for (int a4 = 0; a4 < 4; a4++) {
      const char* row = LX + (w * 4 + a4) * 1024;
      s16x8 xf = trd2(row + lane * 8, row + 512 + lane * 8);
      const f32x4 z4 = {0, 0, 0, 0};
      f32x4 are = mma(xf, as8(b1re), z4), aim = mma(xf, as8(b1im), z4);
      f32x4 tr = are * twc + aim * tws, ti = aim * twc - are * tws;
      asm("" : "+v"(tr), "+v"(ti));
      u32x4 bb;
#pragma unroll
      for (int r = 0; r < 4; r++) bb[r] = pk(tr[r], ti[r]);
      f32x4 xre = mma(as8(a3re), as8(bb), z4), xim = mma(as8(a3im), as8(bb), z4);
      f32x4 sre = mma(as8(a4re), as8(bb), z4), sim = mma(as8(a4im), as8(bb), z4);
#pragma unroll
      for (int r = 0; r < 4; r++) o[r][a4] = pk((i == 0) ? sre[r] : xre[r], (i == 0) ? sim[r] : xim[r]);
    }
    if (lane == 0) {
      u32x4 v = {o[0][0] >> 16, o[0][1] >> 16, o[0][2] >> 16, o[0][3] >> 16};
      *(u32x4*)(LS + 256 * 128 + w * 16) = v;
#pragma unroll
      for (int a4 = 0; a4 < 4; a4++) o[0][a4] &= 0xffffu;
    }
#pragma unroll
    for (int r = 0; r < 4; r++) {
      u32x4 v = {o[r][0], o[r][1], o[r][2], o[r][3]};
      *(u32x4*)(LS + soff[r]) = v;
    }
  }
  TS(14);
  asm volatile("s_mov_b32 m0, %0\n\ts_nop 0\n\tglobal_load_lds_dword %2, %1" :: "s"(laddr_of(lds) + L_ATT + 8192), "s"(wo), "v"((unsigned)(((a & 7) * 8 + w) * 64 + lane) * 128u) : "memory");
  BAR();
  TS(2);
  const int sl = lane & 7;
  const int sc = w * 8 + (lane >> 3);
  const int xline = (amt * 4 + (a >> 4)) * 1024 + ((a >> 2) & 3) * 256 + (a & 1) * 128;
  u32x4 v0[4];
#pragma unroll
  for (int j = 0; j < 4; j++) v0[j] = *(const u32x4*)(LS + (j * 64 + sc) * 128 + ((sl ^ (sc & 7)) * 16));
#pragma unroll
  for (int j = 0; j < 4; j++) stc(rs, WS_X1T + (j * 64 + sc) * CP + xline + sl * 16, v0[j]);
  WAITV(5);
  BAR();
  TS(13);
  {
    const int wline = ((((a & 7) >> 1) * 2 + ((a >> 3) & 1)) * 64 + (a >> 4) * 16 + (a & 1) * 8) * 16;
    const int cb = (lane >> 3) * 16 + (w & 3) + 128 * (w >> 2);
    const char* fp = LF + sl * 514 + cb * 2;
#pragma unroll
    for (int j = 0; j < 4; j++) {
      u32x4 fv;
#pragma unroll
      for (int r = 0; r < 4; r++) fv[r] = (unsigned)*(const unsigned short*)(fp + r * 4352 + j * 8) | ((unsigned)*(const unsigned short*)(fp + (4 + r) * 4352 + j * 8) << 16);
      stc(rs, WS_WT + (cb + 4 * j) * CP + wline + sl * 16, fv);
    }
    if (w == 7 && lane < 8) {
      u32x4 v;
#pragma unroll
      for (int r = 0; r < 4; r++) v[r] = (unsigned)*(const unsigned short*)(LF + sl * 514 + 512 + r * 4352) | ((unsigned)*(const unsigned short*)(LF + sl * 514 + 512 + (4 + r) * 4352) << 16);
      stc(rs, WS_WT + 256 * CP + wline + sl * 16, v);
    }
  }
  if (w == 3 && lane < 8) stc(rs, WS_X1T + 256 * CP + xline + lane * 16, *(const u32x4*)(LS + 256 * 128 + lane * 16));
  TS(15);
  const u32x4 ones = {0x3f803f80u, 0x3f803f80u, 0x3f803f80u, 0x3f803f80u};
  float gsum[4];
#pragma unroll
  for (int j = 0; j < 4; j++) {
    u32x4 xv = *(const u32x4*)(LX + (w * 4 + j) * 1024 + lane * 16);
    u32x4 pr;
#pragma unroll
    for (int d = 0; d < 4; d++) pr[d] = pk(bflo(xv[d]) * bflo(gwv[d]), bfhi(xv[d]) * bfhi(gwv[d]));
    gsum[j] = mma(as8(ones), as8(pr), f32x4{0, 0, 0, 0})[0];
  }
  float gs = (g == 0) ? gsum[0] : (g == 1) ? gsum[1] : (g == 2) ? gsum[2] : gsum[3];
  gs = rbf(dpp_sum16(gs));
  const float gg = rbf(__builtin_amdgcn_rcpf(1.f + __builtin_amdgcn_exp2f(gs * -1.44269504088896341f)));
  const int s = 32 * (4 * (a >> 2) + g) + n2s;
  if (i == 0) stc1(rs, WS_GT + (s >> 6) * 256 + (s & 15) * 16 + ((s >> 4) & 3) * 4, pk(gg, 1.f - gg));
}

template <class F> DI void p2_fft(TSP F idle, char* lb, char* fl, char* f1seen, const char* ws, rsrc_t rs, int c, int cnt, int rep, unsigned& ep, int w, int lane) {
  const int i = lane & 15, g = lane >> 4;
  const int k1 = 16 * w + i;
  const float fk1 = (float)k1, fg = (float)g;
  u32x4 fre[4], fim[4], fc[2], fs[2], fn[2];
  {
    const float st = fk1 * (1.f / 64.f);
    const float a0 = fg * 4.f * st;
    const bool odd = i & 1;
    const unsigned m2 = odd ? 0x80008000u : 0u;
    const unsigned mre = (i & 2) ? 0x80008000u : 0u;
    const unsigned mim = ((i + 1) & 2) ? 0x80008000u : 0u;
#pragma unroll
    for (int d = 0; d < 4; d++) {
      const float ang = __builtin_fmaf((float)d, st, a0);
      const float cs = fcos(ang), sn = fsin(ang);
      const unsigned r0 = pk(cs, sn), i0 = pk(sn, cs) ^ 0x8000u;
      const unsigned r1 = (odd ? i0 : r0) ^ mre, i1 = (odd ? r0 : i0) ^ mim;
      fre[0][d] = r0; fim[0][d] = i0;
      fre[1][d] = r1; fim[1][d] = i1;
      fre[2][d] = r0 ^ m2; fim[2][d] = i0 ^ m2;
      fre[3][d] = r1 ^ m2; fim[3][d] = i1 ^ m2;
    }
#pragma unroll
    for (int ks = 0; ks < 4; ks++) { PIN(fre[ks]); PIN(fim[ks]); }
  }
  idle(0);
  float c1[2][4], s1[2][4], c2[2][4], s2[2][4];
#pragma unroll
  for (int mt = 0; mt < 2; mt++) {
    const float fa = (float)perm32(mt, i);
    const float st = fa * (1.f / 32.f), a0 = fg * 8.f * st;
    const float st1 = fk1 * (1.f / 2048.f), b0 = (fg * 8.f + (float)(4 * mt)) * st1;
    const float st2 = fa * (1.f / 2048.f), d0 = (fg * 4.f + (float)(16 * w)) * st2;
#pragma unroll
    for (int d = 0; d < 4; d++) {
      const float x0 = __builtin_fmaf((float)(2 * d), st, a0), x1 = __builtin_fmaf((float)(2 * d + 1), st, a0);
      fc[mt][d] = pk(fcos(x0), fcos(x1));
      fs[mt][d] = pk(fsin(x0), fsin(x1));
      fn[mt][d] = fs[mt][d] ^ 0x80008000u;
      const float t1 = __builtin_fmaf((float)d, st1, b0);
      c1[mt][d] = fcos(t1); s1[mt][d] = fsin(t1);
      const float t2 = __builtin_fmaf((float)d, st2, d0);
      c2[mt][d] = fcos(t2); s2[mt][d] = fsin(t2);
      PIN(c1[mt][d]); PIN(s1[mt][d]); PIN(c2[mt][d]); PIN(s2[mt][d]);
    }
    PIN(fc[mt]); PIN(fs[mt]); PIN(fn[mt]);
    idle(1 + mt);
  }
  const unsigned yt = WS_YT + (32 * k1 + 8 * g) * 2 + ((c == 256) ? 1 : (c & 1) * 256 + (c & ~1)) * YP;
  stc(rs, yt, u32x4{SENT, SENT, SENT, SENT});
  if (c & 255) stc(rs, yt + YP, u32x4{SENT, SENT, SENT, SENT});
  idle(3);
  TS(4);
  if (w == 0) { gwait<SLP>(rs, WS_F1 + rep * 128, ep); lset(f1seen); }
  else lwait(f1seen, 1);
  TS(5);
  asm volatile("s_mov_b32 m0, %0\n\ts_nop 0\n\tglobal_load_lds_dwordx4 %2, %1 sc1\n\tglobal_load_lds_dwordx4 %2, %1 offset:1024 sc1"
               :: "s"(laddr_of(lb) + 8192 + w * 2048), "s"(ws + WS_X1T + (size_t)c * CP + w * 2048), "v"(lane * 16) : "memory");
  u32x4 wf[2];
#pragma unroll
  for (int mt = 0; mt < 2; mt++) wf[mt] = ldc(rs, WS_WT + c * CP + ((w * 2 + mt) * 64 + lane) * 16);
  WAITV(2);
  linc(fl, lane);
  const unsigned tk = ticket_issue(rs, 50 + w * 4 + cnt, lane);
  lwait(fl, 4);
  TS(6);
  const f32x4 z4 = {0, 0, 0, 0};
  f32x4 re[2], im[2];
#pragma unroll
  for (int mt = 0; mt < 2; mt++) { re[mt] = z4; im[mt] = z4; }
#pragma unroll
  for (int ks = 0; ks < 4; ks++)
#pragma unroll
    for (int mt = 0; mt < 2; mt++) {
      s16x8 xa = lrd(lb + 8192 + ((mt * 4 + ks) * 64 + lane) * 16);
      re[mt] = mma(xa, as8(fre[ks]), re[mt]); im[mt] = mma(xa, as8(fim[ks]), im[mt]);
    }
  u32x4 pr, pi;
#pragma unroll
  for (int mt = 0; mt < 2; mt++) {
    float tr[4], ti[4];
#pragma unroll
    for (int r = 0; r < 4; r++) { tr[r] = re[mt][r] * c1[mt][r] + im[mt][r] * s1[mt][r]; ti[r] = im[mt][r] * c1[mt][r] - re[mt][r] * s1[mt][r]; }
    pr[2 * mt] = pk(tr[0], tr[1]); pr[2 * mt + 1] = pk(tr[2], tr[3]);
    pi[2 * mt] = pk(ti[0], ti[1]); pi[2 * mt + 1] = pk(ti[2], ti[3]);
  }
#pragma unroll
  for (int mt = 0; mt < 2; mt++) {
    re[mt] = mma(as8(fs[mt]), as8(pi), mma(as8(fc[mt]), as8(pr), z4));
    im[mt] = mma(as8(fn[mt]), as8(pr), mma(as8(fc[mt]), as8(pi), z4));
  }
#pragma unroll
  for (int mt = 0; mt < 2; mt++) {
    float tr[4], ti[4];
#pragma unroll
    for (int r = 0; r < 4; r++) {
      const float a = bflo(wf[mt][r]), b = bfhi(wf[mt][r]);
      tr[r] = re[mt][r] * a - im[mt][r] * b; ti[r] = re[mt][r] * b + im[mt][r] * a;
    }
    pr[2 * mt] = pk(tr[0], tr[1]); pr[2 * mt + 1] = pk(tr[2], tr[3]);
    pi[2 * mt] = pk(ti[0], ti[1]); pi[2 * mt + 1] = pk(ti[2], ti[3]);
  }
#pragma unroll
  for (int nt = 0; nt < 2; nt++) {
    re[nt] = mma(as8(pi), as8(fn[nt]), mma(as8(pr), as8(fc[nt]), z4));
    im[nt] = mma(as8(pi), as8(fc[nt]), mma(as8(pr), as8(fs[nt]), z4));
  }
#pragma unroll
  for (int nt = 0; nt < 2; nt++) {
    u32x4 v;
#pragma unroll
    for (int r = 0; r < 4; r++) v[r] = pk(re[nt][r] * c2[nt][r] - im[nt][r] * s2[nt][r], -re[nt][r] * s2[nt][r] - im[nt][r] * c2[nt][r]);
    *(u32x4*)(lb + ((nt * 4 + w) * 64 + lane) * 16) = v;
  }
  linc(fl + 4, lane);
  lwait(fl + 4, 4);
#pragma unroll
  for (int mt = 0; mt < 2; mt++) { re[mt] = z4; im[mt] = z4; }
#pragma unroll
  for (int ks = 0; ks < 4; ks++)
#pragma unroll
    for (int mt = 0; mt < 2; mt++) {
      s16x8 ya = lrd(lb + ((mt * 4 + ks) * 64 + lane) * 16);
      re[mt] = mma(ya, as8(fre[ks]), re[mt]); im[mt] = mma(ya, as8(fim[ks]), im[mt]);
    }
  u32x4 vr = {pk(re[0][0], re[0][1]), pk(re[0][2], re[0][3]), pk(re[1][0], re[1][1]), pk(re[1][2], re[1][3])};
  u32x4 vi = {pk(im[0][0], im[0][1]), pk(im[0][2], im[0][3]), pk(im[1][0], im[1][1]), pk(im[1][2], im[1][3])};
  const bool last = (unsigned)__builtin_amdgcn_readfirstlane(tk) == (cnt == 0 ? 64u : 63u);
  stc(rs, yt, vr);
  if (c & 255) stc(rs, yt + YP, vi);
  if (last && lane == 0) stc(rs, WS_F3 + (w * 4 + cnt) * 128, u32x4{ep, ep, ep, ep});
  TS(7);
}

extern "C" __global__ __launch_bounds__(512, 2) void gm_all(const char* x, const char* wqkv, const char* wr, const char* wi, const char* gw, const char* wo, char* ws, char* out) {
  __shared__ __attribute__((aligned(16))) char lds[163840];
  const int b = blockIdx.x, xcd = b & 7, ii = b >> 3;
  const int w8 = __builtin_amdgcn_readfirstlane(threadIdx.x >> 6), lane = threadIdx.x & 63;
  const int w = w8 & 3;
  TSDECL
  TS(0);
  if (ii < 2 && w8 < 4) {
    unsigned long long pc; int o0, o1;
    asm volatile("s_getpc_b64 %0\n.Lgm_pc%=:\n\ts_mov_b32 %1, gm_all-.Lgm_pc%=\n\ts_mov_b32 %2, gm_end-.Lgm_pc%=" : "=s"(pc), "=s"(o0), "=s"(o1));
    const unsigned off = (ii * 256 + threadIdx.x) * 128;
    if (off < (unsigned)(o1 - o0))
      asm volatile("s_mov_b32 m0, %0\n\ts_nop 0\n\tglobal_load_lds_dword %2, %1" :: "s"(laddr_of(lds) + L_ATT + 8192), "s"(pc + (long long)o0), "v"(off) : "memory");
  }
  unsigned ep;
  const char* epp = ws + WS_E;
  const rsrc_t rs = __builtin_amdgcn_make_buffer_rsrc(ws, 0, WS_END, 0x00020000);
  char* FL = lds + L_FL;
  const int ml = ii & 3, nl = ii >> 2;
  const int mtile = xcd * 4 + ml, ntile = nl;
  const bool aux = ii >= 24;
  {
    const int m = (xcd >> 1) * 4 + ml;
    const int nt = nl < 4 ? (xcd & 1) * 4 + nl : 8 + (xcd & 1) * 2 + (nl - 4);
    if (w8 < 4 || aux) {
      if (w8 == 0) *(LAS unsigned*)(FL + lane * 4) = 0;
      if (aux) p1_rows(TSA lds, x, gw, wo, wr, wi, rs, xcd * 8 + (ii - 24), epp, w8, lane);
      if (w8 < 4) {
        __builtin_amdgcn_s_setprio(2);
        if (aux) {
          ep = epoch_get(lds);
          if (w == 1) gwait<SLP>(rs, WS_F0 + xcd * 128, ep);
          WAITV(0);
          BAR();
          if (w == 1) arrive(rs, 0, 64, WS_F1, 8, ep, lane);
#pragma unroll 1
          for (int j = 0; j < 4; j++) wo_dma(lds, wo, ntile, j, w, lane);
        }
        else if (nl < 4) p1_gemm<false>(TSA lds, wr, wi, rs, m, nt, ep, epp, w, lane);
        else p1_gemm<true>(TSA lds, wr, wi, rs, m, nt, ep, epp, w, lane);
        __builtin_amdgcn_s_setprio(0);
      }
    } else {
      const int col0 = nl >= 4 ? (nt - 8) * 128 : 512 + nt * 64;
      const int col1 = nl >= 4 ? col0 + 64 : col0 + 512;
      p1_prod(lds, x, wqkv, wo, ntile, m, col0, col1, (2 * ml + nl) & ROT1, w, lane);
    }
  }
  TS(3);
  const int i = lane & 15, g = lane >> 4, q = i >> 2, p = lane & 3;
  const int fsw = (2 * g + (q >> 1)) & 3;
  if (w8 >= 4 || b == SPECIAL) {
    const bool pg = w8 >= 4;
    if (pg) {
      ep = epoch_get(lds);
      if (!aux) {
        if (w == 0) { gwait<SLP>(rs, WS_F0 + xcd * 128, ep); lset(FL + 100); }
        wo_dma(lds, wo, ntile, 2, w, lane); wo_dma(lds, wo, ntile, 3, w, lane);
      }
    }
    if (!aux && nl >= 4) __builtin_amdgcn_s_setprio(3); else __builtin_amdgcn_s_setprio(1);
    auto idle = [&](int st) {
      if (st == AST && pg && aux) { WAITV(0); BAR(); }
      if (st == 3 && !aux) { WAITV(0); linc(FL, lane); }
    };
    p2_fft(TSA idle, pg ? lds + L_FFT : lds + L_Y, pg ? FL + 4 : FL + 20, pg ? FL + 16 : FL + 28, ws, rs, pg ? b : 256, pg ? (b & 1) * 2 + (b >> 7) : 0, xcd, ep, w, lane);
  }
  if (w8 < 4) {
    linc(FL + 80, lane);
    if (aux) { if (b != SPECIAL) WAITV(0); linc(FL, lane); }
    TS(7);
    const int stg = 4 * mtile + w;
    bool kok = false;
    if (w == 0) {
#pragma unroll 1
      for (int sp = 0; sp < 200000; sp++) {
        FENCE();
        const bool ok = ldc(rs, (lane & 1) ? WS_F2K + xcd * 128 : WS_F2Q + (mtile >> 1) * 128)[0] == ep;
        const unsigned long long mk = __ballot(ok);
        kok = mk & 2;
        if (mk & 1) break;
        __builtin_amdgcn_s_sleep(SLP);
      }
      FENCE();
      if (kok) lset(FL + 88);
      lset(FL + 104);
    } else lwait(FL + 104, 1);
    u32x4 pq[4][4];
#pragma unroll
    for (int jj = 0; jj < 4; jj++)
#pragma unroll
      for (int u = 0; u < 4; u++) pq[jj][u] = ldc(rs, WS_PQ + (((4 * mtile + ((w + 1 + jj) & 3)) * 16 + 4 * w + u) * 64 + lane) * 16);
    lwait(FL, 4);
    s16x8 wf[4][4];
    {
      const int toffA = L_WO + w * 16384 + (4 * g + q) * 128 + (fsw * 32) + p * 8;
#pragma unroll
      for (int u = 0; u < 4; u++)
#pragma unroll
        for (int a = 0; a < 4; a++) { const char* pp = lds + (toffA ^ (32 * a)) + u * 2048; wf[u][a] = trd2(pp, pp + 8192); }
    }
    if (w == 0) { if (!kok) { gwait<SLP>(rs, WS_F2K + xcd * 128, ep); lset(FL + 88); } }
    else lwait(FL + 88, 1);
    TS(8);
    u32x4 kr[6];
#pragma unroll 1
    for (int sp = 0; sp < 20000; sp++) {
      FENCE();
      bool bad = false;
#pragma unroll
      for (int j = 0; j < 6; j++) kr[j] = ldc(rs, WS_KVR + ((j < 4 ? 4 * w + j : 12 + 2 * w + j) * 64 + lane) * 16);
#pragma unroll
      for (int j = 0; j < 6; j++) bad |= has_sent(kr[j]);
      if (!__any(bad)) break;
    }
    lwait(FL + 16, 1);
    const unsigned gv = __builtin_amdgcn_raw_buffer_load_b32(rs, WS_GT + mtile * 256 + i * 16 + w * 4, 0, 16);
    TS(13);
    const f32x4 z4 = {0, 0, 0, 0};
    const f32x4 eps = {1e-6f, 1e-6f, 1e-6f, 1e-6f};
    f32x4 acc[4];
    f32x4 nm[2][4][2], dn[2][4];
    auto nd = [&](int jj) {
#pragma unroll
      for (int u = 0; u < 4; u++) {
        dn[jj & 1][u] = mma(as8(kr[4 + (u >> 1)]), as8(pq[jj][u]), eps);
        nm[jj & 1][u][0] = mma16(as4(u32x2{kr[u][0], kr[u][1]}), as4(u32x2{pq[jj][u][0], pq[jj][u][1]}), z4);
        nm[jj & 1][u][1] = mma16(as4(u32x2{kr[u][2], kr[u][3]}), as4(u32x2{pq[jj][u][2], pq[jj][u][3]}), z4);
      }
    };
    u32x4 of[2][4];
    auto sc = [&](int jj) {
#pragma unroll
      for (int u = 0; u < 4; u++) {
        const f32x4 n0 = nm[jj & 1][u][0] * __builtin_amdgcn_rcpf(dn[jj & 1][u][2 * (u & 1)]);
        const f32x4 n1 = nm[jj & 1][u][1] * __builtin_amdgcn_rcpf(dn[jj & 1][u][2 * (u & 1) + 1]);
        of[jj & 1][u] = u32x4{pk(n0[0], n0[1]), pk(n0[2], n0[3]), pk(n1[0], n1[1]), pk(n1[2], n1[3])};
      }
    };
    auto pj = [&](int jj) {
#pragma unroll
      for (int u = 0; u < 4; u++)
#pragma unroll
        for (int a = 0; a < 4; a++) acc[a] = mma(wf[u][a], as8(of[jj & 1][u]), u ? acc[a] : z4);
      if (jj < 3) {
#pragma unroll
        for (int a = 0; a < 4; a++) *(f32x4*)(lds + L_WO + w * 16384 + (jj * 4 + a) * 1024 + lane * 16) = acc[a];
      }
    };
    nd(0); SCHED();
    nd(1); sc(0);
#pragma unroll
    for (int t = 0; t < 12; t++) { SGB(0x008, 1); SGB(0x002, 3); }
    SCHED();
#pragma unroll
    for (int jj = 0; jj < 3; jj++) {
      pj(jj); sc(jj + 1);
#pragma unroll
      for (int t = 0; t < 16; t++) { SGB(0x008, 1); SGB(0x002, 3); }
      SCHED();
      if (jj < 2) { nd(jj + 2); SCHED(); }
    }
    linc(FL + 92, lane);
    pj(3);
    TS(9);
    lwait(FL + 92, 4);
#pragma unroll
    for (int ww = 1; ww < 4; ww++)
#pragma unroll
      for (int a = 0; a < 4; a++) acc[a] += *(const f32x4*)(lds + L_WO + ((w + ww) & 3) * 16384 + ((3 - ww) * 4 + a) * 1024 + lane * 16);
    lwait(FL + 48, 4);
    TS(10);
    const float ga = bflo(gv), gb = bfhi(gv);
    unsigned pv[4][2];
#pragma unroll
    for (int a = 0; a < 2; a++) {
      const f32x4 t0 = *(const f32x4*)(lds + L_FFT + ((w * 4 + a) * 64 + lane) * 16);
      const f32x4 t1 = *(const f32x4*)(lds + L_FFT + ((w * 4 + a + 2) * 64 + lane) * 16);
      const f32x4 v0 = acc[a] * ga + (t0 + t1) * gb, v1 = acc[a + 2] * ga + (t0 - t1) * gb;
      pv[a][0] = pk(v0[0], v0[1]); pv[a][1] = pk(v0[2], v0[3]);
      pv[a + 2][0] = pk(v1[0], v1[1]); pv[a + 2][1] = pk(v1[2], v1[3]);
    }
#pragma unroll
    for (int a = 0; a < 2; a++) {
      auto s0 = __builtin_amdgcn_permlane16_swap(pv[2 * a][0], pv[2 * a + 1][0], false, false);
      auto s1 = __builtin_amdgcn_permlane16_swap(pv[2 * a][1], pv[2 * a + 1][1], false, false);
      u32x4 v = {s0[0], s1[0], s0[1], s1[1]};
      char* op = out + (size_t)(16 * stg + i) * 1024 + (256 * a + 32 * ntile + 16 * (g & 1) + 8 * (g >> 1)) * 2;
      asm volatile("global_store_dwordx4 %0, %1, off sc1\n\ts_nop 1" :: "v"(op), "v"(v) : "memory");
    }
    TS(11);
    TSFLUSH();
    return;
  }
  {
    const float sgn = (i & 1) ? -1.f : 1.f;
    const unsigned k0v = (i & 1) ? 0xb5803580u : 0x35803580u;
    const float fn = (float)(32 * ntile + 16 * (w & 1) + i) * (1.f / 512.f);
    const float t2 = 2.f * fcos(fn * 32.f);
    f32x2 c0[4], c1[4];
#pragma unroll
    for (int d = 0; d < 4; d++) {
      const float kf = (float)(4 * g + 128 * (w & 1) + (w >> 1)) + (float)(16 * (d >> 1) + 2 * (d & 1));
      const float a0 = kf * fn, a1 = (kf + 32.f) * fn;
      c0[d] = f32x2{fcos(a0), fsin(a0)} * 0x1p-19f;
      c1[d] = f32x2{fcos(a1), fsin(a1)} * 0x1p-19f;
    }
    u32x4 gf[8];
#pragma unroll
    for (int j = 0; j < 2; j++) {
#pragma unroll
      for (int h = 0; h < 2; h++) {
#pragma unroll
        for (int d = 0; d < 4; d++) {
          gf[4 * j + 2 * h][d] = pk(c0[d][0], c0[d][1]);
          gf[4 * j + 2 * h + 1][d] = pk(c1[d][0], c1[d][1]);
          c0[d] = c1[d] * t2 - c0[d];
        }
#pragma unroll
        for (int d = 0; d < 4; d++) c1[d] = c0[d] * t2 - c1[d];
      }
      gf[4 * j][0] = (w == j && g == 0) ? k0v : gf[4 * j][0];
      if (j == 0) {
        const float sg = (w & 1) ? sgn : 1.f;
#pragma unroll
        for (int d = 0; d < 4; d++) { c0[d] *= sg; c1[d] *= sg; }
      }
    }
#pragma unroll
    for (int k = 0; k < 8; k++) PIN(gf[k]);
    TS(8);
    gwait<SLP>(rs, WS_F3 + ((mtile >> 3) * 4 + w) * 128, ep);
    lwait(FL + 80, 4);
    linc(FL + 96, lane);
    TS(9);
    const int toffB = L_Y + (4 * g + q) * 128 + (fsw * 32) + p * 8;
    f32x4 accb[4];
    s16x8 tb_[2][2][4];
    auto rd = [&](int u) {
      const char* slot = lds + (w ^ (u >> 1)) * 16384 + (u & 1) * 8192;
#pragma unroll
      for (int ks = 0; ks < 2; ks++)
#pragma unroll
        for (int c = 0; c < 4; c++) tb_[u & 1][ks][c] = trd2(slot + (toffB ^ (32 * c)) + ks * 4096, slot + (toffB ^ (32 * c)) + ks * 4096 + 2048);
    };
    auto mm = [&](int u) {
#pragma unroll
      for (int ks = 0; ks < 2; ks++)
#pragma unroll
        for (int c = 0; c < 4; c++) accb[c] = mma(as8(gf[2 * u + ks]), tb_[u & 1][ks][c], accb[c]);
    };
#pragma unroll 1
    for (int sp = 0; sp < 20000; sp++) {
      const unsigned v = (lane >> 3) * YP + ((((lane & 7) >> 1) ^ ((lane >> 4) & 3)) * 32) + (lane & 1) * 16;
      const char* src = ws + WS_YT + (size_t)(w * 128) * YP + mtile * 128;
      const unsigned la = laddr_of(lds) + L_Y + w * 16384;
#pragma unroll
      for (int j = 0; j < 4; j++) glds4c(la + j * 4096, src + (size_t)j * (32 * YP), v, v + (8 * YP - 1024), v + 2 * (8 * YP - 1024), v + 3 * (8 * YP - 1024));
#pragma unroll
      for (int c = 0; c < 4; c++) accb[c] = f32x4{0, 0, 0, 0};
      WAITV(8);
      rd(0);
      WAITV(0);
      TS(10);
      rd(1); mm(0);
#pragma unroll
      for (int t = 0; t < 8; t++) { SGB(0x100, 2); SGB(0x008, 1); }
      SCHED();
      mm(1);
      const f32x4 s4 = (accb[0] + accb[1]) + (accb[2] + accb[3]);
      const float s1 = (s4[0] + s4[1]) + (s4[2] + s4[3]);
      if (!__any(s1 != s1)) break;
      bool bad = false;
#pragma unroll 1
      for (int j = 0; j < 16; j++) bad |= has_sent(*(const u32x4*)(lds + L_Y + w * 16384 + j * 1024 + lane * 16));
      if (!__any(bad)) break;
    }
    lset(FL + 32 + w * 4);
    lwait(FL + 32 + (w ^ 1) * 4, 1);
    rd(2);
    SCHED();
    rd(3); mm(2);
#pragma unroll
    for (int t = 0; t < 8; t++) { SGB(0x100, 2); SGB(0x008, 1); }
    SCHED();
    mm(3);
    lwait(FL + 96, 4);
    if (b == 0 && w == 1 && lane == 0) stc1(rs, WS_E, ep);
#pragma unroll
    for (int c = 0; c < 4; c++) *(f32x4*)(lds + L_FFT + ((c * 4 + w) * 64 + lane) * 16) = accb[c];
    linc(FL + 48, lane);
    TS(11);
    TSFLUSH();
  }
}

extern "C" __global__ void gm_end() {}

extern "C" void kernel_launch(void* const* d_in, const int* in_sizes, int n_in, void* d_out, int out_size, void* d_ws, size_t ws_size, hipStream_t stream) {
  gm_all<<<256, 512, 0, stream>>>((const char*)d_in[0], (const char*)d_in[1], (const char*)d_in[4], (const char*)d_in[5], (const char*)d_in[6],
                                  (const char*)d_in[2], (char*)d_ws, (char*)d_out);
}
```

```cpp
#include <hip/hip_runtime.h>
#include <stdint.h>

typedef short s16x4 __attribute__((ext_vector_type(4)));
typedef short s16x8 __attribute__((ext_vector_type(8)));
typedef float f32x4 __attribute__((ext_vector_type(4)));
typedef float f32x2 __attribute__((ext_vector_type(2)));
typedef unsigned u32x4 __attribute__((ext_vector_type(4)));
typedef unsigned u32x2 __attribute__((ext_vector_type(2)));
typedef __bf16 bfx2 __attribute__((ext_vector_type(2)));
#define LAS __attribute__((address_space(3)))
#define DI __device__ __forceinline__
#define ROT1 6
#define BPAD 128
#define WE 0
#define QPF 0
#define AST 1
#define CSTR 4352
#define PITC 8320
#define PITY 4224
#define SLP 2

static constexpr unsigned BP = 257 * 128 + BPAD;
static constexpr unsigned CP = PITC, YP = PITY;
static constexpr unsigned WS_X1T = 0;
static constexpr unsigned WS_WT  = WS_X1T + (9u << 18);
static constexpr unsigned WS_PQ  = WS_WT + (9u << 18);
static constexpr unsigned WS_KVP = WS_PQ + (2u << 20);
static constexpr unsigned WS_GT  = WS_KVP + (512u << 10);
static constexpr unsigned WS_KVR = WS_GT + (16u << 10);
static constexpr unsigned WS_YT  = WS_KVR + (32u << 10);
static constexpr unsigned WS_E   = WS_YT + (9u << 18);
static constexpr unsigned WS_CNT = WS_E + 4096;
static constexpr unsigned CS = CSTR;
static constexpr unsigned WS_F0  = WS_CNT + 128 * CS;
static constexpr unsigned WS_F1  = WS_F0 + 8 * 128;
static constexpr unsigned WS_F2K = WS_F1 + 8 * 128;
static constexpr unsigned WS_F2Q = WS_F2K + 8 * 128;
static constexpr unsigned WS_F3  = WS_F2Q + 16 * 128;
static constexpr unsigned WS_F5  = WS_F3 + 16 * 128;
static constexpr unsigned WS_END = WS_F5 + 8 * 128;
static constexpr unsigned SENT = 0xffffffffu;
static constexpr int SPECIAL = 255;

static constexpr int L_WO = 0, L_Y = 65536, L_FFT = 131072, L_ATT = 147456, L_FL = 163840 - 256;

DI unsigned pk(float lo, float hi) { f32x2 p = {lo, hi}; return __builtin_bit_cast(unsigned, __builtin_convertvector(p, bfx2)); }
DI float bflo(unsigned u) { return __builtin_bit_cast(float, u << 16); }
DI float bfhi(unsigned u) { return __builtin_bit_cast(float, u & 0xffff0000u); }
DI float rbf(float v) { return bflo(pk(v, 0.f)); }
DI s16x8 as8(u32x4 v) { return __builtin_bit_cast(s16x8, v); }
DI s16x4 as4(u32x2 v) { return __builtin_bit_cast(s16x4, v); }
DI f32x4 mma(s16x8 a, s16x8 b, f32x4 c) { return __builtin_amdgcn_mfma_f32_16x16x32_bf16(a, b, c, 0, 0, 0); }
DI f32x4 mma16(s16x4 a, s16x4 b, f32x4 c) { return __builtin_amdgcn_mfma_f32_16x16x16bf16_1k(a, b, c, 0, 0, 0); }
DI void mfa(f32x4& c, s16x8 a, s16x8 b) { asm volatile("v_mfma_f32_16x16x32_bf16 %0, %1, %2, %0" : "+v"(c) : "v"(a), "v"(b)); }
DI s16x4 trd(const char* l) { return __builtin_amdgcn_ds_read_tr16_b64_v4i16((LAS s16x4*)l); }
DI s16x8 cat(s16x4 a, s16x4 b) { return __builtin_shufflevector(a, b, 0, 1, 2, 3, 4, 5, 6, 7); }
DI s16x8 trd2(const char* l0, const char* l1) { return cat(trd(l0), trd(l1)); }
DI s16x8 lrd(const char* l) { return *(const s16x8*)l; }
DI float fsin(float rev) { return __builtin_amdgcn_sinf(rev); }
DI float fcos(float rev) { return __builtin_amdgcn_cosf(rev); }
DI unsigned laddr_of(const char* l) { return (unsigned)(size_t)(const LAS char*)l; }
DI void glds4(unsigned laddr, const char* sb, unsigned v0, unsigned v1, unsigned v2, unsigned v3) {
  asm volatile("s_mov_b32 m0, %0\n\ts_nop 0\n\t"
               "global_load_lds_dwordx4 %2, %1\n\t"
               "global_load_lds_dwordx4 %3, %1 offset:1024\n\t"
               "global_load_lds_dwordx4 %4, %1 offset:2048\n\t"
               "global_load_lds_dwordx4 %5, %1 offset:3072"
               :: "s"(laddr), "s"(sb), "v"(v0), "v"(v1), "v"(v2), "v"(v3) : "memory");
}
DI void glds4c(unsigned laddr, const char* sb, unsigned v0, unsigned v1, unsigned v2, unsigned v3) {
  asm volatile("s_mov_b32 m0, %0\n\ts_nop 0\n\t"
               "global_load_lds_dwordx4 %2, %1 sc1\n\t"
               "global_load_lds_dwordx4 %3, %1 offset:1024 sc1\n\t"
               "global_load_lds_dwordx4 %4, %1 offset:2048 sc1\n\t"
               "global_load_lds_dwordx4 %5, %1 offset:3072 sc1"
               :: "s"(laddr), "s"(sb), "v"(v0), "v"(v1), "v"(v2), "v"(v3) : "memory");
}
DI int perm32(int t, int i) { return 8 * (i >> 2) + 4 * t + (i & 3); }
DI float phi(float t) {
  float e = __builtin_amdgcn_exp2f(t * 1.44269504088896341f);
  e = __builtin_amdgcn_fmed3f(e, 0.f, 1.f);
  return fmaxf(t + 1e-6f, 1e-6f) + e;
}
#define TSP
#define TSA
#define TSDECL
#define TS(slot)
#define TSFLUSH()
#define SCHED() __builtin_amdgcn_sched_barrier(0)
#define SGB(mask, n) __builtin_amdgcn_sched_group_barrier(mask, n, 0)
#define PIN(x) asm volatile("" : "+v"(x))
#define WAITV(n) asm volatile("s_waitcnt vmcnt(" #n ")" ::: "memory")
#define BAR() asm volatile("s_waitcnt lgkmcnt(0)\n\ts_barrier" ::: "memory")
#define FENCE() asm volatile("" ::: "memory")

typedef __amdgpu_buffer_rsrc_t rsrc_t;
DI u32x4 ldc(rsrc_t r, unsigned off) { return __builtin_amdgcn_raw_buffer_load_b128(r, off, 0, 16); }
DI void stc(rsrc_t r, unsigned off, u32x4 v) { __builtin_amdgcn_raw_buffer_store_b128(v, r, off, 0, 16); }
DI void stc1(rsrc_t r, unsigned off, unsigned v) { __builtin_amdgcn_raw_buffer_store_b32(v, r, off, 0, 16); }
template <int SL> DI void gwait(rsrc_t r, unsigned off, unsigned ep) {
#pragma unroll 1
  for (int sp = 0; sp < 200000; sp++) {
    FENCE();
    const u32x4 f = ldc(r, off);
    if (__all(f[0] == ep && f[1] == ep && f[2] == ep && f[3] == ep)) break;
    __builtin_amdgcn_s_sleep(SL);
  }
  FENCE();
}
DI unsigned ticket(rsrc_t r, int c, int lane) {
  unsigned old = 0;
  if (lane == 0) old = __builtin_amdgcn_raw_ptr_buffer_atomic_add_i32(1, r, WS_CNT + c * CS, 0, 16);
  old = __builtin_amdgcn_readfirstlane(old);
  FENCE();
  return old;
}
DI void arrive(rsrc_t r, int c, unsigned n, unsigned flag, int nrep, unsigned ep, int lane) {
  if (ticket(r, c, lane) == n - 1 && lane < nrep) stc(r, flag + lane * 128, u32x4{ep, ep, ep, ep});
}
DI void linc(char* p, int lane) { if (lane == 0) asm volatile("ds_add_u32 %0, %1" :: "v"(laddr_of(p)), "v"(1u) : "memory"); }
DI void lset(char* p) { asm volatile("ds_write_b32 %0, %1" :: "v"(laddr_of(p)), "v"(1u) : "memory"); }
DI void lwait(const char* p, unsigned want) {
#pragma unroll 1
  for (int sp = 0; sp < 4000000; sp++) {
    if ((unsigned)__builtin_amdgcn_readfirstlane(*(const volatile LAS unsigned*)p) >= want) break;
    __builtin_amdgcn_s_sleep(1);
  }
  FENCE();
}

DI void epoch_dma(char* lds, const char* p, int lane) {
  if (lane == 0) asm volatile("s_mov_b32 m0, %0\n\ts_nop 0\n\tglobal_load_lds_dword %2, %1" :: "s"(laddr_of(lds) + L_FL - 16), "s"(p), "v"(0u) : "memory");
}
DI unsigned epoch_get(const char* lds) { return (unsigned)__builtin_amdgcn_readfirstlane(*(const volatile LAS unsigned*)(lds + L_FL - 16)) + 1u; }
DI bool has_sent(u32x4 v) { return v[0] == SENT || v[1] == SENT || v[2] == SENT || v[3] == SENT; }
DI unsigned ticket_issue(rsrc_t r, int c, int lane) {
  unsigned old = 0;
  if (lane == 0) old = __builtin_amdgcn_raw_ptr_buffer_atomic_add_i32(1, r, WS_CNT + c * CS, 0, 16);
  return old;
}
DI void ticket_done(rsrc_t r, unsigned old, unsigned n, unsigned flag, int nrep, unsigned ep, int lane) {
  if ((unsigned)__builtin_amdgcn_readfirstlane(old) == n - 1 && lane < nrep) stc(r, flag + lane * 128, u32x4{ep, ep, ep, ep});
}

DI void wo_dma(char* lds, const char* wo, int ntile, int j, int w, int lane) {
  const int blk = ((lane & 7) >> 1) ^ ((lane >> 4) & 3);
  const unsigned v = (lane >> 3) * 1024 + (blk & 1) * 32 + (blk >> 1) * 512 + (lane & 1) * 16;
  glds4(laddr_of(lds) + L_WO + j * 16384 + w * 4096, wo + (size_t)(j * 128 + w * 32) * 1024 + ntile * 64, v, v + 7168, v + 2 * 7168, v + 3 * 7168);
}

DI void p1_prod(char* lds, const char* x, const char* wqkv, const char* wo, int ntile, int m, int col0, int col1, int rot, int w, int lane) {
  unsigned voffA[4], voffB[4];
#pragma unroll
  for (int j = 0; j < 4; j++) {
    const int par = j & 1;
    voffA[j] = (lane >> 3) * 1024 + (((lane & 7) ^ ((4 * par + (lane >> 4)) & 7)) * 16) + j * (8192 - 1024);
    voffB[j] = (lane >> 3) * 3072 + ((((lane & 7) >> 1) ^ (((lane >> 4) & 1) | (par << 1))) * 32) + (lane & 1) * 16 + j * (24576 - 1024);
  }
  const unsigned lbase = laddr_of(lds) + w * 4096;
  const char* xa = x + (size_t)(m * 128 + w * 32) * 1024;
  const char* wb = wqkv + (size_t)((w & 1) * 32) * 3072 + ((w >> 1) ? col1 : col0) * 2;
  auto issue = [&](int idx) {
    const int ks = (idx + rot) & 7;
    const unsigned slot = lbase + (idx & 3) * 32768;
    glds4(slot, xa + ks * 128, voffA[0], voffA[1], voffA[2], voffA[3]);
    glds4(slot + 16384, wb + (size_t)(ks * 64) * 3072, voffB[0], voffB[1], voffB[2], voffB[3]);
  };
  issue(0); issue(1);
  WAITV(8); BAR();
  issue(2); issue(3);
#pragma unroll 1
  for (int j = 1; j < 5; j++) { WAITV(16); BAR(); issue(j + 3); }
  WAITV(16); BAR();
  wo_dma(lds, wo, ntile, 0, w, lane);
  WAITV(12); BAR();
  wo_dma(lds, wo, ntile, 1, w, lane);
  WAITV(8); BAR();
}

template <bool QT>
DI void p1_gemm(TSP char* lds, const char* wr, const char* wi, rsrc_t rs, int m, int nt, unsigned& ep, const char* epp, int w, int lane) {
  const int i = lane & 15, g = lane >> 4, q = i >> 2, p = lane & 3;
  f32x4 acc[8][2];
#pragma unroll
  for (int a = 0; a < 8; a++) { acc[a][0] = f32x4{0, 0, 0, 0}; acc[a][1] = f32x4{0, 0, 0, 0}; }
  const int aoff = i * 128 + ((g ^ (i >> 1)) * 16);
  const int boff = 16384 + (8 * g + q) * 128 + ((w ^ (((q >> 1) & 1) | ((g & 1) << 1))) * 32) + p * 8;
  s16x8 A[2][2][8]; s16x4 B[2][2][2][2];
  auto rd1 = [&](int buf, const char* slot, int r) {
    const int ks = r / 12, rr = r % 12;
    const int t = rr == 0 ? 0 : rr == 1 ? 1 : rr == 3 ? 2 : rr == 4 ? 3 : -1;
    const int a = rr == 2 ? 0 : rr - 4;
    if (t >= 0) B[buf][ks][t >> 1][t & 1] = trd(slot + boff + (t >> 1) * 8192 + ks * 4096 + (t & 1) * 512);
    else A[buf][ks][a] = lrd(slot + (aoff ^ (ks * 64)) + a * 2048);
  };
  auto mf1 = [&](int buf, int ks, int mt, int hh) {
    const s16x8 bf = cat(B[buf][ks][hh][0], B[buf][ks][hh][1]);
    if (QT) mfa(acc[mt][hh], bf, A[buf][ks][mt]); else mfa(acc[mt][hh], A[buf][ks][mt], bf);
  };
  auto region = [&](int cur, const char* slot) {
#pragma unroll
    for (int mi = 0; mi < 32; mi++) {
      mf1(cur, mi >> 4, (mi & 15) >> 1, mi & 1);
      SCHED();
      if (mi < 24) { rd1(cur ^ 1, slot, mi); SCHED(); }
    }
  };
  if (QT && QPF) {
    const unsigned ln = (((blockIdx.x >> 3) & 7) * 2 + (w & 1)) * 64 + lane;
    asm volatile("s_mov_b32 m0, %0\n\ts_nop 0\n\tglobal_load_lds_dword %2, %1" :: "s"(laddr_of(lds) + L_ATT + 8192), "s"(((w >> 1) ? wi : wr) + (size_t)(blockIdx.x & 7) * 131584), "v"(ln * 128u) : "memory");
  }
  if (w == 0) epoch_dma(lds, epp, lane);
  if (QT && blockIdx.x == 128 && w == 0) {
    const u32x4 sent4 = {SENT, SENT, SENT, SENT};
    stc1(rs, WS_CNT + lane * CS, 0u);
    if (lane < 4) stc1(rs, WS_CNT + (64 + lane) * CS, 0u);
#pragma unroll 1
    for (int j = 0; j < 24; j++) stc(rs, WS_KVR + (j * 64 + lane) * 16, sent4);
    WAITV(0);
    const unsigned e = epoch_get(lds);
    if (lane < 8) stc(rs, WS_F0 + lane * 128, u32x4{e, e, e, e});
  }
  __builtin_amdgcn_s_waitcnt(0xC07F);
  BAR();
  TS(1);
#pragma unroll
  for (int r = 0; r < 24; r++) rd1(0, lds, r);
  SCHED();
#pragma unroll 1
  for (int it = 0; it < 3; it++) {
    BAR(); SCHED();
    region(0, lds + ((2 * it + 1) & 3) * 32768);
    if (it == 1) WAITV(0);
    BAR(); SCHED();
    region(1, lds + ((2 * it + 2) & 3) * 32768);
  }
  ep = epoch_get(lds);
  BAR(); SCHED();
  region(0, lds + 3 * 32768);
  asm volatile("s_nop 7");
  SCHED();
  auto mmh = [&](int hh) {
#pragma unroll
    for (int mt = 0; mt < 8; mt++)
#pragma unroll
      for (int ks = 0; ks < 2; ks++) {
        const s16x8 bf = cat(B[1][ks][hh][0], B[1][ks][hh][1]);
        acc[mt][hh] = QT ? mma(bf, A[1][ks][mt], acc[mt][hh]) : mma(A[1][ks][mt], bf, acc[mt][hh]);
      }
  };
  mmh(0);
  SCHED();
  TS(2);
  if (QT) {
    mmh(1);
#pragma unroll
    for (int mt = 0; mt < 8; mt++) {
      u32x4 v = {pk(phi(acc[mt][0][0]), phi(acc[mt][0][1])), pk(phi(acc[mt][0][2]), phi(acc[mt][0][3])),
                 pk(phi(acc[mt][1][0]), phi(acc[mt][1][1])), pk(phi(acc[mt][1][2]), phi(acc[mt][1][3]))};
      stc(rs, WS_PQ + (((m * 8 + mt) * 16 + (nt - 8) * 4 + w) * 64 + lane) * 16, v);
    }
    lwait(lds + L_FL + 100, 1);
    WAITV(0);
    arrive(rs, 34 + m, 16, WS_F2Q + m * 128, 1, ep, lane);
  } else {
    mmh(1);
    u32x4 pa[4];
#pragma unroll
    for (int u = 0; u < 4; u++)
      pa[u] = u32x4{pk(phi(acc[2 * u][0][0]), phi(acc[2 * u][0][1])), pk(phi(acc[2 * u][0][2]), phi(acc[2 * u][0][3])),
                    pk(phi(acc[2 * u + 1][0][0]), phi(acc[2 * u + 1][0][1])), pk(phi(acc[2 * u + 1][0][2]), phi(acc[2 * u + 1][0][3]))};
#pragma unroll
    for (int t = 0; t < 16; t++) { SGB(0x008, 1); SGB(0x002, 2); }
    SCHED();
    f32x4 kv = {0, 0, 0, 0}, z = {0, 0, 0, 0};
    const u32x4 ones = {0x3f803f80u, 0x3f803f80u, 0x3f803f80u, 0x3f803f80u};
#pragma unroll
    for (int u = 0; u < 4; u++) {
      u32x4 b = {pk(acc[2 * u][1][0], acc[2 * u][1][1]), pk(acc[2 * u][1][2], acc[2 * u][1][3]),
                 pk(acc[2 * u + 1][1][0], acc[2 * u + 1][1][1]), pk(acc[2 * u + 1][1][2], acc[2 * u + 1][1][3])};
      z = mma(as8(pa[u]), as8(ones), z);
      kv = mma(as8(pa[u]), as8(b), kv);
    }
    const int head = nt * 4 + w;
    u32x4 v = {pk(kv[0], kv[1]), pk(kv[2], kv[3]), pk(z[0], z[1]), pk(z[2], z[3])};
    stc(rs, WS_KVP + ((m * 32 + head) * 64 + lane) * 16, v);
    lwait(lds + L_FL + 100, 1);
    WAITV(0);
    if (ticket(rs, 1 + head, lane) == 15) {
      TS(4);
      u32x4 kl[16];
#pragma unroll
      for (int pp = 0; pp < 16; pp++) kl[pp] = ldc(rs, WS_KVP + ((pp * 32 + head) * 64 + lane) * 16);
      FENCE();
      const unsigned tk = ticket_issue(rs, 33, lane);
      u32x4 r;
#pragma unroll
      for (int d = 0; d < 4; d++) {
        float lo[4], hi[4];
#pragma unroll
        for (int j = 0; j < 4; j++) {
          lo[j] = (bflo(kl[4 * j][d]) + bflo(kl[4 * j + 1][d])) + (bflo(kl[4 * j + 2][d]) + bflo(kl[4 * j + 3][d]));
          hi[j] = (bfhi(kl[4 * j][d]) + bfhi(kl[4 * j + 1][d])) + (bfhi(kl[4 * j + 2][d]) + bfhi(kl[4 * j + 3][d]));
        }
        r[d] = pk((lo[0] + lo[1]) + (lo[2] + lo[3]), (hi[0] + hi[1]) + (hi[2] + hi[3]));
      }
      ticket_done(rs, tk, 32, WS_F2K, 8, ep, lane);
      const int u = (head >> 3) * 4 + (head & 3), hh = (head >> 2) & 1;
      const bool zr = (i & 3) == hh + 2 * (u & 1);
      __builtin_amdgcn_raw_buffer_store_b64(u32x2{r[0], r[1]}, rs, WS_KVR + (u * 64 + lane) * 16 + hh * 8, 0, 16);
      if (zr) stc(rs, WS_KVR + 16384 + ((u >> 1) * 64 + lane) * 16, hh ? u32x4{0u, 0u, r[2], r[3]} : u32x4{r[2], r[3], 0u, 0u});
      TS(5);
    }
  }
}

DI float dpp_sum16(float v) {
  v += __builtin_bit_cast(float, __builtin_amdgcn_update_dpp(0, __builtin_bit_cast(int, v), 0x128, 0xf, 0xf, false));
  v += __builtin_bit_cast(float, __builtin_amdgcn_update_dpp(0, __builtin_bit_cast(int, v), 0x124, 0xf, 0xf, false));
  v += __builtin_bit_cast(float, __builtin_amdgcn_update_dpp(0, __builtin_bit_cast(int, v), 0x122, 0xf, 0xf, false));
  v += __builtin_bit_cast(float, __builtin_amdgcn_update_dpp(0, __builtin_bit_cast(int, v), 0x121, 0xf, 0xf, false));
  return v;
}

DI void p1_rows(TSP char* lds, const char* x, const char* gw, const char* wo, const char* wr, const char* wi, rsrc_t rs, int a, const char* epp, int w, int lane) {
  const int i = lane & 15, g = lane >> 4;
  char* LX = lds;
  char* LS = lds + 32768;
  char* LC = lds + 106496;
  const int amt = (a >> 1) & 1;
  const int n2s = perm32(amt, 8 * (a & 1) + w);
  {
    const unsigned v = lane * 16;
    glds4(laddr_of(LX) + w * 4096, x + (size_t)(128 * (a >> 2) + n2s) * 1024, v, v + 31 * 1024, v + 62 * 1024, v + 93 * 1024);
  }
  char* LF = lds + 69632;
  auto wdma = [&]() {
    const unsigned v = lane * 16;
    const char* fsrc = ((w >> 2) ? wi : wr) + (size_t)(8 * (a & 7) + 64 * (4 * (a >> 3) + (w & 3))) * 514;
    glds4(laddr_of(LF) + w * 4352, fsrc, v, v, v, v);
    if (lane == 0) asm volatile("s_mov_b32 m0, %0\n\ts_nop 0\n\tglobal_load_lds_dwordx4 %2, %1" :: "s"(laddr_of(LF) + w * 4352 + 4096), "s"(fsrc + 4096), "v"(v) : "memory");
  };
  if (WE) wdma();
  {
    const float fi = (float)i;
    const int d = w & 3;
    char* cp = LC + lane * 16 + d * 4;
    const float n2i = (float)(4 * g + d) * fi;
    if (w < 4) {
      float c[2], s[2];
#pragma unroll
      for (int e = 0; e < 2; e++) {
        const float ang = (float)(4 * g + 16 * (d >> 1) + 2 * (d & 1) + e) * (fi * (1.f / 32.f));
        c[e] = fcos(ang);
        s[e] = (i == 0) ? (e ? -1.f : 1.f) : fsin(-ang);
      }
      *(unsigned*)(cp) = pk(c[0], c[1]);
      *(unsigned*)(cp + 1024) = pk(s[0], s[1]);
      *(float*)(cp + 6144) = fcos(n2i * (1.f / 512.f));
      *(float*)(cp + 7168) = fsin(n2i * (1.f / 512.f));
    } else {
      {
        const float ang = n2i * (1.f / 16.f);
        const float cs = fcos(ang), sn = fsin(ang);
        *(unsigned*)(cp + 2048) = pk(cs, sn);
        *(unsigned*)(cp + 3072) = pk(sn, cs) ^ ((i >= 8) ? 0x80000000u : 0x00008000u);
      }
      {
        const float ang = n2i * (1.f / 32.f);
        const float cs = fcos(ang);
        const float sn = (i == 0) ? ((d & 1) ? -1.f : 1.f) : fsin(-ang);
        const unsigned u = pk(cs, sn);
        const int sh = (i & 1) * 16;
        *(unsigned*)(cp + 4096) = (u & 0xffffu) << sh;
        *(unsigned*)(cp + 5120) = (u >> 16) << sh;
      }
    }
  }
  int soff[4];
#pragma unroll
  for (int r = 0; r < 4; r++) {
    const int k2 = 4 * g + r;
    const int k = i + 32 * k2;
    const int col = (i == 0) ? 16 * k2 : (k2 < 8 ? k : 512 - k);
    soff[r] = col * 128 + ((w ^ (col & 7)) * 16);
    PIN(soff[r]);
  }
  TS(12);
  BAR();
  if (!WE) wdma();
  epoch_dma(lds, epp, lane);
  const u32x4 gwv = *(const u32x4*)(gw + lane * 16);
  asm volatile("s_mov_b32 m0, %0\n\ts_nop 0\n\tglobal_load_lds_dword %2, %1" :: "s"(laddr_of(lds) + L_ATT + 8192), "s"(wo), "v"((unsigned)((a * 8 + w) * 8 + (lane & 7)) * 128u) : "memory");
  const u32x4 b1re = *(const u32x4*)(LC + lane * 16), b1im = *(const u32x4*)(LC + 1024 + lane * 16);
  const u32x4 a3re = *(const u32x4*)(LC + 2048 + lane * 16), a3im = *(const u32x4*)(LC + 3072 + lane * 16);
  const u32x4 a4re = *(const u32x4*)(LC + 4096 + lane * 16), a4im = *(const u32x4*)(LC + 5120 + lane * 16);
  const f32x4 twc = *(const f32x4*)(LC + 6144 + lane * 16), tws = *(const f32x4*)(LC + 7168 + lane * 16);
  WAITV(8);
  {
    unsigned o[4][4];
#pragma unroll
    for (int a4 = 0; a4 < 4; a4++) {
      const char* row = LX + (w * 4 + a4) * 1024;
      s16x8 xf = trd2(row + lane * 8, row + 512 + lane * 8);
      const f32x4 z4 = {0, 0, 0, 0};
      f32x4 are = mma(xf, as8(b1re), z4), aim = mma(xf, as8(b1im), z4);
      f32x4 tr = are * twc + aim * tws, ti = aim * twc - are * tws;
      asm("" : "+v"(tr), "+v"(ti));
      u32x4 bb;
#pragma unroll
      for (int r = 0; r < 4; r++) bb[r] = pk(tr[r], ti[r]);
      f32x4 xre = mma(as8(a3re), as8(bb), z4), xim = mma(as8(a3im), as8(bb), z4);
      f32x4 sre = mma(as8(a4re), as8(bb), z4), sim = mma(as8(a4im), as8(bb), z4);
#pragma unroll
      for (int r = 0; r < 4; r++) o[r][a4] = pk((i == 0) ? sre[r] : xre[r], (i == 0) ? sim[r] : xim[r]);
    }
    if (lane == 0) {
      u32x4 v = {o[0][0] >> 16, o[0][1] >> 16, o[0][2] >> 16, o[0][3] >> 16};
      *(u32x4*)(LS + 256 * 128 + w * 16) = v;
#pragma unroll
      for (int a4 = 0; a4 < 4; a4++) o[0][a4] &= 0xffffu;
    }
#pragma unroll
    for (int r = 0; r < 4; r++) {
      u32x4 v = {o[r][0], o[r][1], o[r][2], o[r][3]};
      *(u32x4*)(LS + soff[r]) = v;
    }
  }
  TS(14);
  BAR();
  TS(2);
  const int sl = lane & 7;
  const int sc = w * 8 + (lane >> 3);
  const int xline = (amt * 4 + (a >> 4)) * 1024 + ((a >> 2) & 3) * 256 + (a & 1) * 128;
  u32x4 v0[4];
#pragma unroll
  for (int j = 0; j < 4; j++) v0[j] = *(const u32x4*)(LS + (j * 64 + sc) * 128 + ((sl ^ (sc & 7)) * 16));
#pragma unroll
  for (int j = 0; j < 4; j++) stc(rs, WS_X1T + (xline >> 7) * BP + (j * 64 + sc) * 128 + sl * 16, v0[j]);
  TS(12);
  WAITV(7);
  TS(14);
  WAITV(5);
  TS(1);
  BAR();
  TS(13);
  {
    const int wline = ((((a & 7) >> 1) * 2 + ((a >> 3) & 1)) * 64 + (a >> 4) * 16 + (a & 1) * 8) * 16;
    const int cb = (lane >> 3) * 16 + (w & 3) + 128 * (w >> 2);
    const char* fp = LF + sl * 514 + cb * 2;
#pragma unroll
    for (int j = 0; j < 4; j++) {
      u32x4 fv;
#pragma unroll
      for (int r = 0; r < 4; r++) fv[r] = (unsigned)*(const unsigned short*)(fp + r * 4352 + j * 8) | ((unsigned)*(const unsigned short*)(fp + (4 + r) * 4352 + j * 8) << 16);
      stc(rs, WS_WT + (wline >> 7) * BP + (cb + 4 * j) * 128 + sl * 16, fv);
    }
    if (w == 7 && lane < 8) {
      u32x4 v;
#pragma unroll
      for (int r = 0; r < 4; r++) v[r] = (unsigned)*(const unsigned short*)(LF + sl * 514 + 512 + r * 4352) | ((unsigned)*(const unsigned short*)(LF + sl * 514 + 512 + (4 + r) * 4352) << 16);
      stc(rs, WS_WT + (wline >> 7) * BP + 256 * 128 + sl * 16, v);
    }
  }
  if (w == 3 && lane < 8) stc(rs, WS_X1T + (xline >> 7) * BP + 256 * 128 + lane * 16, *(const u32x4*)(LS + 256 * 128 + lane * 16));
  TS(15);
  const u32x4 ones = {0x3f803f80u, 0x3f803f80u, 0x3f803f80u, 0x3f803f80u};
  float gsum[4];
#pragma unroll
  for (int j = 0; j < 4; j++) {
    u32x4 xv = *(const u32x4*)(LX + (w * 4 + j) * 1024 + lane * 16);
    u32x4 pr;
#pragma unroll
    for (int d = 0; d < 4; d++) pr[d] = pk(bflo(xv[d]) * bflo(gwv[d]), bfhi(xv[d]) * bfhi(gwv[d]));
    gsum[j] = mma(as8(ones), as8(pr), f32x4{0, 0, 0, 0})[0];
  }
  float gs = (g == 0) ? gsum[0] : (g == 1) ? gsum[1] : (g == 2) ? gsum[2] : gsum[3];
  gs = rbf(dpp_sum16(gs));
  const float gg = rbf(__builtin_amdgcn_rcpf(1.f + __builtin_amdgcn_exp2f(gs * -1.44269504088896341f)));
  const int s = 32 * (4 * (a >> 2) + g) + n2s;
  if (i == 0) stc1(rs, WS_GT + (s >> 6) * 256 + (s & 15) * 16 + ((s >> 4) & 3) * 4, pk(gg, 1.f - gg));
}

template <class F> DI void p2_fft(TSP F idle, char* lb, char* fl, char* f1seen, const char* ws, rsrc_t rs, int c, int cnt, int rep, unsigned& ep, int w, int lane) {
  const int i = lane & 15, g = lane >> 4;
  const int k1 = 16 * w + i;
  const float fk1 = (float)k1, fg = (float)g;
  u32x4 fre[4], fim[4], fc[2], fs[2], fn[2];
  {
    const float st = fk1 * (1.f / 64.f);
    const float a0 = fg * 4.f * st;
    const bool odd = i & 1;
    const unsigned m2 = odd ? 0x80008000u : 0u;
    const unsigned mre = (i & 2) ? 0x80008000u : 0u;
    const unsigned mim = ((i + 1) & 2) ? 0x80008000u : 0u;
#pragma unroll
    for (int d = 0; d < 4; d++) {
      const float ang = __builtin_fmaf((float)d, st, a0);
      const float cs = fcos(ang), sn = fsin(ang);
      const unsigned r0 = pk(cs, sn), i0 = pk(sn, cs) ^ 0x8000u;
      const unsigned r1 = (odd ? i0 : r0) ^ mre, i1 = (odd ? r0 : i0) ^ mim;
      fre[0][d] = r0; fim[0][d] = i0;
      fre[1][d] = r1; fim[1][d] = i1;
      fre[2][d] = r0 ^ m2; fim[2][d] = i0 ^ m2;
      fre[3][d] = r1 ^ m2; fim[3][d] = i1 ^ m2;
    }
#pragma unroll
    for (int ks = 0; ks < 4; ks++) { PIN(fre[ks]); PIN(fim[ks]); }
  }
  idle(0);
  float c1[2][4], s1[2][4], c2[2][4], s2[2][4];
#pragma unroll
  for (int mt = 0; mt < 2; mt++) {
    const float fa = (float)perm32(mt, i);
    const float st = fa * (1.f / 32.f), a0 = fg * 8.f * st;
    const float st1 = fk1 * (1.f / 2048.f), b0 = (fg * 8.f + (float)(4 * mt)) * st1;
    const float st2 = fa * (1.f / 2048.f), d0 = (fg * 4.f + (float)(16 * w)) * st2;
#pragma unroll
    for (int d = 0; d < 4; d++) {
      const float x0 = __builtin_fmaf((float)(2 * d), st, a0), x1 = __builtin_fmaf((float)(2 * d + 1), st, a0);
      fc[mt][d] = pk(fcos(x0), fcos(x1));
      fs[mt][d] = pk(fsin(x0), fsin(x1));
      fn[mt][d] = fs[mt][d] ^ 0x80008000u;
      const float t1 = __builtin_fmaf((float)d, st1, b0);
      c1[mt][d] = fcos(t1); s1[mt][d] = fsin(t1);
      const float t2 = __builtin_fmaf((float)d, st2, d0);
      c2[mt][d] = fcos(t2); s2[mt][d] = fsin(t2);
      PIN(c1[mt][d]); PIN(s1[mt][d]); PIN(c2[mt][d]); PIN(s2[mt][d]);
    }
    PIN(fc[mt]); PIN(fs[mt]); PIN(fn[mt]);
    idle(1 + mt);
  }
  const unsigned yt = WS_YT + (32 * k1 + 8 * g) * 2 + ((c == 256) ? 1 : (c & 1) * 256 + (c & ~1)) * YP;
  stc(rs, yt, u32x4{SENT, SENT, SENT, SENT});
  if (c & 255) stc(rs, yt + YP, u32x4{SENT, SENT, SENT, SENT});
  idle(3);
  TS(4);
  if (w == 0) { gwait<SLP>(rs, WS_F1 + rep * 128, ep); lset(f1seen); }
  else lwait(f1seen, 1);
  TS(5);
  const unsigned sv = (w * 16 + (lane >> 3)) * BP + c * 128 + (lane & 7) * 16;
  asm volatile("s_mov_b32 m0, %0\n\ts_nop 0\n\tglobal_load_lds_dwordx4 %2, %1 sc1\n\tglobal_load_lds_dwordx4 %3, %1 offset:1024 sc1"
               :: "s"(laddr_of(lb) + 8192 + w * 2048), "s"(ws + WS_X1T), "v"(sv), "v"(sv + 8 * BP - 1024) : "memory");
  u32x4 wf[2];
#pragma unroll
  for (int mt = 0; mt < 2; mt++) wf[mt] = ldc(rs, WS_WT + sv + mt * 8 * BP);
  WAITV(2);
  linc(fl, lane);
  const unsigned tk = ticket_issue(rs, 50 + w * 4 + cnt, lane);
  lwait(fl, 4);
  TS(6);
  const f32x4 z4 = {0, 0, 0, 0};
  f32x4 re[2], im[2];
#pragma unroll
  for (int mt = 0; mt < 2; mt++) { re[mt] = z4; im[mt] = z4; }
#pragma unroll
  for (int ks = 0; ks < 4; ks++)
#pragma unroll
    for (int mt = 0; mt < 2; mt++) {
      s16x8 xa = lrd(lb + 8192 + ((mt * 4 + ks) * 64 + lane) * 16);
      re[mt] = mma(xa, as8(fre[ks]), re[mt]); im[mt] = mma(xa, as8(fim[ks]), im[mt]);
    }
  u32x4 pr, pi;
#pragma unroll
  for (int mt = 0; mt < 2; mt++) {
    float tr[4], ti[4];
#pragma unroll
    for (int r = 0; r < 4; r++) { tr[r] = re[mt][r] * c1[mt][r] + im[mt][r] * s1[mt][r]; ti[r] = im[mt][r] * c1[mt][r] - re[mt][r] * s1[mt][r]; }
    pr[2 * mt] = pk(tr[0], tr[1]); pr[2 * mt + 1] = pk(tr[2], tr[3]);
    pi[2 * mt] = pk(ti[0], ti[1]); pi[2 * mt + 1] = pk(ti[2], ti[3]);
  }
#pragma unroll
  for (int mt = 0; mt < 2; mt++) {
    re[mt] = mma(as8(fs[mt]), as8(pi), mma(as8(fc[mt]), as8(pr), z4));
    im[mt] = mma(as8(fn[mt]), as8(pr), mma(as8(fc[mt]), as8(pi), z4));
  }
#pragma unroll
  for (int mt = 0; mt < 2; mt++) {
    float tr[4], ti[4];
#pragma unroll
    for (int r = 0; r < 4; r++) {
      const float a = bflo(wf[mt][r]), b = bfhi(wf[mt][r]);
      tr[r] = re[mt][r] * a - im[mt][r] * b; ti[r] = re[mt][r] * b + im[mt][r] * a;
    }
    pr[2 * mt] = pk(tr[0], tr[1]); pr[2 * mt + 1] = pk(tr[2], tr[3]);
    pi[2 * mt] = pk(ti[0], ti[1]); pi[2 * mt + 1] = pk(ti[2], ti[3]);
  }
#pragma unroll
  for (int nt = 0; nt < 2; nt++) {
    re[nt] = mma(as8(pi), as8(fn[nt]), mma(as8(pr), as8(fc[nt]), z4));
    im[nt] = mma(as8(pi), as8(fc[nt]), mma(as8(pr), as8(fs[nt]), z4));
  }
#pragma unroll
  for (int nt = 0; nt < 2; nt++) {
    u32x4 v;
#pragma unroll
    for (int r = 0; r < 4; r++) v[r] = pk(re[nt][r] * c2[nt][r] - im[nt][r] * s2[nt][r], -re[nt][r] * s2[nt][r] - im[nt][r] * c2[nt][r]);
    *(u32x4*)(lb + ((nt * 4 + w) * 64 + lane) * 16) = v;
  }
  linc(fl + 4, lane);
  lwait(fl + 4, 4);
#pragma unroll
  for (int mt = 0; mt < 2; mt++) { re[mt] = z4; im[mt] = z4; }
#pragma unroll
  for (int ks = 0; ks < 4; ks++)
#pragma unroll
    for (int mt = 0; mt < 2; mt++) {
      s16x8 ya = lrd(lb + ((mt * 4 + ks) * 64 + lane) * 16);
      re[mt] = mma(ya, as8(fre[ks]), re[mt]); im[mt] = mma(ya, as8(fim[ks]), im[mt]);
    }
  u32x4 vr = {pk(re[0][0], re[0][1]), pk(re[0][2], re[0][3]), pk(re[1][0], re[1][1]), pk(re[1][2], re[1][3])};
  u32x4 vi = {pk(im[0][0], im[0][1]), pk(im[0][2], im[0][3]), pk(im[1][0], im[1][1]), pk(im[1][2], im[1][3])};
  const bool last = (unsigned)__builtin_amdgcn_readfirstlane(tk) == (cnt == 0 ? 64u : 63u);
  stc(rs, yt, vr);
  if (c & 255) stc(rs, yt + YP, vi);
  if (last && lane == 0) stc(rs, WS_F3 + (w * 4 + cnt) * 128, u32x4{ep, ep, ep, ep});
  TS(7);
}

extern "C" __global__ __launch_bounds__(512, 2) void gm_all(const char* x, const char* wqkv, const char* wr, const char* wi, const char* gw, const char* wo, char* ws, char* out) {
  __shared__ __attribute__((aligned(16))) char lds[163840];
  const int b = blockIdx.x, xcd = b & 7, ii = b >> 3;
  const int w8 = __builtin_amdgcn_readfirstlane(threadIdx.x >> 6), lane = threadIdx.x & 63;
  const int w = w8 & 3;
  TSDECL
  TS(0);
  if (ii < 2 && w8 < 4) {
    unsigned long long pc; int o0, o1;
    asm volatile("s_getpc_b64 %0\n.Lgm_pc%=:\n\ts_mov_b32 %1, gm_all-.Lgm_pc%=\n\ts_mov_b32 %2, gm_end-.Lgm_pc%=" : "=s"(pc), "=s"(o0), "=s"(o1));
    const unsigned off = (ii * 256 + threadIdx.x) * 128;
    if (off < (unsigned)(o1 - o0))
      asm volatile("s_mov_b32 m0, %0\n\ts_nop 0\n\tglobal_load_lds_dword %2, %1" :: "s"(laddr_of(lds) + L_ATT + 8192), "s"(pc + (long long)o0), "v"(off) : "memory");
  }
  unsigned ep;
  const char* epp = ws + WS_E;
  const rsrc_t rs = __builtin_amdgcn_make_buffer_rsrc(ws, 0, WS_END, 0x00020000);
  char* FL = lds + L_FL;
  const int ml = ii & 3, nl = ii >> 2;
  const int mtile = xcd * 4 + ml, ntile = nl;
  const bool aux = ii >= 24;
  {
    const int m = (xcd >> 1) * 4 + ml;
    const int nt = nl < 4 ? (xcd & 1) * 4 + nl : 8 + (xcd & 1) * 2 + (nl - 4);
    if (w8 < 4 || aux) {
      if (w8 == 0) *(LAS unsigned*)(FL + lane * 4) = 0;
      if (aux) p1_rows(TSA lds, x, gw, wo, wr, wi, rs, xcd * 8 + (ii - 24), epp, w8, lane);
      if (w8 < 4) {
        __builtin_amdgcn_s_setprio(2);
        if (aux) {
          ep = epoch_get(lds);
          if (w == 1) gwait<SLP>(rs, WS_F0 + xcd * 128, ep);
          TS(4);
          WAITV(0);
          TS(5);
          BAR();
          TS(6);
          if (w == 1) { arrive(rs, 0, 64, WS_F1, 8, ep, lane); lset(FL + 124); }
          lwait(FL + 124, 1);
#pragma unroll 1
          for (int j = 0; j < 4; j++) wo_dma(lds, wo, ntile, j, w, lane);
        }
        else if (nl < 4) p1_gemm<false>(TSA lds, wr, wi, rs, m, nt, ep, epp, w, lane);
        else p1_gemm<true>(TSA lds, wr, wi, rs, m, nt, ep, epp, w, lane);
        __builtin_amdgcn_s_setprio(0);
      }
    } else {
      const int col0 = nl >= 4 ? (nt - 8) * 128 : 512 + nt * 64;
      const int col1 = nl >= 4 ? col0 + 64 : col0 + 512;
      p1_prod(lds, x, wqkv, wo, ntile, m, col0, col1, (2 * ml + nl) & ROT1, w, lane);
    }
  }
  TS(3);
  const int i = lane & 15, g = lane >> 4, q = i >> 2, p = lane & 3;
  const int fsw = (2 * g + (q >> 1)) & 3;
  if (w8 >= 4 || b == SPECIAL) {
    const bool pg = w8 >= 4;
    if (pg) {
      ep = epoch_get(lds);
      if (!aux) {
        if (w == 0) { gwait<SLP>(rs, WS_F0 + xcd * 128, ep); lset(FL + 100); }
        wo_dma(lds, wo, ntile, 2, w, lane); wo_dma(lds, wo, ntile, 3, w, lane);
      }
    }
    if (!aux && nl >= 4) __builtin_amdgcn_s_setprio(3); else __builtin_amdgcn_s_setprio(1);
    auto idle = [&](int st) {
      if (st == AST && pg && aux) { WAITV(0); BAR(); }
      if (st == 3 && !aux) { WAITV(0); linc(FL, lane); }
    };
    p2_fft(TSA idle, pg ? lds + L_FFT : lds + L_Y, pg ? FL + 4 : FL + 20, pg ? FL + 16 : FL + 28, ws, rs, pg ? b : 256, pg ? (b & 1) * 2 + (b >> 7) : 0, xcd, ep, w, lane);
  }
  if (w8 < 4) {
    linc(FL + 80, lane);
    if (aux) { if (b != SPECIAL) WAITV(0); linc(FL, lane); }
    TS(7);
    const int stg = 4 * mtile + w;
    bool kok = false;
    if (w == 0) {
#pragma unroll 1
      for (int sp = 0; sp < 200000; sp++) {
        FENCE();
        const bool ok = ldc(rs, (lane & 1) ? WS_F2K + xcd * 128 : WS_F2Q + (mtile >> 1) * 128)[0] == ep;
        const unsigned long long mk = __ballot(ok);
        kok = mk & 2;
        if (mk & 1) break;
        __builtin_amdgcn_s_sleep(SLP);
      }
      FENCE();
      if (kok) lset(FL + 88);
      lset(FL + 104);
    } else lwait(FL + 104, 1);
    u32x4 pq[4][4];
#pragma unroll
    for (int jj = 0; jj < 4; jj++)
#pragma unroll
      for (int u = 0; u < 4; u++) pq[jj][u] = ldc(rs, WS_PQ + (((4 * mtile + ((w + 1 + jj) & 3)) * 16 + 4 * w + u) * 64 + lane) * 16);
    lwait(FL, 4);
    s16x8 wf[4][4];
    {
      const int toffA = L_WO + w * 16384 + (4 * g + q) * 128 + (fsw * 32) + p * 8;
#pragma unroll
      for (int u = 0; u < 4; u++)
#pragma unroll
        for (int a = 0; a < 4; a++) { const char* pp = lds + (toffA ^ (32 * a)) + u * 2048; wf[u][a] = trd2(pp, pp + 8192); }
    }
    if (w == 0) { if (!kok) { gwait<SLP>(rs, WS_F2K + xcd * 128, ep); lset(FL + 88); } }
    else lwait(FL + 88, 1);
    TS(8);
    u32x4 kr[6];
#pragma unroll 1
    for (int sp = 0; sp < 20000; sp++) {
      FENCE();
      bool bad = false;
#pragma unroll
      for (int j = 0; j < 6; j++) kr[j] = ldc(rs, WS_KVR + ((j < 4 ? 4 * w + j : 12 + 2 * w + j) * 64 + lane) * 16);
#pragma unroll
      for (int j = 0; j < 6; j++) bad |= has_sent(kr[j]);
      if (!__any(bad)) break;
    }
    lwait(FL + 16, 1);
    const unsigned gv = __builtin_amdgcn_raw_buffer_load_b32(rs, WS_GT + mtile * 256 + i * 16 + w * 4, 0, 16);
    TS(13);
    const f32x4 z4 = {0, 0, 0, 0};
    const f32x4 eps = {1e-6f, 1e-6f, 1e-6f, 1e-6f};
    f32x4 acc[4];
    f32x4 nm[2][4][2], dn[2][4];
    auto nd = [&](int jj) {
#pragma unroll
      for (int u = 0; u < 4; u++) {
        dn[jj & 1][u] = mma(as8(kr[4 + (u >> 1)]), as8(pq[jj][u]), eps);
        nm[jj & 1][u][0] = mma16(as4(u32x2{kr[u][0], kr[u][1]}), as4(u32x2{pq[jj][u][0], pq[jj][u][1]}), z4);
        nm[jj & 1][u][1] = mma16(as4(u32x2{kr[u][2], kr[u][3]}), as4(u32x2{pq[jj][u][2], pq[jj][u][3]}), z4);
      }
    };
    u32x4 of[2][4];
    auto sc = [&](int jj) {
#pragma unroll
      for (int u = 0; u < 4; u++) {
        const f32x4 n0 = nm[jj & 1][u][0] * __builtin_amdgcn_rcpf(dn[jj & 1][u][2 * (u & 1)]);
        const f32x4 n1 = nm[jj & 1][u][1] * __builtin_amdgcn_rcpf(dn[jj & 1][u][2 * (u & 1) + 1]);
        of[jj & 1][u] = u32x4{pk(n0[0], n0[1]), pk(n0[2], n0[3]), pk(n1[0], n1[1]), pk(n1[2], n1[3])};
      }
    };
    auto pj = [&](int jj) {
#pragma unroll
      for (int u = 0; u < 4; u++)
#pragma unroll
        for (int a = 0; a < 4; a++) acc[a] = mma(wf[u][a], as8(of[jj & 1][u]), u ? acc[a] : z4);
      if (jj < 3) {
#pragma unroll
        for (int a = 0; a < 4; a++) *(f32x4*)(lds + L_WO + w * 16384 + (jj * 4 + a) * 1024 + lane * 16) = acc[a];
      }
    };
    nd(0); SCHED();
    nd(1); sc(0);
#pragma unroll
    for (int t = 0; t < 12; t++) { SGB(0x008, 1); SGB(0x002, 3); }
    SCHED();
#pragma unroll
    for (int jj = 0; jj < 3; jj++) {
      pj(jj); sc(jj + 1);
#pragma unroll
      for (int t = 0; t < 16; t++) { SGB(0x008, 1); SGB(0x002, 3); }
      SCHED();
      if (jj < 2) { nd(jj + 2); SCHED(); }
    }
    linc(FL + 92, lane);
    pj(3);
    TS(9);
    lwait(FL + 92, 4);
#pragma unroll
    for (int ww = 1; ww < 4; ww++)
#pragma unroll
      for (int a = 0; a < 4; a++) acc[a] += *(const f32x4*)(lds + L_WO + ((w + ww) & 3) * 16384 + ((3 - ww) * 4 + a) * 1024 + lane * 16);
    lwait(FL + 48, 4);
    TS(10);
    const float ga = bflo(gv), gb = bfhi(gv);
    unsigned pv[4][2];
#pragma unroll
    for (int a = 0; a < 2; a++) {
      const f32x4 t0 = *(const f32x4*)(lds + L_FFT + ((w * 4 + a) * 64 + lane) * 16);
      const f32x4 t1 = *(const f32x4*)(lds + L_FFT + ((w * 4 + a + 2) * 64 + lane) * 16);
      const f32x4 v0 = acc[a] * ga + (t0 + t1) * gb, v1 = acc[a + 2] * ga + (t0 - t1) * gb;
      pv[a][0] = pk(v0[0], v0[1]); pv[a][1] = pk(v0[2], v0[3]);
      pv[a + 2][0] = pk(v1[0], v1[1]); pv[a + 2][1] = pk(v1[2], v1[3]);
    }
#pragma unroll
    for (int a = 0; a < 2; a++) {
      auto s0 = __builtin_amdgcn_permlane16_swap(pv[2 * a][0], pv[2 * a + 1][0], false, false);
      auto s1 = __builtin_amdgcn_permlane16_swap(pv[2 * a][1], pv[2 * a + 1][1], false, false);
      u32x4 v = {s0[0], s1[0], s0[1], s1[1]};
      char* op = out + (size_t)(16 * stg + i) * 1024 + (256 * a + 32 * ntile + 16 * (g & 1) + 8 * (g >> 1)) * 2;
      asm volatile("global_store_dwordx4 %0, %1, off sc1\n\ts_nop 1" :: "v"(op), "v"(v) : "memory");
    }
    TS(11);
    TSFLUSH();
    return;
  }
  {
    const float sgn = (i & 1) ? -1.f : 1.f;
    const unsigned k0v = (i & 1) ? 0xb5803580u : 0x35803580u;
    const float fn = (float)(32 * ntile + 16 * (w & 1) + i) * (1.f / 512.f);
    const float t2 = 2.f * fcos(fn * 32.f);
    f32x2 c0[4], c1[4];
#pragma unroll
    for (int d = 0; d < 4; d++) {
      const float kf = (float)(4 * g + 128 * (w & 1) + (w >> 1)) + (float)(16 * (d >> 1) + 2 * (d & 1));
      const float a0 = kf * fn, a1 = (kf + 32.f) * fn;
      c0[d] = f32x2{fcos(a0), fsin(a0)} * 0x1p-19f;
      c1[d] = f32x2{fcos(a1), fsin(a1)} * 0x1p-19f;
    }
    u32x4 gf[8];
#pragma unroll
    for (int j = 0; j < 2; j++) {
#pragma unroll
      for (int h = 0; h < 2; h++) {
#pragma unroll
        for (int d = 0; d < 4; d++) {
          gf[4 * j + 2 * h][d] = pk(c0[d][0], c0[d][1]);
          gf[4 * j + 2 * h + 1][d] = pk(c1[d][0], c1[d][1]);
          c0[d] = c1[d] * t2 - c0[d];
        }
#pragma unroll
        for (int d = 0; d < 4; d++) c1[d] = c0[d] * t2 - c1[d];
      }
      gf[4 * j][0] = (w == j && g == 0) ? k0v : gf[4 * j][0];
      if (j == 0) {
        const float sg = (w & 1) ? sgn : 1.f;
#pragma unroll
        for (int d = 0; d < 4; d++) { c0[d] *= sg; c1[d] *= sg; }
      }
    }
#pragma unroll
    for (int k = 0; k < 8; k++) PIN(gf[k]);
    TS(8);
    gwait<SLP>(rs, WS_F3 + ((mtile >> 3) * 4 + w) * 128, ep);
    lwait(FL + 80, 4);
    linc(FL + 96, lane);
    TS(9);
    const int toffB = L_Y + (4 * g + q) * 128 + (fsw * 32) + p * 8;
    f32x4 accb[4];
    s16x8 tb_[2][2][4];
    auto rd = [&](int u) {
      const char* slot = lds + (w ^ (u >> 1)) * 16384 + (u & 1) * 8192;
#pragma unroll
      for (int ks = 0; ks < 2; ks++)
#pragma unroll
        for (int c = 0; c < 4; c++) tb_[u & 1][ks][c] = trd2(slot + (toffB ^ (32 * c)) + ks * 4096, slot + (toffB ^ (32 * c)) + ks * 4096 + 2048);
    };
    auto mm = [&](int u) {
#pragma unroll
      for (int ks = 0; ks < 2; ks++)
#pragma unroll
        for (int c = 0; c < 4; c++) accb[c] = mma(as8(gf[2 * u + ks]), tb_[u & 1][ks][c], accb[c]);
    };
#pragma unroll 1
    for (int sp = 0; sp < 20000; sp++) {
      const unsigned v = (lane >> 3) * YP + ((((lane & 7) >> 1) ^ ((lane >> 4) & 3)) * 32) + (lane & 1) * 16;
      const char* src = ws + WS_YT + (size_t)(w * 128) * YP + mtile * 128;
      const unsigned la = laddr_of(lds) + L_Y + w * 16384;
#pragma unroll
      for (int j = 0; j < 4; j++) glds4c(la + j * 4096, src + (size_t)j * (32 * YP), v, v + (8 * YP - 1024), v + 2 * (8 * YP - 1024), v + 3 * (8 * YP - 1024));
#pragma unroll
      for (int c = 0; c < 4; c++) accb[c] = f32x4{0, 0, 0, 0};
      WAITV(8);
      rd(0);
      WAITV(0);
      TS(10);
      rd(1); mm(0);
#pragma unroll
      for (int t = 0; t < 8; t++) { SGB(0x100, 2); SGB(0x008, 1); }
      SCHED();
      mm(1);
      const f32x4 s4 = (accb[0] + accb[1]) + (accb[2] + accb[3]);
      const float s1 = (s4[0] + s4[1]) + (s4[2] + s4[3]);
      if (!__any(s1 != s1)) break;
      bool bad = false;
#pragma unroll 1
      for (int j = 0; j < 16; j++) bad |= has_sent(*(const u32x4*)(lds + L_Y + w * 16384 + j * 1024 + lane * 16));
      if (!__any(bad)) break;
    }
    lset(FL + 32 + w * 4);
    lwait(FL + 32 + (w ^ 1) * 4, 1);
    rd(2);
    SCHED();
    rd(3); mm(2);
#pragma unroll
    for (int t = 0; t < 8; t++) { SGB(0x100, 2); SGB(0x008, 1); }
    SCHED();
    mm(3);
    lwait(FL + 96, 4);
    if (b == 0 && w == 1 && lane == 0) stc1(rs, WS_E, ep);
#pragma unroll
    for (int c = 0; c < 4; c++) *(f32x4*)(lds + L_FFT + ((c * 4 + w) * 64 + lane) * 16) = accb[c];
    linc(FL + 48, lane);
    TS(11);
    TSFLUSH();
  }
}

extern "C" __global__ void gm_end() {}

extern "C" void kernel_launch(void* const* d_in, const int* in_sizes, int n_in, void* d_out, int out_size, void* d_ws, size_t ws_size, hipStream_t stream) {
  gm_all<<<256, 512, 0, stream>>>((const char*)d_in[0], (const char*)d_in[1], (const char*)d_in[4], (const char*)d_in[5], (const char*)d_in[6],
                                  (const char*)d_in[2], (char*)d_ws, (char*)d_out);
}
```

```cpp
#include <hip/hip_runtime.h>
#include <stdint.h>

typedef short s16x4 __attribute__((ext_vector_type(4)));
typedef short s16x8 __attribute__((ext_vector_type(8)));
typedef float f32x4 __attribute__((ext_vector_type(4)));
typedef float f32x2 __attribute__((ext_vector_type(2)));
typedef unsigned u32x4 __attribute__((ext_vector_type(4)));
typedef unsigned u32x2 __attribute__((ext_vector_type(2)));
typedef __bf16 bfx2 __attribute__((ext_vector_type(2)));
#define LAS __attribute__((address_space(3)))
#define DI __device__ __forceinline__
#define ROT1 6
#define BPAD 128
#define WE 0
#define QPF 0
#define AST 0
#define CSTR 4352
#define PITC 8320
#define PITY 4224
#define SLP 2

static constexpr unsigned BP = 257 * 128 + BPAD;
static constexpr unsigned CP = PITC, YP = PITY;
static constexpr unsigned WS_X1T = 0;
static constexpr unsigned WS_WT  = WS_X1T + (9u << 18);
static constexpr unsigned WS_PQ  = WS_WT + (9u << 18);
static constexpr unsigned WS_KVP = WS_PQ + (2u << 20);
static constexpr unsigned WS_GT  = WS_KVP + (512u << 10);
static constexpr unsigned WS_KVR = WS_GT + (16u << 10);
static constexpr unsigned WS_YT  = WS_KVR + (32u << 10);
static constexpr unsigned WS_E   = WS_YT + (9u << 18);
static constexpr unsigned WS_CNT = WS_E + 4096;
static constexpr unsigned CS = CSTR;
static constexpr unsigned WS_F0  = WS_CNT + 128 * CS;
static constexpr unsigned WS_F1  = WS_F0 + 8 * 128;
static constexpr unsigned WS_F2K = WS_F1 + 8 * 128;
static constexpr unsigned WS_F2Q = WS_F2K + 8 * 128;
static constexpr unsigned WS_F3  = WS_F2Q + 16 * 128;
static constexpr unsigned WS_F5  = WS_F3 + 16 * 128;
static constexpr unsigned WS_END = WS_F5 + 8 * 128;
static constexpr unsigned SENT = 0xffffffffu;
static constexpr int SPECIAL = 255;

static constexpr int L_WO = 0, L_Y = 65536, L_FFT = 131072, L_ATT = 147456, L_FL = 163840 - 256;

DI unsigned pk(float lo, float hi) { f32x2 p = {lo, hi}; return __builtin_bit_cast(unsigned, __builtin_convertvector(p, bfx2)); }
DI float bflo(unsigned u) { return __builtin_bit_cast(float, u << 16); }
DI float bfhi(unsigned u) { return __builtin_bit_cast(float, u & 0xffff0000u); }
DI float rbf(float v) { return bflo(pk(v, 0.f)); }
DI s16x8 as8(u32x4 v) { return __builtin_bit_cast(s16x8, v); }
DI s16x4 as4(u32x2 v) { return __builtin_bit_cast(s16x4, v); }
DI f32x4 mma(s16x8 a, s16x8 b, f32x4 c) { return __builtin_amdgcn_mfma_f32_16x16x32_bf16(a, b, c, 0, 0, 0); }
DI f32x4 mma16(s16x4 a, s16x4 b, f32x4 c) { return __builtin_amdgcn_mfma_f32_16x16x16bf16_1k(a, b, c, 0, 0, 0); }
DI void mfa(f32x4& c, s16x8 a, s16x8 b) { asm volatile("v_mfma_f32_16x16x32_bf16 %0, %1, %2, %0" : "+v"(c) : "v"(a), "v"(b)); }
DI s16x4 trd(const char* l) { return __builtin_amdgcn_ds_read_tr16_b64_v4i16((LAS s16x4*)l); }
DI s16x8 cat(s16x4 a, s16x4 b) { return __builtin_shufflevector(a, b, 0, 1, 2, 3, 4, 5, 6, 7); }
DI s16x8 trd2(const char* l0, const char* l1) { return cat(trd(l0), trd(l1)); }
DI s16x8 lrd(const char* l) { return *(const s16x8*)l; }
DI float fsin(float rev) { return __builtin_amdgcn_sinf(rev); }
DI float fcos(float rev) { return __builtin_amdgcn_cosf(rev); }
DI unsigned laddr_of(const char* l) { return (unsigned)(size_t)(const LAS char*)l; }
DI void glds4(unsigned laddr, const char* sb, unsigned v0, unsigned v1, unsigned v2, unsigned v3) {
  asm volatile("s_mov_b32 m0, %0\n\ts_nop 0\n\t"
               "global_load_lds_dwordx4 %2, %1\n\t"
               "global_load_lds_dwordx4 %3, %1 offset:1024\n\t"
               "global_load_lds_dwordx4 %4, %1 offset:2048\n\t"
               "global_load_lds_dwordx4 %5, %1 offset:3072"
               :: "s"(laddr), "s"(sb), "v"(v0), "v"(v1), "v"(v2), "v"(v3) : "memory");
}
DI void glds4c(unsigned laddr, const char* sb, unsigned v0, unsigned v1, unsigned v2, unsigned v3) {
  asm volatile("s_mov_b32 m0, %0\n\ts_nop 0\n\t"
               "global_load_lds_dwordx4 %2, %1 sc1\n\t"
               "global_load_lds_dwordx4 %3, %1 offset:1024 sc1\n\t"
               "global_load_lds_dwordx4 %4, %1 offset:2048 sc1\n\t"
               "global_load_lds_dwordx4 %5, %1 offset:3072 sc1"
               :: "s"(laddr), "s"(sb), "v"(v0), "v"(v1), "v"(v2), "v"(v3) : "memory");
}
DI int perm32(int t, int i) { return 8 * (i >> 2) + 4 * t + (i & 3); }
DI float phi(float t) {
  float e = __builtin_amdgcn_exp2f(t * 1.44269504088896341f);
  e = __builtin_amdgcn_fmed3f(e, 0.f, 1.f);
  return fmaxf(t + 1e-6f, 1e-6f) + e;
}
#define TSP
#define TSA
#define TSDECL
#define TS(slot)
#define TSFLUSH()
#define SCHED() __builtin_amdgcn_sched_barrier(0)
#define SGB(mask, n) __builtin_amdgcn_sched_group_barrier(mask, n, 0)
#define PIN(x) asm volatile("" : "+v"(x))
#define WAITV(n) asm volatile("s_waitcnt vmcnt(" #n ")" ::: "memory")
#define BAR() asm volatile("s_waitcnt lgkmcnt(0)\n\ts_barrier" ::: "memory")
#define FENCE() asm volatile("" ::: "memory")

typedef __amdgpu_buffer_rsrc_t rsrc_t;
DI u32x4 ldc(rsrc_t r, unsigned off) { return __builtin_amdgcn_raw_buffer_load_b128(r, off, 0, 16); }
DI void stc(rsrc_t r, unsigned off, u32x4 v) { __builtin_amdgcn_raw_buffer_store_b128(v, r, off, 0, 16); }
DI void stc1(rsrc_t r, unsigned off, unsigned v) { __builtin_amdgcn_raw_buffer_store_b32(v, r, off, 0, 16); }
template <int SL> DI void gwait(rsrc_t r, unsigned off, unsigned ep) {
#pragma unroll 1
  for (int sp = 0; sp < 200000; sp++) {
    FENCE();
    const u32x4 f = ldc(r, off);
    if (__all(f[0] == ep && f[1] == ep && f[2] == ep && f[3] == ep)) break;
    __builtin_amdgcn_s_sleep(SL);
  }
  FENCE();
}
DI unsigned ticket(rsrc_t r, int c, int lane) {
  unsigned old = 0;
  if (lane == 0) old = __builtin_amdgcn_raw_ptr_buffer_atomic_add_i32(1, r, WS_CNT + c * CS, 0, 16);
  old = __builtin_amdgcn_readfirstlane(old);
  FENCE();
  return old;
}
DI void arrive(rsrc_t r, int c, unsigned n, unsigned flag, int nrep, unsigned ep, int lane) {
  if (ticket(r, c, lane) == n - 1 && lane < nrep) stc(r, flag + lane * 128, u32x4{ep, ep, ep, ep});
}
DI void linc(char* p, int lane) { if (lane == 0) asm volatile("ds_add_u32 %0, %1" :: "v"(laddr_of(p)), "v"(1u) : "memory"); }
DI void lset(char* p) { asm volatile("ds_write_b32 %0, %1" :: "v"(laddr_of(p)), "v"(1u) : "memory"); }
DI void lwait(const char* p, unsigned want) {
#pragma unroll 1
  for (int sp = 0; sp < 4000000; sp++) {
    if ((unsigned)__builtin_amdgcn_readfirstlane(*(const volatile LAS unsigned*)p) >= want) break;
    __builtin_amdgcn_s_sleep(1);
  }
  FENCE();
}

DI void epoch_dma(char* lds, const char* p, int lane) {
  if (lane == 0) asm volatile("s_mov_b32 m0, %0\n\ts_nop 0\n\tglobal_load_lds_dword %2, %1 sc1" :: "s"(laddr_of(lds) + L_FL - 16), "s"(p), "v"(0u) : "memory");
}
DI unsigned epoch_get(const char* lds) { return (unsigned)__builtin_amdgcn_readfirstlane(*(const volatile LAS unsigned*)(lds + L_FL - 16)) + 1u; }
DI bool has_sent(u32x4 v) { return v[0] == SENT || v[1] == SENT || v[2] == SENT || v[3] == SENT; }
DI unsigned ticket_issue(rsrc_t r, int c, int lane) {
  unsigned old = 0;
  if (lane == 0) old = __builtin_amdgcn_raw_ptr_buffer_atomic_add_i32(1, r, WS_CNT + c * CS, 0, 16);
  return old;
}
DI void ticket_done(rsrc_t r, unsigned old, unsigned n, unsigned flag, int nrep, unsigned ep, int lane) {
  if ((unsigned)__builtin_amdgcn_readfirstlane(old) == n - 1 && lane < nrep) stc(r, flag + lane * 128, u32x4{ep, ep, ep, ep});
}

DI void wo_dma(char* lds, const char* wo, int ntile, int j, int w, int lane) {
  const int blk = ((lane & 7) >> 1) ^ ((lane >> 4) & 3);
  const unsigned v = (lane >> 3) * 1024 + (blk & 1) * 32 + (blk >> 1) * 512 + (lane & 1) * 16;
  glds4(laddr_of(lds) + L_WO + j * 16384 + w * 4096, wo + (size_t)(j * 128 + w * 32) * 1024 + ntile * 64, v, v + 7168, v + 2 * 7168, v + 3 * 7168);
}

DI void p1_prod(char* lds, const char* x, const char* wqkv, const char* wo, int ntile, int m, int col0, int col1, int rot, int w, int lane) {
  unsigned voffA[4], voffB[4];
#pragma unroll
  for (int j = 0; j < 4; j++) {
    const int par = j & 1;
    voffA[j] = (lane >> 3) * 1024 + (((lane & 7) ^ ((4 * par + (lane >> 4)) & 7)) * 16) + j * (8192 - 1024);
    voffB[j] = (lane >> 3) * 3072 + ((((lane & 7) >> 1) ^ (((lane >> 4) & 1) | (par << 1))) * 32) + (lane & 1) * 16 + j * (24576 - 1024);
  }
  const unsigned lbase = laddr_of(lds) + w * 4096;
  const char* xa = x + (size_t)(m * 128 + w * 32) * 1024;
  const char* wb = wqkv + (size_t)((w & 1) * 32) * 3072 + ((w >> 1) ? col1 : col0) * 2;
  auto issue = [&](int idx) {
    const int ks = (idx + rot) & 7;
    const unsigned slot = lbase + (idx & 3) * 32768;
    glds4(slot, xa + ks * 128, voffA[0], voffA[1], voffA[2], voffA[3]);
    glds4(slot + 16384, wb + (size_t)(ks * 64) * 3072, voffB[0], voffB[1], voffB[2], voffB[3]);
  };
  issue(0); issue(1);
  WAITV(8); BAR();
  issue(2); issue(3);
#pragma unroll 1
  for (int j = 1; j < 5; j++) { WAITV(16); BAR(); issue(j + 3); }
  WAITV(16); BAR();
  wo_dma(lds, wo, ntile, 0, w, lane);
  WAITV(12); BAR();
  wo_dma(lds, wo, ntile, 1, w, lane);
  WAITV(8); BAR();
}

template <bool QT>
DI void p1_gemm(TSP char* lds, const char* wr, const char* wi, rsrc_t rs, int m, int nt, unsigned& ep, const char* epp, int w, int lane) {
  const int i = lane & 15, g = lane >> 4, q = i >> 2, p = lane & 3;
  f32x4 acc[8][2];
#pragma unroll
  for (int a = 0; a < 8; a++) { acc[a][0] = f32x4{0, 0, 0, 0}; acc[a][1] = f32x4{0, 0, 0, 0}; }
  const int aoff = i * 128 + ((g ^ (i >> 1)) * 16);
  const int boff = 16384 + (8 * g + q) * 128 + ((w ^ (((q >> 1) & 1) | ((g & 1) << 1))) * 32) + p * 8;
  s16x8 A[2][2][8]; s16x4 B[2][2][2][2];
  auto rd1 = [&](int buf, const char* slot, int r) {
    const int ks = r / 12, rr = r % 12;
    const int t = rr == 0 ? 0 : rr == 1 ? 1 : rr == 3 ? 2 : rr == 4 ? 3 : -1;
    const int a = rr == 2 ? 0 : rr - 4;
    if (t >= 0) B[buf][ks][t >> 1][t & 1] = trd(slot + boff + (t >> 1) * 8192 + ks * 4096 + (t & 1) * 512);
    else A[buf][ks][a] = lrd(slot + (aoff ^ (ks * 64)) + a * 2048);
  };
  auto mf1 = [&](int buf, int ks, int mt, int hh) {
    const s16x8 bf = cat(B[buf][ks][hh][0], B[buf][ks][hh][1]);
    if (QT) mfa(acc[mt][hh], bf, A[buf][ks][mt]); else mfa(acc[mt][hh], A[buf][ks][mt], bf);
  };
  auto region = [&](int cur, const char* slot) {
#pragma unroll
    for (int mi = 0; mi < 32; mi++) {
      mf1(cur, mi >> 4, (mi & 15) >> 1, mi & 1);
      SCHED();
      if (mi < 24) { rd1(cur ^ 1, slot, mi); SCHED(); }
    }
  };
  if (QT && QPF) {
    const unsigned ln = (((blockIdx.x >> 3) & 7) * 2 + (w & 1)) * 64 + lane;
    asm volatile("s_mov_b32 m0, %0\n\ts_nop 0\n\tglobal_load_lds_dword %2, %1" :: "s"(laddr_of(lds) + L_ATT + 8192), "s"(((w >> 1) ? wi : wr) + (size_t)(blockIdx.x & 7) * 131584), "v"(ln * 128u) : "memory");
  }
  if (w == 0) epoch_dma(lds, epp, lane);
  const bool zw = QT && blockIdx.x >= 128 && blockIdx.x < 136;
  if (zw && w < 3) stc(rs, WS_KVR + (((blockIdx.x & 7) * 3 + w) * 64 + lane) * 16, u32x4{SENT, SENT, SENT, SENT});
  if (zw && w == 3 && lane < 9) stc1(rs, WS_CNT + ((blockIdx.x & 7) * 9 + lane) * CS, 0u);
  __builtin_amdgcn_s_waitcnt(0xC07F);
  BAR();
  TS(1);
#pragma unroll
  for (int r = 0; r < 24; r++) rd1(0, lds, r);
  SCHED();
#pragma unroll 1
  for (int it = 0; it < 3; it++) {
    BAR(); SCHED();
    region(0, lds + ((2 * it + 1) & 3) * 32768);
    if (it == (zw ? 0 : 1)) WAITV(0);
    BAR(); SCHED();
    if (zw && it == 0 && w == 3) { const unsigned e = epoch_get(lds); if (lane < 8) stc1(rs, WS_F0 + lane * 128 + (blockIdx.x & 7) * 4, e); }
    region(1, lds + ((2 * it + 2) & 3) * 32768);
  }
  ep = epoch_get(lds);
  BAR(); SCHED();
  region(0, lds + 3 * 32768);
  asm volatile("s_nop 7");
  SCHED();
  auto mmh = [&](int hh) {
#pragma unroll
    for (int mt = 0; mt < 8; mt++)
#pragma unroll
      for (int ks = 0; ks < 2; ks++) {
        const s16x8 bf = cat(B[1][ks][hh][0], B[1][ks][hh][1]);
        acc[mt][hh] = QT ? mma(bf, A[1][ks][mt], acc[mt][hh]) : mma(A[1][ks][mt], bf, acc[mt][hh]);
      }
  };
  mmh(0);
  SCHED();
  TS(2);
  if (QT) {
    mmh(1);
#pragma unroll
    for (int mt = 0; mt < 8; mt++) {
      u32x4 v = {pk(phi(acc[mt][0][0]), phi(acc[mt][0][1])), pk(phi(acc[mt][0][2]), phi(acc[mt][0][3])),
                 pk(phi(acc[mt][1][0]), phi(acc[mt][1][1])), pk(phi(acc[mt][1][2]), phi(acc[mt][1][3]))};
      stc(rs, WS_PQ + (((m * 8 + mt) * 16 + (nt - 8) * 4 + w) * 64 + lane) * 16, v);
    }
    lwait(lds + L_FL + 100, 1);
    WAITV(0);
    arrive(rs, 34 + m, 16, WS_F2Q + m * 128, 1, ep, lane);
  } else {
    mmh(1);
    u32x4 pa[4];
#pragma unroll
    for (int u = 0; u < 4; u++)
      pa[u] = u32x4{pk(phi(acc[2 * u][0][0]), phi(acc[2 * u][0][1])), pk(phi(acc[2 * u][0][2]), phi(acc[2 * u][0][3])),
                    pk(phi(acc[2 * u + 1][0][0]), phi(acc[2 * u + 1][0][1])), pk(phi(acc[2 * u + 1][0][2]), phi(acc[2 * u + 1][0][3]))};
#pragma unroll
    for (int t = 0; t < 16; t++) { SGB(0x008, 1); SGB(0x002, 2); }
    SCHED();
    f32x4 kv = {0, 0, 0, 0}, z = {0, 0, 0, 0};
    const u32x4 ones = {0x3f803f80u, 0x3f803f80u, 0x3f803f80u, 0x3f803f80u};
#pragma unroll
    for (int u = 0; u < 4; u++) {
      u32x4 b = {pk(acc[2 * u][1][0], acc[2 * u][1][1]), pk(acc[2 * u][1][2], acc[2 * u][1][3]),
                 pk(acc[2 * u + 1][1][0], acc[2 * u + 1][1][1]), pk(acc[2 * u + 1][1][2], acc[2 * u + 1][1][3])};
      z = mma(as8(pa[u]), as8(ones), z);
      kv = mma(as8(pa[u]), as8(b), kv);
    }
    const int head = nt * 4 + w;
    u32x4 v = {pk(kv[0], kv[1]), pk(kv[2], kv[3]), pk(z[0], z[1]), pk(z[2], z[3])};
    stc(rs, WS_KVP + ((m * 32 + head) * 64 + lane) * 16, v);
    lwait(lds + L_FL + 100, 1);
    WAITV(0);
    if (ticket(rs, 1 + head, lane) == 15) {
      TS(4);
      u32x4 kl[16];
#pragma unroll
      for (int pp = 0; pp < 16; pp++) kl[pp] = ldc(rs, WS_KVP + ((pp * 32 + head) * 64 + lane) * 16);
      FENCE();
      const unsigned tk = ticket_issue(rs, 33, lane);
      u32x4 r;
#pragma unroll
      for (int d = 0; d < 4; d++) {
        float lo[4], hi[4];
#pragma unroll
        for (int j = 0; j < 4; j++) {
          lo[j] = (bflo(kl[4 * j][d]) + bflo(kl[4 * j + 1][d])) + (bflo(kl[4 * j + 2][d]) + bflo(kl[4 * j + 3][d]));
          hi[j] = (bfhi(kl[4 * j][d]) + bfhi(kl[4 * j + 1][d])) + (bfhi(kl[4 * j + 2][d]) + bfhi(kl[4 * j + 3][d]));
        }
        r[d] = pk((lo[0] + lo[1]) + (lo[2] + lo[3]), (hi[0] + hi[1]) + (hi[2] + hi[3]));
      }
      ticket_done(rs, tk, 32, WS_F2K, 8, ep, lane);
      const int u = (head >> 3) * 4 + (head & 3), hh = (head >> 2) & 1;
      const bool zr = (i & 3) == hh + 2 * (u & 1);
      __builtin_amdgcn_raw_buffer_store_b64(u32x2{r[0], r[1]}, rs, WS_KVR + (u * 64 + lane) * 16 + hh * 8, 0, 16);
      if (zr) stc(rs, WS_KVR + 16384 + ((u >> 1) * 64 + lane) * 16, hh ? u32x4{0u, 0u, r[2], r[3]} : u32x4{r[2], r[3], 0u, 0u});
      TS(5);
    }
  }
}

DI float dpp_sum16(float v) {
  v += __builtin_bit_cast(float, __builtin_amdgcn_update_dpp(0, __builtin_bit_cast(int, v), 0x128, 0xf, 0xf, false));
  v += __builtin_bit_cast(float, __builtin_amdgcn_update_dpp(0, __builtin_bit_cast(int, v), 0x124, 0xf, 0xf, false));
  v += __builtin_bit_cast(float, __builtin_amdgcn_update_dpp(0, __builtin_bit_cast(int, v), 0x122, 0xf, 0xf, false));
  v += __builtin_bit_cast(float, __builtin_amdgcn_update_dpp(0, __builtin_bit_cast(int, v), 0x121, 0xf, 0xf, false));
  return v;
}

DI void p1_rows(TSP char* lds, const char* x, const char* gw, const char* wo, const char* wr, const char* wi, rsrc_t rs, int a, const char* epp, int w, int lane) {
  const int i = lane & 15, g = lane >> 4;
  char* LX = lds;
  char* LS = lds + 32768;
  char* LC = lds + 106496;
  const int amt = (a >> 1) & 1;
  const int n2s = perm32(amt, 8 * (a & 1) + w);
  {
    const unsigned v = lane * 16;
    glds4(laddr_of(LX) + w * 4096, x + (size_t)(128 * (a >> 2) + n2s) * 1024, v, v + 31 * 1024, v + 62 * 1024, v + 93 * 1024);
  }
  char* LF = lds + 69632;
  auto wdma = [&]() {
    const unsigned v = lane * 16;
    const char* fsrc = ((w >> 2) ? wi : wr) + (size_t)(8 * (a & 7) + 64 * (4 * (a >> 3) + (w & 3))) * 514;
    glds4(laddr_of(LF) + w * 4352, fsrc, v, v, v, v);
    if (lane == 0) asm volatile("s_mov_b32 m0, %0\n\ts_nop 0\n\tglobal_load_lds_dwordx4 %2, %1" :: "s"(laddr_of(LF) + w * 4352 + 4096), "s"(fsrc + 4096), "v"(v) : "memory");
  };
  if (WE) wdma();
  {
    const float fi = (float)i;
    const int d = w & 3;
    char* cp = LC + lane * 16 + d * 4;
    const float n2i = (float)(4 * g + d) * fi;
    if (w < 4) {
      float c[2], s[2];
#pragma unroll
      for (int e = 0; e < 2; e++) {
        const float ang = (float)(4 * g + 16 * (d >> 1) + 2 * (d & 1) + e) * (fi * (1.f / 32.f));
        c[e] = fcos(ang);
        s[e] = (i == 0) ? (e ? -1.f : 1.f) : fsin(-ang);
      }
      *(unsigned*)(cp) = pk(c[0], c[1]);
      *(unsigned*)(cp + 1024) = pk(s[0], s[1]);
      *(float*)(cp + 6144) = fcos(n2i * (1.f / 512.f));
      *(float*)(cp + 7168) = fsin(n2i * (1.f / 512.f));
    } else {
      {
        const float ang = n2i * (1.f / 16.f);
        const float cs = fcos(ang), sn = fsin(ang);
        *(unsigned*)(cp + 2048) = pk(cs, sn);
        *(unsigned*)(cp + 3072) = pk(sn, cs) ^ ((i >= 8) ? 0x80000000u : 0x00008000u);
      }
      {
        const float ang = n2i * (1.f / 32.f);
        const float cs = fcos(ang);
        const float sn = (i == 0) ? ((d & 1) ? -1.f : 1.f) : fsin(-ang);
        const unsigned u = pk(cs, sn);
        const int sh = (i & 1) * 16;
        *(unsigned*)(cp + 4096) = (u & 0xffffu) << sh;
        *(unsigned*)(cp + 5120) = (u >> 16) << sh;
      }
    }
  }
  int soff[4];
#pragma unroll
  for (int r = 0; r < 4; r++) {
    const int k2 = 4 * g + r;
    const int k = i + 32 * k2;
    const int col = (i == 0) ? 16 * k2 : (k2 < 8 ? k : 512 - k);
    soff[r] = col * 128 + ((w ^ (col & 7)) * 16);
    PIN(soff[r]);
  }
  TS(12);
  BAR();
  if (!WE) wdma();
  epoch_dma(lds, epp, lane);
  const u32x4 gwv = *(const u32x4*)(gw + lane * 16);
  asm volatile("s_mov_b32 m0, %0\n\ts_nop 0\n\tglobal_load_lds_dword %2, %1" :: "s"(laddr_of(lds) + L_ATT + 8192), "s"(wo), "v"((unsigned)((a * 8 + w) * 8 + (lane & 7)) * 128u) : "memory");
  const u32x4 b1re = *(const u32x4*)(LC + lane * 16), b1im = *(const u32x4*)(LC + 1024 + lane * 16);
  const u32x4 a3re = *(const u32x4*)(LC + 2048 + lane * 16), a3im = *(const u32x4*)(LC + 3072 + lane * 16);
  const u32x4 a4re = *(const u32x4*)(LC + 4096 + lane * 16), a4im = *(const u32x4*)(LC + 5120 + lane * 16);
  const f32x4 twc = *(const f32x4*)(LC + 6144 + lane * 16), tws = *(const f32x4*)(LC + 7168 + lane * 16);
  WAITV(8);
  {
    unsigned o[4][4];
#pragma unroll
    for (int a4 = 0; a4 < 4; a4++) {
      const char* row = LX + (w * 4 + a4) * 1024;
      s16x8 xf = trd2(row + lane * 8, row + 512 + lane * 8);
      const f32x4 z4 = {0, 0, 0, 0};
      f32x4 are = mma(xf, as8(b1re), z4), aim = mma(xf, as8(b1im), z4);
      f32x4 tr = are * twc + aim * tws, ti = aim * twc - are * tws;
      asm("" : "+v"(tr), "+v"(ti));
      u32x4 bb;
#pragma unroll
      for (int r = 0; r < 4; r++) bb[r] = pk(tr[r], ti[r]);
      f32x4 xre = mma(as8(a3re), as8(bb), z4), xim = mma(as8(a3im), as8(bb), z4);
      f32x4 sre = mma(as8(a4re), as8(bb), z4), sim = mma(as8(a4im), as8(bb), z4);
#pragma unroll
      for (int r = 0; r < 4; r++) o[r][a4] = pk((i == 0) ? sre[r] : xre[r], (i == 0) ? sim[r] : xim[r]);
    }
    if (lane == 0) {
      u32x4 v = {o[0][0] >> 16, o[0][1] >> 16, o[0][2] >> 16, o[0][3] >> 16};
      *(u32x4*)(LS + 256 * 128 + w * 16) = v;
#pragma unroll
      for (int a4 = 0; a4 < 4; a4++) o[0][a4] &= 0xffffu;
    }
#pragma unroll
    for (int r = 0; r < 4; r++) {
      u32x4 v = {o[r][0], o[r][1], o[r][2], o[r][3]};
      *(u32x4*)(LS + soff[r]) = v;
    }
  }
  TS(14);
  BAR();
  TS(2);
  const int sl = lane & 7;
  const int sc = w * 8 + (lane >> 3);
  const int xline = (amt * 4 + (a >> 4)) * 1024 + ((a >> 2) & 3) * 256 + (a & 1) * 128;
  u32x4 v0[4];
#pragma unroll
  for (int j = 0; j < 4; j++) v0[j] = *(const u32x4*)(LS + (j * 64 + sc) * 128 + ((sl ^ (sc & 7)) * 16));
#pragma unroll
  for (int j = 0; j < 4; j++) stc(rs, WS_X1T + (xline >> 7) * BP + (j * 64 + sc) * 128 + sl * 16, v0[j]);
  TS(12);
  WAITV(7);
  TS(14);
  WAITV(5);
  TS(1);
  BAR();
  TS(13);
  {
    const int wline = ((((a & 7) >> 1) * 2 + ((a >> 3) & 1)) * 64 + (a >> 4) * 16 + (a & 1) * 8) * 16;
    const int cb = (lane >> 3) * 16 + (w & 3) + 128 * (w >> 2);
    const char* fp = LF + sl * 514 + cb * 2;
#pragma unroll
    for (int j = 0; j < 4; j++) {
      u32x4 fv;
#pragma unroll
      for (int r = 0; r < 4; r++) fv[r] = (unsigned)*(const unsigned short*)(fp + r * 4352 + j * 8) | ((unsigned)*(const unsigned short*)(fp + (4 + r) * 4352 + j * 8) << 16);
      stc(rs, WS_WT + (wline >> 7) * BP + (cb + 4 * j) * 128 + sl * 16, fv);
    }
    if (w == 7 && lane < 8) {
      u32x4 v;
#pragma unroll
      for (int r = 0; r < 4; r++) v[r] = (unsigned)*(const unsigned short*)(LF + sl * 514 + 512 + r * 4352) | ((unsigned)*(const unsigned short*)(LF + sl * 514 + 512 + (4 + r) * 4352) << 16);
      stc(rs, WS_WT + (wline >> 7) * BP + 256 * 128 + sl * 16, v);
    }
  }
  if (w == 3 && lane < 8) stc(rs, WS_X1T + (xline >> 7) * BP + 256 * 128 + lane * 16, *(const u32x4*)(LS + 256 * 128 + lane * 16));
  TS(15);
  const u32x4 ones = {0x3f803f80u, 0x3f803f80u, 0x3f803f80u, 0x3f803f80u};
  float gsum[4];
#pragma unroll
  for (int j = 0; j < 4; j++) {
    u32x4 xv = *(const u32x4*)(LX + (w * 4 + j) * 1024 + lane * 16);
    u32x4 pr;
#pragma unroll
    for (int d = 0; d < 4; d++) pr[d] = pk(bflo(xv[d]) * bflo(gwv[d]), bfhi(xv[d]) * bfhi(gwv[d]));
    gsum[j] = mma(as8(ones), as8(pr), f32x4{0, 0, 0, 0})[0];
  }
  float gs = (g == 0) ? gsum[0] : (g == 1) ? gsum[1] : (g == 2) ? gsum[2] : gsum[3];
  gs = rbf(dpp_sum16(gs));
  const float gg = rbf(__builtin_amdgcn_rcpf(1.f + __builtin_amdgcn_exp2f(gs * -1.44269504088896341f)));
  const int s = 32 * (4 * (a >> 2) + g) + n2s;
  if (i == 0) stc1(rs, WS_GT + (s >> 6) * 256 + (s & 15) * 16 + ((s >> 4) & 3) * 4, pk(gg, 1.f - gg));
}

template <class F> DI void p2_fft(TSP F idle, char* lb, char* fl, char* f1seen, const char* ws, rsrc_t rs, int c, int cnt, int rep, unsigned& ep, int w, int lane) {
  const int i = lane & 15, g = lane >> 4;
  const int k1 = 16 * w + i;
  const float fk1 = (float)k1, fg = (float)g;
  u32x4 fre[4], fim[4], fc[2], fs[2], fn[2];
  {
    const float st = fk1 * (1.f / 64.f);
    const float a0 = fg * 4.f * st;
    const bool odd = i & 1;
    const unsigned m2 = odd ? 0x80008000u : 0u;
    const unsigned mre = (i & 2) ? 0x80008000u : 0u;
    const unsigned mim = ((i + 1) & 2) ? 0x80008000u : 0u;
#pragma unroll
    for (int d = 0; d < 4; d++) {
      const float ang = __builtin_fmaf((float)d, st, a0);
      const float cs = fcos(ang), sn = fsin(ang);
      const unsigned r0 = pk(cs, sn), i0 = pk(sn, cs) ^ 0x8000u;
      const unsigned r1 = (odd ? i0 : r0) ^ mre, i1 = (odd ? r0 : i0) ^ mim;
      fre[0][d] = r0; fim[0][d] = i0;
      fre[1][d] = r1; fim[1][d] = i1;
      fre[2][d] = r0 ^ m2; fim[2][d] = i0 ^ m2;
      fre[3][d] = r1 ^ m2; fim[3][d] = i1 ^ m2;
    }
#pragma unroll
    for (int ks = 0; ks < 4; ks++) { PIN(fre[ks]); PIN(fim[ks]); }
  }
  idle(0);
  float c1[2][4], s1[2][4], c2[2][4], s2[2][4];
#pragma unroll
  for (int mt = 0; mt < 2; mt++) {
    const float fa = (float)perm32(mt, i);
    const float st = fa * (1.f / 32.f), a0 = fg * 8.f * st;
    const float st1 = fk1 * (1.f / 2048.f), b0 = (fg * 8.f + (float)(4 * mt)) * st1;
    const float st2 = fa * (1.f / 2048.f), d0 = (fg * 4.f + (float)(16 * w)) * st2;
#pragma unroll
    for (int d = 0; d < 4; d++) {
      const float x0 = __builtin_fmaf((float)(2 * d), st, a0), x1 = __builtin_fmaf((float)(2 * d + 1), st, a0);
      fc[mt][d] = pk(fcos(x0), fcos(x1));
      fs[mt][d] = pk(fsin(x0), fsin(x1));
      fn[mt][d] = fs[mt][d] ^ 0x80008000u;
      const float t1 = __builtin_fmaf((float)d, st1, b0);
      c1[mt][d] = fcos(t1); s1[mt][d] = fsin(t1);
      const float t2 = __builtin_fmaf((float)d, st2, d0);
      c2[mt][d] = fcos(t2); s2[mt][d] = fsin(t2);
      PIN(c1[mt][d]); PIN(s1[mt][d]); PIN(c2[mt][d]); PIN(s2[mt][d]);
    }
    PIN(fc[mt]); PIN(fs[mt]); PIN(fn[mt]);
    idle(1 + mt);
  }
  const unsigned yt = WS_YT + (32 * k1 + 8 * g) * 2 + ((c == 256) ? 1 : (c & 1) * 256 + (c & ~1)) * YP;
  stc(rs, yt, u32x4{SENT, SENT, SENT, SENT});
  if (c & 255) stc(rs, yt + YP, u32x4{SENT, SENT, SENT, SENT});
  idle(3);
  TS(4);
  if (w == 0) { gwait<SLP>(rs, WS_F1 + rep * 128, ep); lset(f1seen); }
  else lwait(f1seen, 1);
  TS(5);
  const unsigned sv = (w * 16 + (lane >> 3)) * BP + c * 128 + (lane & 7) * 16;
  asm volatile("s_mov_b32 m0, %0\n\ts_nop 0\n\tglobal_load_lds_dwordx4 %2, %1 sc1\n\tglobal_load_lds_dwordx4 %3, %1 offset:1024 sc1"
               :: "s"(laddr_of(lb) + 8192 + w * 2048), "s"(ws + WS_X1T), "v"(sv), "v"(sv + 8 * BP - 1024) : "memory");
  u32x4 wf[2];
#pragma unroll
  for (int mt = 0; mt < 2; mt++) wf[mt] = ldc(rs, WS_WT + sv + mt * 8 * BP);
  WAITV(2);
  linc(fl, lane);
  const unsigned tk = ticket_issue(rs, 50 + w * 4 + cnt, lane);
  lwait(fl, 4);
  TS(6);
  const f32x4 z4 = {0, 0, 0, 0};
  f32x4 re[2], im[2];
#pragma unroll
  for (int mt = 0; mt < 2; mt++) { re[mt] = z4; im[mt] = z4; }
#pragma unroll
  for (int ks = 0; ks < 4; ks++)
#pragma unroll
    for (int mt = 0; mt < 2; mt++) {
      s16x8 xa = lrd(lb + 8192 + ((mt * 4 + ks) * 64 + lane) * 16);
      re[mt] = mma(xa, as8(fre[ks]), re[mt]); im[mt] = mma(xa, as8(fim[ks]), im[mt]);
    }
  u32x4 pr, pi;
#pragma unroll
  for (int mt = 0; mt < 2; mt++) {
    float tr[4], ti[4];
#pragma unroll
    for (int r = 0; r < 4; r++) { tr[r] = re[mt][r] * c1[mt][r] + im[mt][r] * s1[mt][r]; ti[r] = im[mt][r] * c1[mt][r] - re[mt][r] * s1[mt][r]; }
    pr[2 * mt] = pk(tr[0], tr[1]); pr[2 * mt + 1] = pk(tr[2], tr[3]);
    pi[2 * mt] = pk(ti[0], ti[1]); pi[2 * mt + 1] = pk(ti[2], ti[3]);
  }
#pragma unroll
  for (int mt = 0; mt < 2; mt++) {
    re[mt] = mma(as8(fs[mt]), as8(pi), mma(as8(fc[mt]), as8(pr), z4));
    im[mt] = mma(as8(fn[mt]), as8(pr), mma(as8(fc[mt]), as8(pi), z4));
  }
#pragma unroll
  for (int mt = 0; mt < 2; mt++) {
    float tr[4], ti[4];
#pragma unroll
    for (int r = 0; r < 4; r++) {
      const float a = bflo(wf[mt][r]), b = bfhi(wf[mt][r]);
      tr[r] = re[mt][r] * a - im[mt][r] * b; ti[r] = re[mt][r] * b + im[mt][r] * a;
    }
    pr[2 * mt] = pk(tr[0], tr[1]); pr[2 * mt + 1] = pk(tr[2], tr[3]);
    pi[2 * mt] = pk(ti[0], ti[1]); pi[2 * mt + 1] = pk(ti[2], ti[3]);
  }
#pragma unroll
  for (int nt = 0; nt < 2; nt++) {
    re[nt] = mma(as8(pi), as8(fn[nt]), mma(as8(pr), as8(fc[nt]), z4));
    im[nt] = mma(as8(pi), as8(fc[nt]), mma(as8(pr), as8(fs[nt]), z4));
  }
#pragma unroll
  for (int nt = 0; nt < 2; nt++) {
    u32x4 v;
#pragma unroll
    for (int r = 0; r < 4; r++) v[r] = pk(re[nt][r] * c2[nt][r] - im[nt][r] * s2[nt][r], -re[nt][r] * s2[nt][r] - im[nt][r] * c2[nt][r]);
    *(u32x4*)(lb + ((nt * 4 + w) * 64 + lane) * 16) = v;
  }
  linc(fl + 4, lane);
  lwait(fl + 4, 4);
#pragma unroll
  for (int mt = 0; mt < 2; mt++) { re[mt] = z4; im[mt] = z4; }
#pragma unroll
  for (int ks = 0; ks < 4; ks++)
#pragma unroll
    for (int mt = 0; mt < 2; mt++) {
      s16x8 ya = lrd(lb + ((mt * 4 + ks) * 64 + lane) * 16);
      re[mt] = mma(ya, as8(fre[ks]), re[mt]); im[mt] = mma(ya, as8(fim[ks]), im[mt]);
    }
  u32x4 vr = {pk(re[0][0], re[0][1]), pk(re[0][2], re[0][3]), pk(re[1][0], re[1][1]), pk(re[1][2], re[1][3])};
  u32x4 vi = {pk(im[0][0], im[0][1]), pk(im[0][2], im[0][3]), pk(im[1][0], im[1][1]), pk(im[1][2], im[1][3])};
  const bool last = (unsigned)__builtin_amdgcn_readfirstlane(tk) == (cnt == 0 ? 64u : 63u);
  stc(rs, yt, vr);
  if (c & 255) stc(rs, yt + YP, vi);
  if (last && lane == 0) stc(rs, WS_F3 + (w * 4 + cnt) * 128, u32x4{ep, ep, ep, ep});
  TS(7);
}

extern "C" __global__ __launch_bounds__(512, 2) void gm_all(const char* x, const char* wqkv, const char* wr, const char* wi, const char* gw, const char* wo, char* ws, char* out) {
  __shared__ __attribute__((aligned(16))) char lds[163840];
  const int b = blockIdx.x, xcd = b & 7, ii = b >> 3;
  const int w8 = __builtin_amdgcn_readfirstlane(threadIdx.x >> 6), lane = threadIdx.x & 63;
  const int w = w8 & 3;
  TSDECL
  TS(0);
  if (ii < 2 && w8 < 4) {
    unsigned long long pc; int o0, o1;
    asm volatile("s_getpc_b64 %0\n.Lgm_pc%=:\n\ts_mov_b32 %1, gm_all-.Lgm_pc%=\n\ts_mov_b32 %2, gm_end-.Lgm_pc%=" : "=s"(pc), "=s"(o0), "=s"(o1));
    const unsigned off = (ii * 256 + threadIdx.x) * 128;
    if (off < (unsigned)(o1 - o0))
      asm volatile("s_mov_b32 m0, %0\n\ts_nop 0\n\tglobal_load_lds_dword %2, %1" :: "s"(laddr_of(lds) + L_ATT + 8192), "s"(pc + (long long)o0), "v"(off) : "memory");
  }
  unsigned ep;
  const char* epp = ws + WS_E;
  const rsrc_t rs = __builtin_amdgcn_make_buffer_rsrc(ws, 0, WS_END, 0x00020000);
  char* FL = lds + L_FL;
  const int ml = ii & 3, nl = ii >> 2;
  const int mtile = xcd * 4 + ml, ntile = nl;
  const bool aux = ii >= 24;
  {
    const int m = (xcd >> 1) * 4 + ml;
    const int nt = nl < 4 ? (xcd & 1) * 4 + nl : 8 + (xcd & 1) * 2 + (nl - 4);
    if (w8 < 4 || aux) {
      if (w8 == 0) *(LAS unsigned*)(FL + lane * 4) = 0;
      if (aux) p1_rows(TSA lds, x, gw, wo, wr, wi, rs, xcd * 8 + (ii - 24), epp, w8, lane);
      if (w8 < 4) {
        __builtin_amdgcn_s_setprio(2);
        if (aux) {
          ep = epoch_get(lds);
          if (w == 1) gwait<SLP>(rs, WS_F0 + xcd * 128 + (lane & 1) * 16, ep);
          TS(4);
          WAITV(0);
          TS(5);
          BAR();
          TS(6);
          if (w == 1) { arrive(rs, 0, 64, WS_F1, 8, ep, lane); lset(FL + 124); }
          lwait(FL + 124, 1);
#pragma unroll 1
          for (int j = 0; j < 4; j++) wo_dma(lds, wo, ntile, j, w, lane);
        }
        else if (nl < 4) p1_gemm<false>(TSA lds, wr, wi, rs, m, nt, ep, epp, w, lane);
        else p1_gemm<true>(TSA lds, wr, wi, rs, m, nt, ep, epp, w, lane);
        __builtin_amdgcn_s_setprio(0);
      }
    } else {
      const int col0 = nl >= 4 ? (nt - 8) * 128 : 512 + nt * 64;
      const int col1 = nl >= 4 ? col0 + 64 : col0 + 512;
      p1_prod(lds, x, wqkv, wo, ntile, m, col0, col1, (2 * ml + nl) & ROT1, w, lane);
    }
  }
  TS(3);
  const int i = lane & 15, g = lane >> 4, q = i >> 2, p = lane & 3;
  const int fsw = (2 * g + (q >> 1)) & 3;
  if (w8 >= 4 || b == SPECIAL) {
    const bool pg = w8 >= 4;
    if (pg) {
      ep = epoch_get(lds);
      if (!aux) {
        if (w == 0) { gwait<SLP>(rs, WS_F0 + xcd * 128 + (lane & 1) * 16, ep); lset(FL + 100); }
        wo_dma(lds, wo, ntile, 2, w, lane); wo_dma(lds, wo, ntile, 3, w, lane);
      }
    }
    if (!aux && nl >= 4) __builtin_amdgcn_s_setprio(3); else __builtin_amdgcn_s_setprio(1);
    auto idle = [&](int st) {
      if (st == AST && pg && aux) { WAITV(0); BAR(); }
      if (st == 3 && !aux) { WAITV(0); linc(FL, lane); }
    };
    p2_fft(TSA idle, pg ? lds + L_FFT : lds + L_Y, pg ? FL + 4 : FL + 20, pg ? FL + 16 : FL + 28, ws, rs, pg ? b : 256, pg ? (b & 1) * 2 + (b >> 7) : 0, xcd, ep, w, lane);
  }
  if (w8 < 4) {
    linc(FL + 80, lane);
    if (aux) { if (b != SPECIAL) WAITV(0); linc(FL, lane); }
    TS(7);
    const int stg = 4 * mtile + w;
    bool kok = false;
    if (w == 0) {
#pragma unroll 1
      for (int sp = 0; sp < 200000; sp++) {
        FENCE();
        const bool ok = ldc(rs, (lane & 1) ? WS_F2K + xcd * 128 : WS_F2Q + (mtile >> 1) * 128)[0] == ep;
        const unsigned long long mk = __ballot(ok);
        kok = mk & 2;
        if (mk & 1) break;
        __builtin_amdgcn_s_sleep(SLP);
      }
      FENCE();
      if (kok) lset(FL + 88);
      lset(FL + 104);
    } else lwait(FL + 104, 1);
    u32x4 pq[4][4];
#pragma unroll
    for (int jj = 0; jj < 4; jj++)
#pragma unroll
      for (int u = 0; u < 4; u++) pq[jj][u] = ldc(rs, WS_PQ + (((4 * mtile + ((w + 1 + jj) & 3)) * 16 + 4 * w + u) * 64 + lane) * 16);
    lwait(FL, 4);
    s16x8 wf[4][4];
    {
      const int toffA = L_WO + w * 16384 + (4 * g + q) * 128 + (fsw * 32) + p * 8;
#pragma unroll
      for (int u = 0; u < 4; u++)
#pragma unroll
        for (int a = 0; a < 4; a++) { const char* pp = lds + (toffA ^ (32 * a)) + u * 2048; wf[u][a] = trd2(pp, pp + 8192); }
    }
    if (w == 0) { if (!kok) { gwait<SLP>(rs, WS_F2K + xcd * 128, ep); lset(FL + 88); } }
    else lwait(FL + 88, 1);
    TS(8);
    u32x4 kr[6];
#pragma unroll 1
    for (int sp = 0; sp < 20000; sp++) {
      FENCE();
      bool bad = false;
#pragma unroll
      for (int j = 0; j < 6; j++) kr[j] = ldc(rs, WS_KVR + ((j < 4 ? 4 * w + j : 12 + 2 * w + j) * 64 + lane) * 16);
#pragma unroll
      for (int j = 0; j < 6; j++) bad |= has_sent(kr[j]);
      if (!__any(bad)) break;
    }
    lwait(FL + 16, 1);
    const unsigned gv = __builtin_amdgcn_raw_buffer_load_b32(rs, WS_GT + mtile * 256 + i * 16 + w * 4, 0, 16);
    TS(13);
    const f32x4 z4 = {0, 0, 0, 0};
    const f32x4 eps = {1e-6f, 1e-6f, 1e-6f, 1e-6f};
    f32x4 acc[4];
    f32x4 nm[2][4][2], dn[2][4];
    auto nd = [&](int jj) {
#pragma unroll
      for (int u = 0; u < 4; u++) {
        dn[jj & 1][u] = mma(as8(kr[4 + (u >> 1)]), as8(pq[jj][u]), eps);
        nm[jj & 1][u][0] = mma16(as4(u32x2{kr[u][0], kr[u][1]}), as4(u32x2{pq[jj][u][0], pq[jj][u][1]}), z4);
        nm[jj & 1][u][1] = mma16(as4(u32x2{kr[u][2], kr[u][3]}), as4(u32x2{pq[jj][u][2], pq[jj][u][3]}), z4);
      }
    };
    u32x4 of[2][4];
    auto sc = [&](int jj) {
#pragma unroll
      for (int u = 0; u < 4; u++) {
        const f32x4 n0 = nm[jj & 1][u][0] * __builtin_amdgcn_rcpf(dn[jj & 1][u][2 * (u & 1)]);
        const f32x4 n1 = nm[jj & 1][u][1] * __builtin_amdgcn_rcpf(dn[jj & 1][u][2 * (u & 1) + 1]);
        of[jj & 1][u] = u32x4{pk(n0[0], n0[1]), pk(n0[2], n0[3]), pk(n1[0], n1[1]), pk(n1[2], n1[3])};
      }
    };
    auto pj = [&](int jj) {
#pragma unroll
      for (int u = 0; u < 4; u++)
#pragma unroll
        for (int a = 0; a < 4; a++) acc[a] = mma(wf[u][a], as8(of[jj & 1][u]), u ? acc[a] : z4);
      if (jj < 3) {
#pragma unroll
        for (int a = 0; a < 4; a++) *(f32x4*)(lds + L_WO + w * 16384 + (jj * 4 + a) * 1024 + lane * 16) = acc[a];
      }
    };
    nd(0); SCHED();
    nd(1); sc(0);
#pragma unroll
    for (int t = 0; t < 12; t++) { SGB(0x008, 1); SGB(0x002, 3); }
    SCHED();
#pragma unroll
    for (int jj = 0; jj < 3; jj++) {
      pj(jj); sc(jj + 1);
#pragma unroll
      for (int t = 0; t < 16; t++) { SGB(0x008, 1); SGB(0x002, 3); }
      SCHED();
      if (jj < 2) { nd(jj + 2); SCHED(); }
    }
    linc(FL + 92, lane);
    pj(3);
    TS(9);
    lwait(FL + 92, 4);
#pragma unroll
    for (int ww = 1; ww < 4; ww++)
#pragma unroll
      for (int a = 0; a < 4; a++) acc[a] += *(const f32x4*)(lds + L_WO + ((w + ww) & 3) * 16384 + ((3 - ww) * 4 + a) * 1024 + lane * 16);
    lwait(FL + 48, 4);
    TS(10);
    const float ga = bflo(gv), gb = bfhi(gv);
    unsigned pv[4][2];
#pragma unroll
    for (int a = 0; a < 2; a++) {
      const f32x4 t0 = *(const f32x4*)(lds + L_FFT + ((w * 4 + a) * 64 + lane) * 16);
      const f32x4 t1 = *(const f32x4*)(lds + L_FFT + ((w * 4 + a + 2) * 64 + lane) * 16);
      const f32x4 v0 = acc[a] * ga + (t0 + t1) * gb, v1 = acc[a + 2] * ga + (t0 - t1) * gb;
      pv[a][0] = pk(v0[0], v0[1]); pv[a][1] = pk(v0[2], v0[3]);
      pv[a + 2][0] = pk(v1[0], v1[1]); pv[a + 2][1] = pk(v1[2], v1[3]);
    }
#pragma unroll
    for (int a = 0; a < 2; a++) {
      auto s0 = __builtin_amdgcn_permlane16_swap(pv[2 * a][0], pv[2 * a + 1][0], false, false);
      auto s1 = __builtin_amdgcn_permlane16_swap(pv[2 * a][1], pv[2 * a + 1][1], false, false);
      u32x4 v = {s0[0], s1[0], s0[1], s1[1]};
      char* op = out + (size_t)(16 * stg + i) * 1024 + (256 * a + 32 * ntile + 16 * (g & 1) + 8 * (g >> 1)) * 2;
      asm volatile("global_store_dwordx4 %0, %1, off sc1\n\ts_nop 1" :: "v"(op), "v"(v) : "memory");
    }
    TS(11);
    TSFLUSH();
    return;
  }
  {
    const float sgn = (i & 1) ? -1.f : 1.f;
    const unsigned k0v = (i & 1) ? 0xb5803580u : 0x35803580u;
    const float fn = (float)(32 * ntile + 16 * (w & 1) + i) * (1.f / 512.f);
    const float t2 = 2.f * fcos(fn * 32.f);
    f32x2 c0[4], c1[4];
#pragma unroll
    for (int d = 0; d < 4; d++) {
      const float kf = (float)(4 * g + 128 * (w & 1) + (w >> 1)) + (float)(16 * (d >> 1) + 2 * (d & 1));
      const float a0 = kf * fn, a1 = (kf + 32.f) * fn;
      c0[d] = f32x2{fcos(a0), fsin(a0)} * 0x1p-19f;
      c1[d] = f32x2{fcos(a1), fsin(a1)} * 0x1p-19f;
    }
    u32x4 gf[8];
#pragma unroll
    for (int j = 0; j < 2; j++) {
#pragma unroll
      for (int h = 0; h < 2; h++) {
#pragma unroll
        for (int d = 0; d < 4; d++) {
          gf[4 * j + 2 * h][d] = pk(c0[d][0], c0[d][1]);
          gf[4 * j + 2 * h + 1][d] = pk(c1[d][0], c1[d][1]);
          c0[d] = c1[d] * t2 - c0[d];
        }
#pragma unroll
        for (int d = 0; d < 4; d++) c1[d] = c0[d] * t2 - c1[d];
      }
      gf[4 * j][0] = (w == j && g == 0) ? k0v : gf[4 * j][0];
      if (j == 0) {
        const float sg = (w & 1) ? sgn : 1.f;
#pragma unroll
        for (int d = 0; d < 4; d++) { c0[d] *= sg; c1[d] *= sg; }
      }
    }
#pragma unroll
    for (int k = 0; k < 8; k++) PIN(gf[k]);
    TS(8);
    gwait<SLP>(rs, WS_F3 + ((mtile >> 3) * 4 + w) * 128, ep);
    lwait(FL + 80, 4);
    linc(FL + 96, lane);
    TS(9);
    const int toffB = L_Y + (4 * g + q) * 128 + (fsw * 32) + p * 8;
    f32x4 accb[4];
    s16x8 tb_[2][2][4];
    auto rd = [&](int u) {
      const char* slot = lds + (w ^ (u >> 1)) * 16384 + (u & 1) * 8192;
#pragma unroll
      for (int ks = 0; ks < 2; ks++)
#pragma unroll
        for (int c = 0; c < 4; c++) tb_[u & 1][ks][c] = trd2(slot + (toffB ^ (32 * c)) + ks * 4096, slot + (toffB ^ (32 * c)) + ks * 4096 + 2048);
    };
    auto mm = [&](int u) {
#pragma unroll
      for (int ks = 0; ks < 2; ks++)
#pragma unroll
        for (int c = 0; c < 4; c++) accb[c] = mma(as8(gf[2 * u + ks]), tb_[u & 1][ks][c], accb[c]);
    };
#pragma unroll 1
    for (int sp = 0; sp < 20000; sp++) {
      const unsigned v = (lane >> 3) * YP + ((((lane & 7) >> 1) ^ ((lane >> 4) & 3)) * 32) + (lane & 1) * 16;
      const char* src = ws + WS_YT + (size_t)(w * 128) * YP + mtile * 128;
      const unsigned la = laddr_of(lds) + L_Y + w * 16384;
#pragma unroll
      for (int j = 0; j < 4; j++) glds4c(la + j * 4096, src + (size_t)j * (32 * YP), v, v + (8 * YP - 1024), v + 2 * (8 * YP - 1024), v + 3 * (8 * YP - 1024));
#pragma unroll
      for (int c = 0; c < 4; c++) accb[c] = f32x4{0, 0, 0, 0};
      WAITV(8);
      rd(0);
      WAITV(0);
      TS(10);
      rd(1); mm(0);
#pragma unroll
      for (int t = 0; t < 8; t++) { SGB(0x100, 2); SGB(0x008, 1); }
      SCHED();
      mm(1);
      const f32x4 s4 = (accb[0] + accb[1]) + (accb[2] + accb[3]);
      const float s1 = (s4[0] + s4[1]) + (s4[2] + s4[3]);
      if (!__any(s1 != s1)) break;
      bool bad = false;
#pragma unroll 1
      for (int j = 0; j < 16; j++) bad |= has_sent(*(const u32x4*)(lds + L_Y + w * 16384 + j * 1024 + lane * 16));
      if (!__any(bad)) break;
    }
    lset(FL + 32 + w * 4);
    lwait(FL + 32 + (w ^ 1) * 4, 1);
    rd(2);
    SCHED();
    rd(3); mm(2);
#pragma unroll
    for (int t = 0; t < 8; t++) { SGB(0x100, 2); SGB(0x008, 1); }
    SCHED();
    mm(3);
    lwait(FL + 96, 4);
    if (b == 0 && w == 1 && lane == 0) stc1(rs, WS_E, ep);
#pragma unroll
    for (int c = 0; c < 4; c++) *(f32x4*)(lds + L_FFT + ((c * 4 + w) * 64 + lane) * 16) = accb[c];
    linc(FL + 48, lane);
    TS(11);
    TSFLUSH();
  }
}

extern "C" __global__ void gm_end() {}

extern "C" void kernel_launch(void* const* d_in, const int* in_sizes, int n_in, void* d_out, int out_size, void* d_ws, size_t ws_size, hipStream_t stream) {
  gm_all<<<256, 512, 0, stream>>>((const char*)d_in[0], (const char*)d_in[1], (const char*)d_in[4], (const char*)d_in[5], (const char*)d_in[6],
                                  (const char*)d_in[2], (char*)d_ws, (char*)d_out);
}
```

```cpp
#include <hip/hip_runtime.h>
#include <stdint.h>

typedef short s16x4 __attribute__((ext_vector_type(4)));
typedef short s16x8 __attribute__((ext_vector_type(8)));
typedef float f32x4 __attribute__((ext_vector_type(4)));
typedef float f32x2 __attribute__((ext_vector_type(2)));
typedef unsigned u32x4 __attribute__((ext_vector_type(4)));
typedef unsigned u32x2 __attribute__((ext_vector_type(2)));
typedef __bf16 bfx2 __attribute__((ext_vector_type(2)));
#define LAS __attribute__((address_space(3)))
#define DI __device__ __forceinline__
static constexpr int ROT1 = 6;
static constexpr int SLP = 2;

static constexpr unsigned BP = 258 * 128;
static constexpr unsigned YP = 4224;
static constexpr unsigned CS = 4352;
static constexpr unsigned WS_X1T = 0;
static constexpr unsigned WS_WT  = WS_X1T + (9u << 18);
static constexpr unsigned WS_PQ  = WS_WT + (9u << 18);
static constexpr unsigned WS_KVP = WS_PQ + (2u << 20);
static constexpr unsigned WS_GT  = WS_KVP + (512u << 10);
static constexpr unsigned WS_KVR = WS_GT + (16u << 10);
static constexpr unsigned WS_YT  = WS_KVR + (32u << 10);
static constexpr unsigned WS_E   = WS_YT + (9u << 18);
static constexpr unsigned WS_CNT = WS_E + 4096;
static constexpr unsigned WS_F0  = WS_CNT + 72 * CS;
static constexpr unsigned WS_F1  = WS_F0 + 8 * 128;
static constexpr unsigned WS_F2K = WS_F1 + 8 * 128;
static constexpr unsigned WS_F2Q = WS_F2K + 8 * 128;
static constexpr unsigned WS_F3  = WS_F2Q + 16 * 128;
static constexpr unsigned WS_END = WS_F3 + 16 * 128;
static constexpr int SPECIAL = 255;

static constexpr int L_WO = 0, L_Y = 65536, L_FFT = 131072, L_DUMP = 155648, L_FL = 163840 - 256;

DI unsigned pk(float lo, float hi) { f32x2 p = {lo, hi}; return __builtin_bit_cast(unsigned, __builtin_convertvector(p, bfx2)); }
DI float bflo(unsigned u) { return __builtin_bit_cast(float, u << 16); }
DI float bfhi(unsigned u) { return __builtin_bit_cast(float, u & 0xffff0000u); }
DI float rbf(float v) { return bflo(pk(v, 0.f)); }
DI s16x8 as8(u32x4 v) { return __builtin_bit_cast(s16x8, v); }
DI s16x4 as4(u32x2 v) { return __builtin_bit_cast(s16x4, v); }
DI f32x4 mma(s16x8 a, s16x8 b, f32x4 c) { return __builtin_amdgcn_mfma_f32_16x16x32_bf16(a, b, c, 0, 0, 0); }
DI f32x4 mma16(s16x4 a, s16x4 b, f32x4 c) { return __builtin_amdgcn_mfma_f32_16x16x16bf16_1k(a, b, c, 0, 0, 0); }
DI void mfa(f32x4& c, s16x8 a, s16x8 b) { asm volatile("v_mfma_f32_16x16x32_bf16 %0, %1, %2, %0" : "+v"(c) : "v"(a), "v"(b)); }
DI s16x4 trd(const char* l) { return __builtin_amdgcn_ds_read_tr16_b64_v4i16((LAS s16x4*)l); }
DI s16x8 cat(s16x4 a, s16x4 b) { return __builtin_shufflevector(a, b, 0, 1, 2, 3, 4, 5, 6, 7); }
DI s16x8 trd2(const char* l0, const char* l1) { return cat(trd(l0), trd(l1)); }
DI s16x8 lrd(const char* l) { return *(const s16x8*)l; }
DI float fsin(float rev) { return __builtin_amdgcn_sinf(rev); }
DI float fcos(float rev) { return __builtin_amdgcn_cosf(rev); }
DI unsigned laddr_of(const char* l) { return (unsigned)(size_t)(const LAS char*)l; }
DI void glds4(unsigned laddr, const char* sb, unsigned v0, unsigned v1, unsigned v2, unsigned v3) {
  asm volatile("s_mov_b32 m0, %0\n\ts_nop 0\n\t"
               "global_load_lds_dwordx4 %2, %1\n\t"
               "global_load_lds_dwordx4 %3, %1 offset:1024\n\t"
               "global_load_lds_dwordx4 %4, %1 offset:2048\n\t"
               "global_load_lds_dwordx4 %5, %1 offset:3072"
               :: "s"(laddr), "s"(sb), "v"(v0), "v"(v1), "v"(v2), "v"(v3) : "memory");
}
DI void glds4c(unsigned laddr, const char* sb, unsigned v0, unsigned v1, unsigned v2, unsigned v3) {
  asm volatile("s_mov_b32 m0, %0\n\ts_nop 0\n\t"
               "global_load_lds_dwordx4 %2, %1 sc1\n\t"
               "global_load_lds_dwordx4 %3, %1 offset:1024 sc1\n\t"
               "global_load_lds_dwordx4 %4, %1 offset:2048 sc1\n\t"
               "global_load_lds_dwordx4 %5, %1 offset:3072 sc1"
               :: "s"(laddr), "s"(sb), "v"(v0), "v"(v1), "v"(v2), "v"(v3) : "memory");
}
DI int perm32(int t, int i) { return 8 * (i >> 2) + 4 * t + (i & 3); }
DI float phi(float t) {
  float e = __builtin_amdgcn_exp2f(t * 1.44269504088896341f);
  e = __builtin_amdgcn_fmed3f(e, 0.f, 1.f);
  return fmaxf(t + 1e-6f, 1e-6f) + e;
}
#define SCHED() __builtin_amdgcn_sched_barrier(0)
#define SGB(mask, n) __builtin_amdgcn_sched_group_barrier(mask, n, 0)
#define PIN(x) asm volatile("" : "+v"(x))
#define WAITV(n) asm volatile("s_waitcnt vmcnt(" #n ")" ::: "memory")
#define BAR() asm volatile("s_waitcnt lgkmcnt(0)\n\ts_barrier" ::: "memory")
#define FENCE() asm volatile("" ::: "memory")

typedef __amdgpu_buffer_rsrc_t rsrc_t;
DI u32x4 ldc(rsrc_t r, unsigned off) { return __builtin_amdgcn_raw_buffer_load_b128(r, off, 0, 16); }
DI void stc(rsrc_t r, unsigned off, u32x4 v) { __builtin_amdgcn_raw_buffer_store_b128(v, r, off, 0, 16); }
DI void stc1(rsrc_t r, unsigned off, unsigned v) { __builtin_amdgcn_raw_buffer_store_b32(v, r, off, 0, 16); }
template <int SL> DI void gwait(rsrc_t r, unsigned off, unsigned ep) {
#pragma unroll 1
  for (int sp = 0; sp < 200000; sp++) {
    FENCE();
    const u32x4 f = ldc(r, off);
    if (__all(f[0] == ep && f[1] == ep && f[2] == ep && f[3] == ep)) break;
    __builtin_amdgcn_s_sleep(SL);
  }
  FENCE();
}
DI unsigned ticket(rsrc_t r, int c, int lane) {
  unsigned old = 0;
  if (lane == 0) old = __builtin_amdgcn_raw_ptr_buffer_atomic_add_i32(1, r, WS_CNT + c * CS, 0, 16);
  old = __builtin_amdgcn_readfirstlane(old);
  FENCE();
  return old;
}
DI void arrive(rsrc_t r, int c, unsigned n, unsigned flag, int nrep, unsigned ep, int lane) {
  if (ticket(r, c, lane) == n - 1 && lane < nrep) stc(r, flag + lane * 128, u32x4{ep, ep, ep, ep});
}
DI void linc(char* p, int lane) { if (lane == 0) asm volatile("ds_add_u32 %0, %1" :: "v"(laddr_of(p)), "v"(1u) : "memory"); }
DI void lset(char* p) { asm volatile("ds_write_b32 %0, %1" :: "v"(laddr_of(p)), "v"(1u) : "memory"); }
DI void lwait(const char* p, unsigned want) {
#pragma unroll 1
  for (int sp = 0; sp < 4000000; sp++) {
    if ((unsigned)__builtin_amdgcn_readfirstlane(*(const volatile LAS unsigned*)p) >= want) break;
    __builtin_amdgcn_s_sleep(1);
  }
  FENCE();
}

DI void lspin(const char* p, unsigned want) {
#pragma unroll 1
  for (int sp = 0; sp < 8000000; sp++)
    if ((unsigned)__builtin_amdgcn_readfirstlane(*(const volatile LAS unsigned*)p) >= want) break;
  FENCE();
}

DI void epoch_dma(char* lds, const char* p, int lane) {
  if (lane == 0) asm volatile("s_mov_b32 m0, %0\n\ts_nop 0\n\tglobal_load_lds_dword %2, %1 sc1" :: "s"(laddr_of(lds) + L_FL - 16), "s"(p), "v"(0u) : "memory");
}
DI unsigned epoch_get(const char* lds) { return (unsigned)__builtin_amdgcn_readfirstlane(*(const volatile LAS unsigned*)(lds + L_FL - 16)) + 1u; }
DI unsigned ticket_issue(rsrc_t r, int c, int lane) {
  unsigned old = 0;
  if (lane == 0) old = __builtin_amdgcn_raw_ptr_buffer_atomic_add_i32(1, r, WS_CNT + c * CS, 0, 16);
  return old;
}
DI void ticket_done(rsrc_t r, unsigned old, unsigned n, unsigned flag, int nrep, unsigned ep, int lane) {
  if ((unsigned)__builtin_amdgcn_readfirstlane(old) == n - 1 && lane < nrep) stc(r, flag + lane * 128, u32x4{ep, ep, ep, ep});
}

DI void wo_dma(char* lds, const char* wo, int ntile, int j, int w, int lane) {
  const int blk = ((lane & 7) >> 1) ^ ((lane >> 4) & 3);
  const unsigned v = (lane >> 3) * 1024 + (blk & 1) * 32 + (blk >> 1) * 512 + (lane & 1) * 16;
  glds4(laddr_of(lds) + L_WO + j * 16384 + w * 4096, wo + (size_t)(j * 128 + w * 32) * 1024 + ntile * 64, v, v + 7168, v + 2 * 7168, v + 3 * 7168);
}

DI void p1_prod(char* lds, const char* x, const char* wqkv, const char* wo, int ntile, int m, int col0, int col1, int rot, int w, int lane) {
  unsigned voffA[4], voffB[4];
#pragma unroll
  for (int j = 0; j < 4; j++) {
    const int par = j & 1;
    voffA[j] = (lane >> 3) * 1024 + (((lane & 7) ^ ((4 * par + (lane >> 4)) & 7)) * 16) + j * (8192 - 1024);
    voffB[j] = (lane >> 3) * 3072 + ((((lane & 7) >> 1) ^ (((lane >> 4) & 1) | (par << 1))) * 32) + (lane & 1) * 16 + j * (24576 - 1024);
  }
  const unsigned lbase = laddr_of(lds) + w * 4096;
  const char* xa = x + (size_t)(m * 128 + w * 32) * 1024;
  const char* wb = wqkv + (size_t)((w & 1) * 32) * 3072 + ((w >> 1) ? col1 : col0) * 2;
  auto issue = [&](int idx) {
    const int ks = (idx + rot) & 7;
    const unsigned slot = lbase + (idx & 3) * 32768;
    glds4(slot, xa + ks * 128, voffA[0], voffA[1], voffA[2], voffA[3]);
    glds4(slot + 16384, wb + (size_t)(ks * 64) * 3072, voffB[0], voffB[1], voffB[2], voffB[3]);
  };
  issue(0); issue(1);
  WAITV(8); BAR();
  issue(2); issue(3);
#pragma unroll 1
  for (int j = 1; j < 5; j++) { WAITV(16); BAR(); issue(j + 3); }
  WAITV(16); BAR();
  wo_dma(lds, wo, ntile, 0, w, lane);
  WAITV(12); BAR();
  wo_dma(lds, wo, ntile, 1, w, lane);
  WAITV(8); BAR();
}

template <bool QT>
DI void p1_gemm(char* lds, rsrc_t rs, int m, int nt, unsigned& ep, const char* epp, int w, int lane) {
  const int i = lane & 15, g = lane >> 4, q = i >> 2, p = lane & 3;
  f32x4 acc[8][2];
#pragma unroll
  for (int a = 0; a < 8; a++) { acc[a][0] = f32x4{0, 0, 0, 0}; acc[a][1] = f32x4{0, 0, 0, 0}; }
  const int aoff = i * 128 + ((g ^ (i >> 1)) * 16);
  const int boff = 16384 + (8 * g + q) * 128 + ((w ^ (((q >> 1) & 1) | ((g & 1) << 1))) * 32) + p * 8;
  s16x8 A[2][2][8]; s16x4 B[2][2][2][2];
  auto rd1 = [&](int buf, const char* slot, int r) {
    const int ks = r / 12, rr = r % 12;
    const int t = rr == 0 ? 0 : rr == 1 ? 1 : rr == 3 ? 2 : rr == 4 ? 3 : -1;
    const int a = rr == 2 ? 0 : rr - 4;
    if (t >= 0) B[buf][ks][t >> 1][t & 1] = trd(slot + boff + (t >> 1) * 8192 + ks * 4096 + (t & 1) * 512);
    else A[buf][ks][a] = lrd(slot + (aoff ^ (ks * 64)) + a * 2048);
  };
  auto mf1 = [&](int buf, int ks, int mt, int hh) {
    const s16x8 bf = cat(B[buf][ks][hh][0], B[buf][ks][hh][1]);
    if (QT) mfa(acc[mt][hh], bf, A[buf][ks][mt]); else mfa(acc[mt][hh], A[buf][ks][mt], bf);
  };
  auto region = [&](int cur, const char* slot) {
#pragma unroll
    for (int mi = 0; mi < 32; mi++) {
      mf1(cur, mi >> 4, (mi & 15) >> 1, mi & 1);
      SCHED();
      if (mi < 24) { rd1(cur ^ 1, slot, mi); SCHED(); }
    }
  };
  if (w == 0) epoch_dma(lds, epp, lane);
  const bool zw = QT && blockIdx.x >= 128 && blockIdx.x < 136;
  if (zw && w == 3 && lane < 9) stc1(rs, WS_CNT + ((blockIdx.x & 7) * 9 + lane) * CS, 0u);
  __builtin_amdgcn_s_waitcnt(0xC07F);
  BAR();
#pragma unroll
  for (int r = 0; r < 24; r++) rd1(0, lds, r);
  SCHED();
#pragma unroll 1
  for (int it = 0; it < 3; it++) {
    BAR(); SCHED();
    region(0, lds + ((2 * it + 1) & 3) * 32768);
    if (it == (zw ? 0 : 1)) WAITV(0);
    BAR(); SCHED();
    if (zw && it == 0 && w == 3) { const unsigned e = epoch_get(lds); if (lane < 8) stc1(rs, WS_F0 + lane * 128 + (blockIdx.x & 7) * 4, e); }
    region(1, lds + ((2 * it + 2) & 3) * 32768);
  }
  ep = epoch_get(lds);
  BAR(); SCHED();
  region(0, lds + 3 * 32768);
  asm volatile("s_nop 7");
  SCHED();
  auto mmh = [&](int hh) {
#pragma unroll
    for (int mt = 0; mt < 8; mt++)
#pragma unroll
      for (int ks = 0; ks < 2; ks++) {
        const s16x8 bf = cat(B[1][ks][hh][0], B[1][ks][hh][1]);
        acc[mt][hh] = QT ? mma(bf, A[1][ks][mt], acc[mt][hh]) : mma(A[1][ks][mt], bf, acc[mt][hh]);
      }
  };
  mmh(0);
  SCHED();
  if (QT) {
    mmh(1);
#pragma unroll
    for (int mt = 0; mt < 8; mt++) {
      u32x4 v = {pk(phi(acc[mt][0][0]), phi(acc[mt][0][1])), pk(phi(acc[mt][0][2]), phi(acc[mt][0][3])),
                 pk(phi(acc[mt][1][0]), phi(acc[mt][1][1])), pk(phi(acc[mt][1][2]), phi(acc[mt][1][3]))};
      stc(rs, WS_PQ + (((m * 8 + mt) * 16 + (nt - 8) * 4 + w) * 64 + lane) * 16, v);
    }
    lwait(lds + L_FL + 100, 1);
    WAITV(0);
    arrive(rs, 34 + m, 16, WS_F2Q + m * 128, 1, ep, lane);
  } else {
    mmh(1);
    u32x4 pa[4];
#pragma unroll
    for (int u = 0; u < 4; u++)
      pa[u] = u32x4{pk(phi(acc[2 * u][0][0]), phi(acc[2 * u][0][1])), pk(phi(acc[2 * u][0][2]), phi(acc[2 * u][0][3])),
                    pk(phi(acc[2 * u + 1][0][0]), phi(acc[2 * u + 1][0][1])), pk(phi(acc[2 * u + 1][0][2]), phi(acc[2 * u + 1][0][3]))};
#pragma unroll
    for (int t = 0; t < 16; t++) { SGB(0x008, 1); SGB(0x002, 2); }
    SCHED();
    f32x4 kv = {0, 0, 0, 0}, z = {0, 0, 0, 0};
    const u32x4 ones = {0x3f803f80u, 0x3f803f80u, 0x3f803f80u, 0x3f803f80u};
#pragma unroll
    for (int u = 0; u < 4; u++) {
      u32x4 b = {pk(acc[2 * u][1][0], acc[2 * u][1][1]), pk(acc[2 * u][1][2], acc[2 * u][1][3]),
                 pk(acc[2 * u + 1][1][0], acc[2 * u + 1][1][1]), pk(acc[2 * u + 1][1][2], acc[2 * u + 1][1][3])};
      z = mma(as8(pa[u]), as8(ones), z);
      kv = mma(as8(pa[u]), as8(b), kv);
    }
    const int head = nt * 4 + w;
    u32x4 v = {pk(kv[0], kv[1]), pk(kv[2], kv[3]), pk(z[0], z[1]), pk(z[2], z[3])};
    stc(rs, WS_KVP + ((m * 32 + head) * 64 + lane) * 16, v);
    lwait(lds + L_FL + 100, 1);
    WAITV(0);
    if (ticket(rs, 1 + head, lane) == 15) {
      u32x4 kl[16];
#pragma unroll
      for (int pp = 0; pp < 16; pp++) kl[pp] = ldc(rs, WS_KVP + ((pp * 32 + head) * 64 + lane) * 16);
      u32x4 r;
#pragma unroll
      for (int d = 0; d < 4; d++) {
        float lo[4], hi[4];
#pragma unroll
        for (int j = 0; j < 4; j++) {
          lo[j] = (bflo(kl[4 * j][d]) + bflo(kl[4 * j + 1][d])) + (bflo(kl[4 * j + 2][d]) + bflo(kl[4 * j + 3][d]));
          hi[j] = (bfhi(kl[4 * j][d]) + bfhi(kl[4 * j + 1][d])) + (bfhi(kl[4 * j + 2][d]) + bfhi(kl[4 * j + 3][d]));
        }
        r[d] = pk((lo[0] + lo[1]) + (lo[2] + lo[3]), (hi[0] + hi[1]) + (hi[2] + hi[3]));
      }
      const int u = (head >> 3) * 4 + (head & 3), hh = (head >> 2) & 1;
      const bool zr = (i & 3) == hh + 2 * (u & 1);
      __builtin_amdgcn_raw_buffer_store_b64(u32x2{r[0], r[1]}, rs, WS_KVR + (u * 64 + lane) * 16 + hh * 8, 0, 16);
      if (zr) stc(rs, WS_KVR + 16384 + ((u >> 1) * 64 + lane) * 16, hh ? u32x4{0u, 0u, r[2], r[3]} : u32x4{r[2], r[3], 0u, 0u});
      WAITV(0);
      arrive(rs, 33, 32, WS_F2K, 8, ep, lane);
    }
  }
}

DI float dpp_sum16(float v) {
  v += __builtin_bit_cast(float, __builtin_amdgcn_update_dpp(0, __builtin_bit_cast(int, v), 0x128, 0xf, 0xf, false));
  v += __builtin_bit_cast(float, __builtin_amdgcn_update_dpp(0, __builtin_bit_cast(int, v), 0x124, 0xf, 0xf, false));
  v += __builtin_bit_cast(float, __builtin_amdgcn_update_dpp(0, __builtin_bit_cast(int, v), 0x122, 0xf, 0xf, false));
  v += __builtin_bit_cast(float, __builtin_amdgcn_update_dpp(0, __builtin_bit_cast(int, v), 0x121, 0xf, 0xf, false));
  return v;
}

DI void p1_rows(char* lds, const char* x, const char* gw, const char* wo, const char* wr, const char* wi, rsrc_t rs, int a, const char* epp, int w, int lane) {
  const int i = lane & 15, g = lane >> 4;
  char* LX = lds;
  char* LS = lds + 32768;
  char* LC = lds + 106496;
  const int amt = (a >> 1) & 1;
  const int n2s = perm32(amt, 8 * (a & 1) + w);
  {
    const unsigned v = lane * 16;
    glds4(laddr_of(LX) + w * 4096, x + (size_t)(128 * (a >> 2) + n2s) * 1024, v, v + 31 * 1024, v + 62 * 1024, v + 93 * 1024);
  }
  char* LF = lds + 69632;
  auto wdma = [&]() {
    const unsigned v = lane * 16;
    const char* fsrc = ((w >> 2) ? wi : wr) + (size_t)(8 * (a & 7) + 64 * (4 * (a >> 3) + (w & 3))) * 514;
    glds4(laddr_of(LF) + w * 4352, fsrc, v, v, v, v);
    if (lane == 0) asm volatile("s_mov_b32 m0, %0\n\ts_nop 0\n\tglobal_load_lds_dwordx4 %2, %1" :: "s"(laddr_of(LF) + w * 4352 + 4096), "s"(fsrc + 4096), "v"(v) : "memory");
  };
  {
    const float fi = (float)i;
    const int d = w & 3;
    char* cp = LC + lane * 16 + d * 4;
    const float n2i = (float)(4 * g + d) * fi;
    if (w < 4) {
      float c[2], s[2];
#pragma unroll
      for (int e = 0; e < 2; e++) {
        const float ang = (float)(4 * g + 16 * (d >> 1) + 2 * (d & 1) + e) * (fi * (1.f / 32.f));
        c[e] = fcos(ang);
        s[e] = (i == 0) ? (e ? -1.f : 1.f) : fsin(-ang);
      }
      *(unsigned*)(cp) = pk(c[0], c[1]);
      *(unsigned*)(cp + 1024) = pk(s[0], s[1]);
      *(float*)(cp + 6144) = fcos(n2i * (1.f / 512.f));
      *(float*)(cp + 7168) = fsin(n2i * (1.f / 512.f));
    } else {
      {
        const float ang = n2i * (1.f / 16.f);
        const float cs = fcos(ang), sn = fsin(ang);
        *(unsigned*)(cp + 2048) = pk(cs, sn);
        *(unsigned*)(cp + 3072) = pk(sn, cs) ^ ((i >= 8) ? 0x80000000u : 0x00008000u);
      }
      {
        const float ang = n2i * (1.f / 32.f);
        const float cs = fcos(ang);
        const float sn = (i == 0) ? ((d & 1) ? -1.f : 1.f) : fsin(-ang);
        const unsigned u = pk(cs, sn);
        const int sh = (i & 1) * 16;
        *(unsigned*)(cp + 4096) = (u & 0xffffu) << sh;
        *(unsigned*)(cp + 5120) = (u >> 16) << sh;
      }
    }
  }
  int soff[4];
#pragma unroll
  for (int r = 0; r < 4; r++) {
    const int k2 = 4 * g + r;
    const int k = i + 32 * k2;
    const int col = (i == 0) ? 16 * k2 : (k2 < 8 ? k : 512 - k);
    soff[r] = col * 128 + ((w ^ (col & 7)) * 16);
    PIN(soff[r]);
  }
  BAR();
  wdma();
  epoch_dma(lds, epp, lane);
  const u32x4 gwv = *(const u32x4*)(gw + lane * 16);
  asm volatile("s_mov_b32 m0, %0\n\ts_nop 0\n\tglobal_load_lds_dword %2, %1" :: "s"(laddr_of(lds) + L_DUMP), "s"(wo), "v"((unsigned)((a * 8 + w) * 8 + (lane & 7)) * 128u) : "memory");
  const u32x4 b1re = *(const u32x4*)(LC + lane * 16), b1im = *(const u32x4*)(LC + 1024 + lane * 16);
  const u32x4 a3re = *(const u32x4*)(LC + 2048 + lane * 16), a3im = *(const u32x4*)(LC + 3072 + lane * 16);
  const u32x4 a4re = *(const u32x4*)(LC + 4096 + lane * 16), a4im = *(const u32x4*)(LC + 5120 + lane * 16);
  const f32x4 twc = *(const f32x4*)(LC + 6144 + lane * 16), tws = *(const f32x4*)(LC + 7168 + lane * 16);
  WAITV(8);
  {
    unsigned o[4][4];
#pragma unroll
    for (int a4 = 0; a4 < 4; a4++) {
      const char* row = LX + (w * 4 + a4) * 1024;
      s16x8 xf = trd2(row + lane * 8, row + 512 + lane * 8);
      const f32x4 z4 = {0, 0, 0, 0};
      f32x4 are = mma(xf, as8(b1re), z4), aim = mma(xf, as8(b1im), z4);
      f32x4 tr = are * twc + aim * tws, ti = aim * twc - are * tws;
      asm("" : "+v"(tr), "+v"(ti));
      u32x4 bb;
#pragma unroll
      for (int r = 0; r < 4; r++) bb[r] = pk(tr[r], ti[r]);
      f32x4 xre = mma(as8(a3re), as8(bb), z4), xim = mma(as8(a3im), as8(bb), z4);
      f32x4 sre = mma(as8(a4re), as8(bb), z4), sim = mma(as8(a4im), as8(bb), z4);
#pragma unroll
      for (int r = 0; r < 4; r++) o[r][a4] = pk((i == 0) ? sre[r] : xre[r], (i == 0) ? sim[r] : xim[r]);
    }
    if (lane == 0) {
      u32x4 v = {o[0][0] >> 16, o[0][1] >> 16, o[0][2] >> 16, o[0][3] >> 16};
      *(u32x4*)(LS + 256 * 128 + w * 16) = v;
#pragma unroll
      for (int a4 = 0; a4 < 4; a4++) o[0][a4] &= 0xffffu;
    }
#pragma unroll
    for (int r = 0; r < 4; r++) {
      u32x4 v = {o[r][0], o[r][1], o[r][2], o[r][3]};
      *(u32x4*)(LS + soff[r]) = v;
    }
  }
  BAR();
  const int sl = lane & 7;
  const int sc = w * 8 + (lane >> 3);
  const int xline = (amt * 4 + (a >> 4)) * 1024 + ((a >> 2) & 3) * 256 + (a & 1) * 128;
  u32x4 v0[4];
#pragma unroll
  for (int j = 0; j < 4; j++) v0[j] = *(const u32x4*)(LS + (j * 64 + sc) * 128 + ((sl ^ (sc & 7)) * 16));
#pragma unroll
  for (int j = 0; j < 4; j++) stc(rs, WS_X1T + (xline >> 7) * BP + (j * 64 + sc) * 128 + sl * 16, v0[j]);
  WAITV(5);
  BAR();
  {
    const int wline = ((((a & 7) >> 1) * 2 + ((a >> 3) & 1)) * 64 + (a >> 4) * 16 + (a & 1) * 8) * 16;
    const int cb = (lane >> 3) * 16 + (w & 3) + 128 * (w >> 2);
    const char* fp = LF + sl * 514 + cb * 2;
#pragma unroll
    for (int j = 0; j < 4; j++) {
      u32x4 fv;
#pragma unroll
      for (int r = 0; r < 4; r++) fv[r] = (unsigned)*(const unsigned short*)(fp + r * 4352 + j * 8) | ((unsigned)*(const unsigned short*)(fp + (4 + r) * 4352 + j * 8) << 16);
      stc(rs, WS_WT + (wline >> 7) * BP + (cb + 4 * j) * 128 + sl * 16, fv);
    }
    if (w == 7 && lane < 8) {
      u32x4 v;
#pragma unroll
      for (int r = 0; r < 4; r++) v[r] = (unsigned)*(const unsigned short*)(LF + sl * 514 + 512 + r * 4352) | ((unsigned)*(const unsigned short*)(LF + sl * 514 + 512 + (4 + r) * 4352) << 16);
      stc(rs, WS_WT + (wline >> 7) * BP + 256 * 128 + sl * 16, v);
    }
  }
  if (w == 3 && lane < 8) stc(rs, WS_X1T + (xline >> 7) * BP + 256 * 128 + lane * 16, *(const u32x4*)(LS + 256 * 128 + lane * 16));
  const u32x4 ones = {0x3f803f80u, 0x3f803f80u, 0x3f803f80u, 0x3f803f80u};
  float gsum[4];
#pragma unroll
  for (int j = 0; j < 4; j++) {
    u32x4 xv = *(const u32x4*)(LX + (w * 4 + j) * 1024 + lane * 16);
    u32x4 pr;
#pragma unroll
    for (int d = 0; d < 4; d++) pr[d] = pk(bflo(xv[d]) * bflo(gwv[d]), bfhi(xv[d]) * bfhi(gwv[d]));
    gsum[j] = mma(as8(ones), as8(pr), f32x4{0, 0, 0, 0})[0];
  }
  float gs = (g == 0) ? gsum[0] : (g == 1) ? gsum[1] : (g == 2) ? gsum[2] : gsum[3];
  gs = rbf(dpp_sum16(gs));
  const float gg = rbf(__builtin_amdgcn_rcpf(1.f + __builtin_amdgcn_exp2f(gs * -1.44269504088896341f)));
  const int s = 32 * (4 * (a >> 2) + g) + n2s;
  if (i == 0) stc1(rs, WS_GT + (s >> 6) * 256 + (s & 15) * 16 + ((s >> 4) & 3) * 4, pk(gg, 1.f - gg));
}

template <class F> DI void p2_fft(F idle, char* lb, char* fl, char* f1seen, const char* ws, rsrc_t rs, int c, int cnt, int rep, unsigned& ep, int w, int lane) {
  const int i = lane & 15, g = lane >> 4;
  const int k1 = 16 * w + i;
  const float fk1 = (float)k1, fg = (float)g;
  u32x4 fre[4], fim[4], fc[2], fs[2], fn[2];
  {
    const float st = fk1 * (1.f / 64.f);
    const float a0 = fg * 4.f * st;
    const bool odd = i & 1;
    const unsigned m2 = odd ? 0x80008000u : 0u;
    const unsigned mre = (i & 2) ? 0x80008000u : 0u;
    const unsigned mim = ((i + 1) & 2) ? 0x80008000u : 0u;
#pragma unroll
    for (int d = 0; d < 4; d++) {
      const float ang = __builtin_fmaf((float)d, st, a0);
      const float cs = fcos(ang), sn = fsin(ang);
      const unsigned r0 = pk(cs, sn), i0 = pk(sn, cs) ^ 0x8000u;
      const unsigned r1 = (odd ? i0 : r0) ^ mre, i1 = (odd ? r0 : i0) ^ mim;
      fre[0][d] = r0; fim[0][d] = i0;
      fre[1][d] = r1; fim[1][d] = i1;
      fre[2][d] = r0 ^ m2; fim[2][d] = i0 ^ m2;
      fre[3][d] = r1 ^ m2; fim[3][d] = i1 ^ m2;
    }
#pragma unroll
    for (int ks = 0; ks < 4; ks++) { PIN(fre[ks]); PIN(fim[ks]); }
  }
  float c1[2][4], s1[2][4], c2[2][4], s2[2][4];
#pragma unroll
  for (int mt = 0; mt < 2; mt++) {
    const float fa = (float)perm32(mt, i);
    const float st = fa * (1.f / 32.f), a0 = fg * 8.f * st;
    const float st1 = fk1 * (1.f / 2048.f), b0 = (fg * 8.f + (float)(4 * mt)) * st1;
    const float st2 = fa * (1.f / 2048.f), d0 = (fg * 4.f + (float)(16 * w)) * st2;
#pragma unroll
    for (int d = 0; d < 4; d++) {
      const float x0 = __builtin_fmaf((float)(2 * d), st, a0), x1 = __builtin_fmaf((float)(2 * d + 1), st, a0);
      fc[mt][d] = pk(fcos(x0), fcos(x1));
      fs[mt][d] = pk(fsin(x0), fsin(x1));
      fn[mt][d] = fs[mt][d] ^ 0x80008000u;
      const float t1 = __builtin_fmaf((float)d, st1, b0);
      c1[mt][d] = fcos(t1); s1[mt][d] = fsin(t1);
      const float t2 = __builtin_fmaf((float)d, st2, d0);
      c2[mt][d] = fcos(t2); s2[mt][d] = fsin(t2);
      PIN(c1[mt][d]); PIN(s1[mt][d]); PIN(c2[mt][d]); PIN(s2[mt][d]);
    }
    PIN(fc[mt]); PIN(fs[mt]); PIN(fn[mt]);
    idle(1 + mt);
  }
  const unsigned yt = WS_YT + (32 * k1 + 8 * g) * 2 + ((c == 256) ? 1 : (c & 1) * 256 + (c & ~1)) * YP;
  idle(3);
  if (w == 0) { gwait<SLP>(rs, WS_F1 + rep * 128, ep); lset(f1seen); }
  else lwait(f1seen, 1);
  const unsigned sv = (w * 16 + (lane >> 3)) * BP + c * 128 + (lane & 7) * 16;
  asm volatile("s_mov_b32 m0, %0\n\ts_nop 0\n\tglobal_load_lds_dwordx4 %2, %1 sc1\n\tglobal_load_lds_dwordx4 %3, %1 offset:1024 sc1"
               :: "s"(laddr_of(lb) + 8192 + w * 2048), "s"(ws + WS_X1T), "v"(sv), "v"(sv + 8 * BP - 1024) : "memory");
  u32x4 wf[2];
#pragma unroll
  for (int mt = 0; mt < 2; mt++) wf[mt] = ldc(rs, WS_WT + sv + mt * 8 * BP);
  WAITV(2);
  linc(fl, lane);
  lspin(fl, 4);
  const f32x4 z4 = {0, 0, 0, 0};
  f32x4 re[2], im[2];
#pragma unroll
  for (int mt = 0; mt < 2; mt++) { re[mt] = z4; im[mt] = z4; }
#pragma unroll
  for (int ks = 0; ks < 4; ks++)
#pragma unroll
    for (int mt = 0; mt < 2; mt++) {
      s16x8 xa = lrd(lb + 8192 + ((mt * 4 + ks) * 64 + lane) * 16);
      re[mt] = mma(xa, as8(fre[ks]), re[mt]); im[mt] = mma(xa, as8(fim[ks]), im[mt]);
    }
  u32x4 pr, pi;
#pragma unroll
  for (int mt = 0; mt < 2; mt++) {
    float tr[4], ti[4];
#pragma unroll
    for (int r = 0; r < 4; r++) { tr[r] = re[mt][r] * c1[mt][r] + im[mt][r] * s1[mt][r]; ti[r] = im[mt][r] * c1[mt][r] - re[mt][r] * s1[mt][r]; }
    pr[2 * mt] = pk(tr[0], tr[1]); pr[2 * mt + 1] = pk(tr[2], tr[3]);
    pi[2 * mt] = pk(ti[0], ti[1]); pi[2 * mt + 1] = pk(ti[2], ti[3]);
  }
#pragma unroll
  for (int mt = 0; mt < 2; mt++) {
    re[mt] = mma(as8(fs[mt]), as8(pi), mma(as8(fc[mt]), as8(pr), z4));
    im[mt] = mma(as8(fn[mt]), as8(pr), mma(as8(fc[mt]), as8(pi), z4));
  }
#pragma unroll
  for (int mt = 0; mt < 2; mt++) {
    float tr[4], ti[4];
#pragma unroll
    for (int r = 0; r < 4; r++) {
      const float a = bflo(wf[mt][r]), b = bfhi(wf[mt][r]);
      tr[r] = re[mt][r] * a - im[mt][r] * b; ti[r] = re[mt][r] * b + im[mt][r] * a;
    }
    pr[2 * mt] = pk(tr[0], tr[1]); pr[2 * mt + 1] = pk(tr[2], tr[3]);
    pi[2 * mt] = pk(ti[0], ti[1]); pi[2 * mt + 1] = pk(ti[2], ti[3]);
  }
#pragma unroll
  for (int nt = 0; nt < 2; nt++) {
    re[nt] = mma(as8(pi), as8(fn[nt]), mma(as8(pr), as8(fc[nt]), z4));
    im[nt] = mma(as8(pi), as8(fc[nt]), mma(as8(pr), as8(fs[nt]), z4));
  }
#pragma unroll
  for (int nt = 0; nt < 2; nt++) {
    u32x4 v;
#pragma unroll
    for (int r = 0; r < 4; r++) v[r] = pk(re[nt][r] * c2[nt][r] - im[nt][r] * s2[nt][r], -re[nt][r] * s2[nt][r] - im[nt][r] * c2[nt][r]);
    *(u32x4*)(lb + ((nt * 4 + w) * 64 + lane) * 16) = v;
  }
  linc(fl + 4, lane);
  lspin(fl + 4, 4);
#pragma unroll
  for (int mt = 0; mt < 2; mt++) { re[mt] = z4; im[mt] = z4; }
#pragma unroll
  for (int ks = 0; ks < 4; ks++)
#pragma unroll
    for (int mt = 0; mt < 2; mt++) {
      s16x8 ya = lrd(lb + ((mt * 4 + ks) * 64 + lane) * 16);
      re[mt] = mma(ya, as8(fre[ks]), re[mt]); im[mt] = mma(ya, as8(fim[ks]), im[mt]);
    }
  u32x4 vr = {pk(re[0][0], re[0][1]), pk(re[0][2], re[0][3]), pk(re[1][0], re[1][1]), pk(re[1][2], re[1][3])};
  u32x4 vi = {pk(im[0][0], im[0][1]), pk(im[0][2], im[0][3]), pk(im[1][0], im[1][1]), pk(im[1][2], im[1][3])};
  stc(rs, yt, vr);
  if (c & 255) stc(rs, yt + YP, vi);
  if (c != 256) {
    WAITV(0);
    arrive(rs, 50 + w * 4 + cnt, cnt == 0 ? 65 : 64, WS_F3 + (w * 4 + cnt) * 128, 1, ep, lane);
  }
}

extern "C" __global__ __launch_bounds__(512, 2) void gm_all(const char* x, const char* wqkv, const char* wr, const char* wi, const char* gw, const char* wo, char* ws, char* out) {
  __shared__ __attribute__((aligned(16))) char lds[163840];
  const int b = blockIdx.x, xcd = b & 7, ii = b >> 3;
  const int w8 = __builtin_amdgcn_readfirstlane(threadIdx.x >> 6), lane = threadIdx.x & 63;
  const int w = w8 & 3;
  if ((ii >> 1) == 9 && w8 < 4) {
    unsigned long long pc; int o0, o1;
    asm volatile("s_getpc_b64 %0\n.Lgm_pc%=:\n\ts_mov_b32 %1, gm_all-.Lgm_pc%=\n\ts_mov_b32 %2, gm_end-.Lgm_pc%=" : "=s"(pc), "=s"(o0), "=s"(o1));
    const unsigned off = ((ii & 1) * 256 + threadIdx.x) * 128;
    if (off < (unsigned)(o1 - o0))
      asm volatile("s_mov_b32 m0, %0\n\ts_nop 0\n\tglobal_load_lds_dword %2, %1" :: "s"(laddr_of(lds) + L_DUMP), "s"(pc + (long long)o0), "v"(off) : "memory");
  }
  unsigned ep;
  const char* epp = ws + WS_E;
  const rsrc_t rs = __builtin_amdgcn_make_buffer_rsrc(ws, 0, WS_END, 0x00020000);
  char* FL = lds + L_FL;
  const int ml = ii & 3, nl = ii >> 2;
  const int mtile = xcd * 4 + ml, ntile = nl;
  const bool aux = ii >= 24;
  {
    const int m = (xcd >> 1) * 4 + ml;
    const int nt = nl < 4 ? (xcd & 1) * 4 + nl : 8 + (xcd & 1) * 2 + (nl - 4);
    if (w8 < 4 || aux) {
      if (w8 == 0) *(LAS unsigned*)(FL + lane * 4) = 0;
      if (aux) p1_rows(lds, x, gw, wo, wr, wi, rs, xcd * 8 + (ii - 24), epp, w8, lane);
      if (w8 < 4) {
        __builtin_amdgcn_s_setprio(2);
        if (aux) {
          ep = epoch_get(lds);
          if (w == 1) gwait<SLP>(rs, WS_F0 + xcd * 128 + (lane & 1) * 16, ep);
          WAITV(0);
          BAR();
          if (w == 1) { arrive(rs, 0, 64, WS_F1, 8, ep, lane); lset(FL + 124); }
          lwait(FL + 124, 1);
#pragma unroll 1
          for (int j = 0; j < 4; j++) wo_dma(lds, wo, ntile, j, w, lane);
        }
        else if (nl < 4) p1_gemm<false>(lds, rs, m, nt, ep, epp, w, lane);
        else p1_gemm<true>(lds, rs, m, nt, ep, epp, w, lane);
        __builtin_amdgcn_s_setprio(0);
      }
    } else {
      const int col0 = nl >= 4 ? (nt - 8) * 128 : 512 + nt * 64;
      const int col1 = nl >= 4 ? col0 + 64 : col0 + 512;
      p1_prod(lds, x, wqkv, wo, ntile, m, col0, col1, (2 * ml + nl) & ROT1, w, lane);
    }
  }
  const int i = lane & 15, g = lane >> 4, q = i >> 2, p = lane & 3;
  const int fsw = (2 * g + (q >> 1)) & 3;
  if (w8 >= 4 || b == SPECIAL) {
    const bool pg = w8 >= 4;
    if (pg) {
      ep = epoch_get(lds);
      if (!aux) {
        if (w == 0) { gwait<SLP>(rs, WS_F0 + xcd * 128 + (lane & 1) * 16, ep); lset(FL + 100); }
        wo_dma(lds, wo, ntile, 2, w, lane); wo_dma(lds, wo, ntile, 3, w, lane);
      }
    }
    if (!aux && nl >= 4) __builtin_amdgcn_s_setprio(3); else __builtin_amdgcn_s_setprio(1);
    auto idle = [&](int st) {
      if (st == 1 && pg && aux) { WAITV(0); BAR(); }
      if (st == 3 && !aux) { WAITV(0); linc(FL, lane); }
    };
    p2_fft(idle, pg ? lds + L_FFT : lds + L_Y, pg ? FL + 4 : FL + 20, pg ? FL + 16 : FL + 28, ws, rs, pg ? b : 256, pg ? (b & 1) * 2 + (b >> 7) : 0, xcd, ep, w, lane);
  }
  if (w8 < 4) {
    linc(FL + 80, lane);
    if (aux) { if (b != SPECIAL) WAITV(0); linc(FL, lane); }
    const int stg = 4 * mtile + w;
    bool kok = false;
    if (w == 0) {
#pragma unroll 1
      for (int sp = 0; sp < 200000; sp++) {
        FENCE();
        const bool ok = ldc(rs, (lane & 1) ? WS_F2K + xcd * 128 : WS_F2Q + (mtile >> 1) * 128)[0] == ep;
        const unsigned long long mk = __ballot(ok);
        kok = mk & 2;
        if (mk & 1) break;
        __builtin_amdgcn_s_sleep(SLP);
      }
      FENCE();
      if (kok) lset(FL + 88);
      lset(FL + 104);
    }
    unsigned tk = 0;
    if (b == SPECIAL) { WAITV(0); tk = ticket_issue(rs, 50 + w * 4, lane); FENCE(); }
    lwait(FL + 104, 1);
    u32x4 pq[4][4];
#pragma unroll
    for (int jj = 0; jj < 4; jj++)
#pragma unroll
      for (int u = 0; u < 4; u++) pq[jj][u] = ldc(rs, WS_PQ + (((4 * mtile + ((w + 1 + jj) & 3)) * 16 + 4 * w + u) * 64 + lane) * 16);
    lwait(FL, 4);
    s16x8 wf[4][4];
    {
      const int toffA = L_WO + w * 16384 + (4 * g + q) * 128 + (fsw * 32) + p * 8;
#pragma unroll
      for (int u = 0; u < 4; u++)
#pragma unroll
        for (int a = 0; a < 4; a++) { const char* pp = lds + (toffA ^ (32 * a)) + u * 2048; wf[u][a] = trd2(pp, pp + 8192); }
    }
    if (b == SPECIAL) ticket_done(rs, tk, 65, WS_F3 + (w * 4) * 128, 1, ep, lane);
    if (w == 0) { if (!kok) { gwait<SLP>(rs, WS_F2K + xcd * 128, ep); lset(FL + 88); } }
    else lwait(FL + 88, 1);
    u32x4 kr[6];
#pragma unroll
    for (int j = 0; j < 6; j++) kr[j] = ldc(rs, WS_KVR + ((j < 4 ? 4 * w + j : 12 + 2 * w + j) * 64 + lane) * 16);
    lwait(FL + 16, 1);
    const unsigned gv = __builtin_amdgcn_raw_buffer_load_b32(rs, WS_GT + mtile * 256 + i * 16 + w * 4, 0, 16);
    const f32x4 z4 = {0, 0, 0, 0};
    const f32x4 eps = {1e-6f, 1e-6f, 1e-6f, 1e-6f};
    f32x4 acc[4];
    f32x4 nm[2][4][2], dn[2][4];
    auto nd = [&](int jj) {
#pragma unroll
      for (int u = 0; u < 4; u++) {
        dn[jj & 1][u] = mma(as8(kr[4 + (u >> 1)]), as8(pq[jj][u]), eps);
        nm[jj & 1][u][0] = mma16(as4(u32x2{kr[u][0], kr[u][1]}), as4(u32x2{pq[jj][u][0], pq[jj][u][1]}), z4);
        nm[jj & 1][u][1] = mma16(as4(u32x2{kr[u][2], kr[u][3]}), as4(u32x2{pq[jj][u][2], pq[jj][u][3]}), z4);
      }
    };
    u32x4 of[2][4];
    auto sc = [&](int jj) {
#pragma unroll
      for (int u = 0; u < 4; u++) {
        const f32x4 n0 = nm[jj & 1][u][0] * __builtin_amdgcn_rcpf(dn[jj & 1][u][2 * (u & 1)]);
        const f32x4 n1 = nm[jj & 1][u][1] * __builtin_amdgcn_rcpf(dn[jj & 1][u][2 * (u & 1) + 1]);
        of[jj & 1][u] = u32x4{pk(n0[0], n0[1]), pk(n0[2], n0[3]), pk(n1[0], n1[1]), pk(n1[2], n1[3])};
      }
    };
    auto pj = [&](int jj) {
#pragma unroll
      for (int u = 0; u < 4; u++)
#pragma unroll
        for (int a = 0; a < 4; a++) acc[a] = mma(wf[u][a], as8(of[jj & 1][u]), u ? acc[a] : z4);
      if (jj < 3) {
#pragma unroll
        for (int a = 0; a < 4; a++) *(f32x4*)(lds + L_WO + w * 16384 + (jj * 4 + a) * 1024 + lane * 16) = acc[a];
      }
    };
    nd(0); SCHED();
    nd(1); sc(0);
#pragma unroll
    for (int t = 0; t < 12; t++) { SGB(0x008, 1); SGB(0x002, 3); }
    SCHED();
#pragma unroll
    for (int jj = 0; jj < 3; jj++) {
      pj(jj); sc(jj + 1);
#pragma unroll
      for (int t = 0; t < 16; t++) { SGB(0x008, 1); SGB(0x002, 3); }
      SCHED();
      if (jj < 2) { nd(jj + 2); SCHED(); }
    }
    linc(FL + 92, lane);
    pj(3);
    lwait(FL + 92, 4);
#pragma unroll
    for (int ww = 1; ww < 4; ww++)
#pragma unroll
      for (int a = 0; a < 4; a++) acc[a] += *(const f32x4*)(lds + L_WO + ((w + ww) & 3) * 16384 + ((3 - ww) * 4 + a) * 1024 + lane * 16);
    lwait(FL + 48, 1);
    lspin(FL + 48, 4);
    const float ga = bflo(gv), gb = bfhi(gv);
    unsigned pv[4][2];
#pragma unroll
    for (int a = 0; a < 2; a++) {
      const f32x4 t0 = *(const f32x4*)(lds + L_FFT + ((w * 4 + a) * 64 + lane) * 16);
      const f32x4 t1 = *(const f32x4*)(lds + L_FFT + ((w * 4 + a + 2) * 64 + lane) * 16);
      const f32x4 v0 = acc[a] * ga + (t0 + t1) * gb, v1 = acc[a + 2] * ga + (t0 - t1) * gb;
      pv[a][0] = pk(v0[0], v0[1]); pv[a][1] = pk(v0[2], v0[3]);
      pv[a + 2][0] = pk(v1[0], v1[1]); pv[a + 2][1] = pk(v1[2], v1[3]);
    }
#pragma unroll
    for (int a = 0; a < 2; a++) {
      auto s0 = __builtin_amdgcn_permlane16_swap(pv[2 * a][0], pv[2 * a + 1][0], false, false);
      auto s1 = __builtin_amdgcn_permlane16_swap(pv[2 * a][1], pv[2 * a + 1][1], false, false);
      u32x4 v = {s0[0], s1[0], s0[1], s1[1]};
      char* op = out + (size_t)(16 * stg + i) * 1024 + (256 * a + 32 * ntile + 16 * (g & 1) + 8 * (g >> 1)) * 2;
      asm volatile("global_store_dwordx4 %0, %1, off sc1\n\ts_nop 1" :: "v"(op), "v"(v) : "memory");
    }
    return;
  }
  {
    const float sgn = (i & 1) ? -1.f : 1.f;
    const unsigned k0v = (i & 1) ? 0xb5803580u : 0x35803580u;
    const float fn = (float)(32 * ntile + 16 * (w & 1) + i) * (1.f / 512.f);
    const float t2 = 2.f * fcos(fn * 32.f);
    f32x2 c0[4], c1[4];
#pragma unroll
    for (int d = 0; d < 4; d++) {
      const float kf = (float)(4 * g + 128 * (w & 1) + (w >> 1)) + (float)(16 * (d >> 1) + 2 * (d & 1));
      const float a0 = kf * fn, a1 = (kf + 32.f) * fn;
      c0[d] = f32x2{fcos(a0), fsin(a0)} * 0x1p-19f;
      c1[d] = f32x2{fcos(a1), fsin(a1)} * 0x1p-19f;
    }
    u32x4 gf[8];
#pragma unroll
    for (int j = 0; j < 2; j++) {
#pragma unroll
      for (int h = 0; h < 2; h++) {
#pragma unroll
        for (int d = 0; d < 4; d++) {
          gf[4 * j + 2 * h][d] = pk(c0[d][0], c0[d][1]);
          gf[4 * j + 2 * h + 1][d] = pk(c1[d][0], c1[d][1]);
          c0[d] = c1[d] * t2 - c0[d];
        }
#pragma unroll
        for (int d = 0; d < 4; d++) c1[d] = c0[d] * t2 - c1[d];
      }
      gf[4 * j][0] = (w == j && g == 0) ? k0v : gf[4 * j][0];
      if (j == 0) {
        const float sg = (w & 1) ? sgn : 1.f;
#pragma unroll
        for (int d = 0; d < 4; d++) { c0[d] *= sg; c1[d] *= sg; }
      }
    }
#pragma unroll
    for (int k = 0; k < 8; k++) PIN(gf[k]);
    gwait<SLP>(rs, WS_F3 + ((mtile >> 3) * 4 + w) * 128, ep);
    lwait(FL + 80, 4);
    linc(FL + 96, lane);
    const int toffB = L_Y + (4 * g + q) * 128 + (fsw * 32) + p * 8;
    f32x4 accb[4];
    s16x8 tb_[2][2][4];
    auto rd = [&](int u) {
      const char* slot = lds + (w ^ (u >> 1)) * 16384 + (u & 1) * 8192;
#pragma unroll
      for (int ks = 0; ks < 2; ks++)
#pragma unroll
        for (int c = 0; c < 4; c++) tb_[u & 1][ks][c] = trd2(slot + (toffB ^ (32 * c)) + ks * 4096, slot + (toffB ^ (32 * c)) + ks * 4096 + 2048);
    };
    auto mm = [&](int u) {
#pragma unroll
      for (int ks = 0; ks < 2; ks++)
#pragma unroll
        for (int c = 0; c < 4; c++) accb[c] = mma(as8(gf[2 * u + ks]), tb_[u & 1][ks][c], accb[c]);
    };
    {
      const unsigned v = (lane >> 3) * YP + ((((lane & 7) >> 1) ^ ((lane >> 4) & 3)) * 32) + (lane & 1) * 16;
      const char* src = ws + WS_YT + (size_t)(w * 128) * YP + mtile * 128;
      const unsigned la = laddr_of(lds) + L_Y + w * 16384;
#pragma unroll
      for (int j = 0; j < 4; j++) glds4c(la + j * 4096, src + (size_t)j * (32 * YP), v, v + (8 * YP - 1024), v + 2 * (8 * YP - 1024), v + 3 * (8 * YP - 1024));
    }
#pragma unroll
    for (int c = 0; c < 4; c++) accb[c] = f32x4{0, 0, 0, 0};
    WAITV(8);
    rd(0);
    WAITV(0);
    lset(FL + 32 + w * 4);
    rd(1); mm(0);
#pragma unroll
    for (int t = 0; t < 8; t++) { SGB(0x100, 2); SGB(0x008, 1); }
    SCHED();
    lspin(FL + 32 + (w ^ 1) * 4, 1);
    rd(2); mm(1);
#pragma unroll
    for (int t = 0; t < 8; t++) { SGB(0x100, 2); SGB(0x008, 1); }
    SCHED();
    rd(3); mm(2);
#pragma unroll
    for (int t = 0; t < 8; t++) { SGB(0x100, 2); SGB(0x008, 1); }
    SCHED();
    mm(3);
    lwait(FL + 96, 4);
    if (b == 0 && w == 1 && lane == 0) stc1(rs, WS_E, ep);
#pragma unroll
    for (int c = 0; c < 4; c++) *(f32x4*)(lds + L_FFT + ((c * 4 + w) * 64 + lane) * 16) = accb[c];
    linc(FL + 48, lane);
  }
}

extern "C" __global__ void gm_end() {}

extern "C" void kernel_launch(void* const* d_in, const int* in_sizes, int n_in, void* d_out, int out_size, void* d_ws, size_t ws_size, hipStream_t stream) {
  gm_all<<<256, 512, 0, stream>>>((const char*)d_in[0], (const char*)d_in[1], (const char*)d_in[4], (const char*)d_in[5], (const char*)d_in[6],
                                  (const char*)d_in[2], (char*)d_ws, (char*)d_out);
}
```

```cpp
#include <hip/hip_runtime.h>
#include <stdint.h>

typedef short s16x4 __attribute__((ext_vector_type(4)));
typedef short s16x8 __attribute__((ext_vector_type(8)));
typedef float f32x4 __attribute__((ext_vector_type(4)));
typedef float f32x2 __attribute__((ext_vector_type(2)));
typedef unsigned u32x4 __attribute__((ext_vector_type(4)));
typedef unsigned u32x2 __attribute__((ext_vector_type(2)));
typedef __bf16 bfx2 __attribute__((ext_vector_type(2)));
#define LAS __attribute__((address_space(3)))
#define DI __device__ __forceinline__
static constexpr int ROT1 = 6;
static constexpr int SLP = 2;

static constexpr unsigned BP = 258 * 128;
static constexpr unsigned YP = 4224;
static constexpr unsigned CS = 4352;
static constexpr unsigned WS_X1T = 0;
static constexpr unsigned WS_WT  = WS_X1T + (9u << 18);
static constexpr unsigned WS_PQ  = WS_WT + (9u << 18);
static constexpr unsigned WS_KVP = WS_PQ + (2u << 20);
static constexpr unsigned WS_GT  = WS_KVP + (512u << 10);
static constexpr unsigned WS_KVR = WS_GT + (16u << 10);
static constexpr unsigned WS_YT  = WS_KVR + (32u << 10);
static constexpr unsigned WS_E   = WS_YT + (9u << 18);
static constexpr unsigned WS_CNT = WS_E + 4096;
static constexpr unsigned WS_F0  = WS_CNT + 72 * CS;
static constexpr unsigned WS_F1  = WS_F0 + 8 * 128;
static constexpr unsigned WS_F2K = WS_F1 + 8 * 128;
static constexpr unsigned WS_F2Q = WS_F2K + 8 * 128;
static constexpr unsigned WS_F3  = WS_F2Q + 16 * 128;
static constexpr unsigned WS_END = WS_F3 + 16 * 128;
static constexpr int SPECIAL = 255;

static constexpr int L_WO = 0, L_Y = 65536, L_FFT = 131072, L_DUMP = 155648, L_FL = 163840 - 256;

DI unsigned pk(float lo, float hi) { f32x2 p = {lo, hi}; return __builtin_bit_cast(unsigned, __builtin_convertvector(p, bfx2)); }
DI float bflo(unsigned u) { return __builtin_bit_cast(float, u << 16); }
DI float bfhi(unsigned u) { return __builtin_bit_cast(float, u & 0xffff0000u); }
DI float rbf(float v) { return bflo(pk(v, 0.f)); }
DI s16x8 as8(u32x4 v) { return __builtin_bit_cast(s16x8, v); }
DI s16x4 as4(u32x2 v) { return __builtin_bit_cast(s16x4, v); }
DI f32x4 mma(s16x8 a, s16x8 b, f32x4 c) { return __builtin_amdgcn_mfma_f32_16x16x32_bf16(a, b, c, 0, 0, 0); }
DI f32x4 mma16(s16x4 a, s16x4 b, f32x4 c) { return __builtin_amdgcn_mfma_f32_16x16x16bf16_1k(a, b, c, 0, 0, 0); }
DI void mfa(f32x4& c, s16x8 a, s16x8 b) { asm volatile("v_mfma_f32_16x16x32_bf16 %0, %1, %2, %0" : "+v"(c) : "v"(a), "v"(b)); }
DI s16x4 trd(const char* l) { return __builtin_amdgcn_ds_read_tr16_b64_v4i16((LAS s16x4*)l); }
DI s16x8 cat(s16x4 a, s16x4 b) { return __builtin_shufflevector(a, b, 0, 1, 2, 3, 4, 5, 6, 7); }
DI s16x8 trd2(const char* l0, const char* l1) { return cat(trd(l0), trd(l1)); }
DI s16x8 lrd(const char* l) { return *(const s16x8*)l; }
DI float fsin(float rev) { return __builtin_amdgcn_sinf(rev); }
DI float fcos(float rev) { return __builtin_amdgcn_cosf(rev); }
DI unsigned laddr_of(const char* l) { return (unsigned)(size_t)(const LAS char*)l; }
DI void glds4(unsigned laddr, const char* sb, unsigned v0, unsigned v1, unsigned v2, unsigned v3) {
  asm volatile("s_mov_b32 m0, %0\n\ts_nop 0\n\t"
               "global_load_lds_dwordx4 %2, %1\n\t"
               "global_load_lds_dwordx4 %3, %1 offset:1024\n\t"
               "global_load_lds_dwordx4 %4, %1 offset:2048\n\t"
               "global_load_lds_dwordx4 %5, %1 offset:3072"
               :: "s"(laddr), "s"(sb), "v"(v0), "v"(v1), "v"(v2), "v"(v3) : "memory");
}
DI void glds4c(unsigned laddr, const char* sb, unsigned v0, unsigned v1, unsigned v2, unsigned v3) {
  asm volatile("s_mov_b32 m0, %0\n\ts_nop 0\n\t"
               "global_load_lds_dwordx4 %2, %1 sc1\n\t"
               "global_load_lds_dwordx4 %3, %1 offset:1024 sc1\n\t"
               "global_load_lds_dwordx4 %4, %1 offset:2048 sc1\n\t"
               "global_load_lds_dwordx4 %5, %1 offset:3072 sc1"
               :: "s"(laddr), "s"(sb), "v"(v0), "v"(v1), "v"(v2), "v"(v3) : "memory");
}
DI int perm32(int t, int i) { return 8 * (i >> 2) + 4 * t + (i & 3); }
DI float phi(float t) {
  float e = __builtin_amdgcn_exp2f(t * 1.44269504088896341f);
  e = __builtin_amdgcn_fmed3f(e, 0.f, 1.f);
  return fmaxf(t + 1e-6f, 1e-6f) + e;
}
#define SCHED() __builtin_amdgcn_sched_barrier(0)
#define SGB(mask, n) __builtin_amdgcn_sched_group_barrier(mask, n, 0)
#define PIN(x) asm volatile("" : "+v"(x))
#define WAITV(n) asm volatile("s_waitcnt vmcnt(" #n ")" ::: "memory")
#define BAR() asm volatile("s_waitcnt lgkmcnt(0)\n\ts_barrier" ::: "memory")
#define FENCE() asm volatile("" ::: "memory")

typedef __amdgpu_buffer_rsrc_t rsrc_t;
DI u32x4 ldc(rsrc_t r, unsigned off) { return __builtin_amdgcn_raw_buffer_load_b128(r, off, 0, 16); }
DI void stc(rsrc_t r, unsigned off, u32x4 v) { __builtin_amdgcn_raw_buffer_store_b128(v, r, off, 0, 16); }
DI void stc1(rsrc_t r, unsigned off, unsigned v) { __builtin_amdgcn_raw_buffer_store_b32(v, r, off, 0, 16); }
template <int SL> DI void gwait(rsrc_t r, unsigned off, unsigned ep) {
#pragma unroll 1
  for (int sp = 0; sp < 200000; sp++) {
    FENCE();
    const u32x4 f = ldc(r, off);
    if (__all(f[0] == ep && f[1] == ep && f[2] == ep && f[3] == ep)) break;
    __builtin_amdgcn_s_sleep(SL);
  }
  FENCE();
}
DI unsigned ticket(rsrc_t r, int c, int lane) {
  unsigned old = 0;
  if (lane == 0) old = __builtin_amdgcn_raw_ptr_buffer_atomic_add_i32(1, r, WS_CNT + c * CS, 0, 16);
  old = __builtin_amdgcn_readfirstlane(old);
  FENCE();
  return old;
}
DI void arrive(rsrc_t r, int c, unsigned n, unsigned flag, int nrep, unsigned ep, int lane) {
  if (ticket(r, c, lane) == n - 1 && lane < nrep) stc(r, flag + lane * 128, u32x4{ep, ep, ep, ep});
}
DI void linc(char* p, int lane) { if (lane == 0) asm volatile("ds_add_u32 %0, %1" :: "v"(laddr_of(p)), "v"(1u) : "memory"); }
DI void lset(char* p) { asm volatile("ds_write_b32 %0, %1" :: "v"(laddr_of(p)), "v"(1u) : "memory"); }
DI void lwait(const char* p, unsigned want) {
#pragma unroll 1
  for (int sp = 0; sp < 4000000; sp++) {
    if ((unsigned)__builtin_amdgcn_readfirstlane(*(const volatile LAS unsigned*)p) >= want) break;
    __builtin_amdgcn_s_sleep(1);
  }
  FENCE();
}

DI void lspin(const char* p, unsigned want) {
#pragma unroll 1
  for (int sp = 0; sp < 8000000; sp++)
    if ((unsigned)__builtin_amdgcn_readfirstlane(*(const volatile LAS unsigned*)p) >= want) break;
  FENCE();
}

DI void epoch_dma(char* lds, const char* p, int lane) {
  if (lane == 0) asm volatile("s_mov_b32 m0, %0\n\ts_nop 0\n\tglobal_load_lds_dword %2, %1 sc1" :: "s"(laddr_of(lds) + L_FL - 16), "s"(p), "v"(0u) : "memory");
}
DI unsigned epoch_get(const char* lds) { return (unsigned)__builtin_amdgcn_readfirstlane(*(const volatile LAS unsigned*)(lds + L_FL - 16)) * 1664525u + 1013904223u; }
DI unsigned ticket_issue(rsrc_t r, int c, int lane) {
  unsigned old = 0;
  if (lane == 0) old = __builtin_amdgcn_raw_ptr_buffer_atomic_add_i32(1, r, WS_CNT + c * CS, 0, 16);
  return old;
}
DI void ticket_done(rsrc_t r, unsigned old, unsigned n, unsigned flag, int nrep, unsigned ep, int lane) {
  if ((unsigned)__builtin_amdgcn_readfirstlane(old) == n - 1 && lane < nrep) stc(r, flag + lane * 128, u32x4{ep, ep, ep, ep});
}

DI void wo_dma(char* lds, const char* wo, int ntile, int j, int w, int lane) {
  const int blk = ((lane & 7) >> 1) ^ ((lane >> 4) & 3);
  const unsigned v = (lane >> 3) * 1024 + (blk & 1) * 32 + (blk >> 1) * 512 + (lane & 1) * 16;
  glds4(laddr_of(lds) + L_WO + j * 16384 + w * 4096, wo + (size_t)(j * 128 + w * 32) * 1024 + ntile * 64, v, v + 7168, v + 2 * 7168, v + 3 * 7168);
}

DI void p1_prod(char* lds, const char* x, const char* wqkv, const char* wo, int ntile, int m, int col0, int col1, int rot, int w, int lane) {
  unsigned voffA[4], voffB[4];
#pragma unroll
  for (int j = 0; j < 4; j++) {
    const int par = j & 1;
    voffA[j] = (lane >> 3) * 1024 + (((lane & 7) ^ ((4 * par + (lane >> 4)) & 7)) * 16) + j * (8192 - 1024);
    voffB[j] = (lane >> 3) * 3072 + ((((lane & 7) >> 1) ^ (((lane >> 4) & 1) | (par << 1))) * 32) + (lane & 1) * 16 + j * (24576 - 1024);
  }
  const unsigned lbase = laddr_of(lds) + w * 4096;
  const char* xa = x + (size_t)(m * 128 + w * 32) * 1024;
  const char* wb = wqkv + (size_t)((w & 1) * 32) * 3072 + ((w >> 1) ? col1 : col0) * 2;
  auto issue = [&](int idx) {
    const int ks = (idx + rot) & 7;
    const unsigned slot = lbase + (idx & 3) * 32768;
    glds4(slot, xa + ks * 128, voffA[0], voffA[1], voffA[2], voffA[3]);
    glds4(slot + 16384, wb + (size_t)(ks * 64) * 3072, voffB[0], voffB[1], voffB[2], voffB[3]);
  };
  issue(0); issue(1);
  WAITV(8); BAR();
  issue(2); issue(3);
#pragma unroll 1
  for (int j = 1; j < 5; j++) { WAITV(16); BAR(); issue(j + 3); }
  WAITV(16); BAR();
  wo_dma(lds, wo, ntile, 0, w, lane);
  WAITV(12); BAR();
  wo_dma(lds, wo, ntile, 1, w, lane);
  WAITV(8); BAR();
}

template <bool QT>
DI void p1_gemm(char* lds, rsrc_t rs, int m, int nt, unsigned& ep, const char* epp, int w, int lane) {
  const int i = lane & 15, g = lane >> 4, q = i >> 2, p = lane & 3;
  f32x4 acc[8][2];
#pragma unroll
  for (int a = 0; a < 8; a++) { acc[a][0] = f32x4{0, 0, 0, 0}; acc[a][1] = f32x4{0, 0, 0, 0}; }
  const int aoff = i * 128 + ((g ^ (i >> 1)) * 16);
  const int boff = 16384 + (8 * g + q) * 128 + ((w ^ (((q >> 1) & 1) | ((g & 1) << 1))) * 32) + p * 8;
  s16x8 A[2][2][8]; s16x4 B[2][2][2][2];
  auto rd1 = [&](int buf, const char* slot, int r) {
    const int ks = r / 12, rr = r % 12;
    const int t = rr == 0 ? 0 : rr == 1 ? 1 : rr == 3 ? 2 : rr == 4 ? 3 : -1;
    const int a = rr == 2 ? 0 : rr - 4;
    if (t >= 0) B[buf][ks][t >> 1][t & 1] = trd(slot + boff + (t >> 1) * 8192 + ks * 4096 + (t & 1) * 512);
    else A[buf][ks][a] = lrd(slot + (aoff ^ (ks * 64)) + a * 2048);
  };
  auto mf1 = [&](int buf, int ks, int mt, int hh) {
    const s16x8 bf = cat(B[buf][ks][hh][0], B[buf][ks][hh][1]);
    if (QT) mfa(acc[mt][hh], bf, A[buf][ks][mt]); else mfa(acc[mt][hh], A[buf][ks][mt], bf);
  };
  auto region = [&](int cur, const char* slot) {
#pragma unroll
    for (int mi = 0; mi < 32; mi++) {
      mf1(cur, mi >> 4, (mi & 15) >> 1, mi & 1);
      SCHED();
      if (mi < 24) { rd1(cur ^ 1, slot, mi); SCHED(); }
    }
  };
  if (w == 0) epoch_dma(lds, epp, lane);
  const bool zw = QT && blockIdx.x >= 128 && blockIdx.x < 136;
  if (zw && w == 3 && lane < 9) stc1(rs, WS_CNT + ((blockIdx.x & 7) * 9 + lane) * CS, 0u);
  __builtin_amdgcn_s_waitcnt(0xC07F);
  BAR();
#pragma unroll
  for (int r = 0; r < 24; r++) rd1(0, lds, r);
  SCHED();
#pragma unroll 1
  for (int it = 0; it < 3; it++) {
    BAR(); SCHED();
    region(0, lds + ((2 * it + 1) & 3) * 32768);
    if (it == (zw ? 0 : 1)) WAITV(0);
    BAR(); SCHED();
    if (zw && it == 0 && w == 3) { const unsigned e = epoch_get(lds); if (lane < 8) stc1(rs, WS_F0 + lane * 128 + (blockIdx.x & 7) * 4, e); }
    region(1, lds + ((2 * it + 2) & 3) * 32768);
  }
  ep = epoch_get(lds);
  BAR(); SCHED();
  region(0, lds + 3 * 32768);
  asm volatile("s_nop 7");
  SCHED();
  auto mmh = [&](int hh) {
#pragma unroll
    for (int mt = 0; mt < 8; mt++)
#pragma unroll
      for (int ks = 0; ks < 2; ks++) {
        const s16x8 bf = cat(B[1][ks][hh][0], B[1][ks][hh][1]);
        acc[mt][hh] = QT ? mma(bf, A[1][ks][mt], acc[mt][hh]) : mma(A[1][ks][mt], bf, acc[mt][hh]);
      }
  };
  mmh(0);
  SCHED();
  if (QT) {
    mmh(1);
#pragma unroll
    for (int mt = 0; mt < 8; mt++) {
      u32x4 v = {pk(phi(acc[mt][0][0]), phi(acc[mt][0][1])), pk(phi(acc[mt][0][2]), phi(acc[mt][0][3])),
                 pk(phi(acc[mt][1][0]), phi(acc[mt][1][1])), pk(phi(acc[mt][1][2]), phi(acc[mt][1][3]))};
      stc(rs, WS_PQ + (((m * 8 + mt) * 16 + (nt - 8) * 4 + w) * 64 + lane) * 16, v);
    }
    lwait(lds + L_FL + 100, 1);
    WAITV(0);
    arrive(rs, 34 + m, 16, WS_F2Q + m * 128, 1, ep, lane);
  } else {
    mmh(1);
    u32x4 pa[4];
#pragma unroll
    for (int u = 0; u < 4; u++)
      pa[u] = u32x4{pk(phi(acc[2 * u][0][0]), phi(acc[2 * u][0][1])), pk(phi(acc[2 * u][0][2]), phi(acc[2 * u][0][3])),
                    pk(phi(acc[2 * u + 1][0][0]), phi(acc[2 * u + 1][0][1])), pk(phi(acc[2 * u + 1][0][2]), phi(acc[2 * u + 1][0][3]))};
#pragma unroll
    for (int t = 0; t < 16; t++) { SGB(0x008, 1); SGB(0x002, 2); }
    SCHED();
    f32x4 kv = {0, 0, 0, 0}, z = {0, 0, 0, 0};
    const u32x4 ones = {0x3f803f80u, 0x3f803f80u, 0x3f803f80u, 0x3f803f80u};
#pragma unroll
    for (int u = 0; u < 4; u++) {
      u32x4 b = {pk(acc[2 * u][1][0], acc[2 * u][1][1]), pk(acc[2 * u][1][2], acc[2 * u][1][3]),
                 pk(acc[2 * u + 1][1][0], acc[2 * u + 1][1][1]), pk(acc[2 * u + 1][1][2], acc[2 * u + 1][1][3])};
      z = mma(as8(pa[u]), as8(ones), z);
      kv = mma(as8(pa[u]), as8(b), kv);
    }
    const int head = nt * 4 + w;
    u32x4 v = {pk(kv[0], kv[1]), pk(kv[2], kv[3]), pk(z[0], z[1]), pk(z[2], z[3])};
    stc(rs, WS_KVP + ((m * 32 + head) * 64 + lane) * 16, v);
    lwait(lds + L_FL + 100, 1);
    WAITV(0);
    if (ticket(rs, 1 + head, lane) == 15) {
      u32x4 kl[16];
#pragma unroll
      for (int pp = 0; pp < 16; pp++) kl[pp] = ldc(rs, WS_KVP + ((pp * 32 + head) * 64 + lane) * 16);
      u32x4 r;
#pragma unroll
      for (int d = 0; d < 4; d++) {
        float lo[4], hi[4];
#pragma unroll
        for (int j = 0; j < 4; j++) {
          lo[j] = (bflo(kl[4 * j][d]) + bflo(kl[4 * j + 1][d])) + (bflo(kl[4 * j + 2][d]) + bflo(kl[4 * j + 3][d]));
          hi[j] = (bfhi(kl[4 * j][d]) + bfhi(kl[4 * j + 1][d])) + (bfhi(kl[4 * j + 2][d]) + bfhi(kl[4 * j + 3][d]));
        }
        r[d] = pk((lo[0] + lo[1]) + (lo[2] + lo[3]), (hi[0] + hi[1]) + (hi[2] + hi[3]));
      }
      const int u = (head >> 3) * 4 + (head & 3), hh = (head >> 2) & 1;
      const bool zr = (i & 3) == hh + 2 * (u & 1);
      __builtin_amdgcn_raw_buffer_store_b64(u32x2{r[0], r[1]}, rs, WS_KVR + (u * 64 + lane) * 16 + hh * 8, 0, 16);
      if (zr) stc(rs, WS_KVR + 16384 + ((u >> 1) * 64 + lane) * 16, hh ? u32x4{0u, 0u, r[2], r[3]} : u32x4{r[2], r[3], 0u, 0u});
      WAITV(0);
      arrive(rs, 33, 32, WS_F2K, 8, ep, lane);
    }
  }
}

DI float dpp_sum16(float v) {
  v += __builtin_bit_cast(float, __builtin_amdgcn_update_dpp(0, __builtin_bit_cast(int, v), 0x128, 0xf, 0xf, false));
  v += __builtin_bit_cast(float, __builtin_amdgcn_update_dpp(0, __builtin_bit_cast(int, v), 0x124, 0xf, 0xf, false));
  v += __builtin_bit_cast(float, __builtin_amdgcn_update_dpp(0, __builtin_bit_cast(int, v), 0x122, 0xf, 0xf, false));
  v += __builtin_bit_cast(float, __builtin_amdgcn_update_dpp(0, __builtin_bit_cast(int, v), 0x121, 0xf, 0xf, false));
  return v;
}

DI void p1_rows(char* lds, const char* x, const char* gw, const char* wo, const char* wr, const char* wi, rsrc_t rs, int a, const char* epp, int w, int lane) {
  const int i = lane & 15, g = lane >> 4;
  char* LX = lds;
  char* LS = lds + 32768;
  char* LC = lds + 106496;
  const int amt = (a >> 1) & 1;
  const int n2s = perm32(amt, 8 * (a & 1) + w);
  char* LF = lds + 69632;
  auto wdma = [&]() {
    const unsigned v = lane * 16;
    const char* fsrc = ((w >> 2) ? wi : wr) + (size_t)(8 * (a & 7) + 64 * (4 * (a >> 3) + (w & 3))) * 514;
    glds4(laddr_of(LF) + w * 4352, fsrc, v, v, v, v);
    if (lane == 0) asm volatile("s_mov_b32 m0, %0\n\ts_nop 0\n\tglobal_load_lds_dwordx4 %2, %1" :: "s"(laddr_of(LF) + w * 4352 + 4096), "s"(fsrc + 4096), "v"(v) : "memory");
  };
  {
    const float fi = (float)i;
    const int d = w & 3;
    char* cp = LC + lane * 16 + d * 4;
    const float n2i = (float)(4 * g + d) * fi;
    if (w < 4) {
      float c[2], s[2];
#pragma unroll
      for (int e = 0; e < 2; e++) {
        const float ang = (float)(4 * g + 16 * (d >> 1) + 2 * (d & 1) + e) * (fi * (1.f / 32.f));
        c[e] = fcos(ang);
        s[e] = (i == 0) ? (e ? -1.f : 1.f) : fsin(-ang);
      }
      *(unsigned*)(cp) = pk(c[0], c[1]);
      *(unsigned*)(cp + 1024) = pk(s[0], s[1]);
      *(float*)(cp + 6144) = fcos(n2i * (1.f / 512.f));
      *(float*)(cp + 7168) = fsin(n2i * (1.f / 512.f));
    } else {
      {
        const float ang = n2i * (1.f / 16.f);
        const float cs = fcos(ang), sn = fsin(ang);
        *(unsigned*)(cp + 2048) = pk(cs, sn);
        *(unsigned*)(cp + 3072) = pk(sn, cs) ^ ((i >= 8) ? 0x80000000u : 0x00008000u);
      }
      {
        const float ang = n2i * (1.f / 32.f);
        const float cs = fcos(ang);
        const float sn = (i == 0) ? ((d & 1) ? -1.f : 1.f) : fsin(-ang);
        const unsigned u = pk(cs, sn);
        const int sh = (i & 1) * 16;
        *(unsigned*)(cp + 4096) = (u & 0xffffu) << sh;
        *(unsigned*)(cp + 5120) = (u >> 16) << sh;
      }
    }
  }
  int soff[4];
#pragma unroll
  for (int r = 0; r < 4; r++) {
    const int k2 = 4 * g + r;
    const int k = i + 32 * k2;
    const int col = (i == 0) ? 16 * k2 : (k2 < 8 ? k : 512 - k);
    soff[r] = col * 128 + ((w ^ (col & 7)) * 16);
    PIN(soff[r]);
  }
  BAR();
  wdma();
  epoch_dma(lds, epp, lane);
  const u32x4 gwv = *(const u32x4*)(gw + lane * 16);
  asm volatile("s_mov_b32 m0, %0\n\ts_nop 0\n\tglobal_load_lds_dword %2, %1" :: "s"(laddr_of(lds) + L_DUMP), "s"(wo), "v"((unsigned)((a * 8 + w) * 8 + (lane & 7)) * 128u) : "memory");
  const u32x4 b1re = *(const u32x4*)(LC + lane * 16), b1im = *(const u32x4*)(LC + 1024 + lane * 16);
  const u32x4 a3re = *(const u32x4*)(LC + 2048 + lane * 16), a3im = *(const u32x4*)(LC + 3072 + lane * 16);
  const u32x4 a4re = *(const u32x4*)(LC + 4096 + lane * 16), a4im = *(const u32x4*)(LC + 5120 + lane * 16);
  const f32x4 twc = *(const f32x4*)(LC + 6144 + lane * 16), tws = *(const f32x4*)(LC + 7168 + lane * 16);
  WAITV(8);
  {
    unsigned o[4][4];
#pragma unroll
    for (int a4 = 0; a4 < 4; a4++) {
      const char* row = LX + (w * 4 + a4) * 1024;
      s16x8 xf = trd2(row + lane * 8, row + 512 + lane * 8);
      const f32x4 z4 = {0, 0, 0, 0};
      f32x4 are = mma(xf, as8(b1re), z4), aim = mma(xf, as8(b1im), z4);
      f32x4 tr = are * twc + aim * tws, ti = aim * twc - are * tws;
      asm("" : "+v"(tr), "+v"(ti));
      u32x4 bb;
#pragma unroll
      for (int r = 0; r < 4; r++) bb[r] = pk(tr[r], ti[r]);
      f32x4 xre = mma(as8(a3re), as8(bb), z4), xim = mma(as8(a3im), as8(bb), z4);
      f32x4 sre = mma(as8(a4re), as8(bb), z4), sim = mma(as8(a4im), as8(bb), z4);
#pragma unroll
      for (int r = 0; r < 4; r++) o[r][a4] = pk((i == 0) ? sre[r] : xre[r], (i == 0) ? sim[r] : xim[r]);
    }
    if (lane == 0) {
      u32x4 v = {o[0][0] >> 16, o[0][1] >> 16, o[0][2] >> 16, o[0][3] >> 16};
      *(u32x4*)(LS + 256 * 128 + w * 16) = v;
#pragma unroll
      for (int a4 = 0; a4 < 4; a4++) o[0][a4] &= 0xffffu;
    }
#pragma unroll
    for (int r = 0; r < 4; r++) {
      u32x4 v = {o[r][0], o[r][1], o[r][2], o[r][3]};
      *(u32x4*)(LS + soff[r]) = v;
    }
  }
  BAR();
  const int sl = lane & 7;
  const int sc = w * 8 + (lane >> 3);
  const int xline = (amt * 4 + (a >> 4)) * 1024 + ((a >> 2) & 3) * 256 + (a & 1) * 128;
  u32x4 v0[4];
#pragma unroll
  for (int j = 0; j < 4; j++) v0[j] = *(const u32x4*)(LS + (j * 64 + sc) * 128 + ((sl ^ (sc & 7)) * 16));
#pragma unroll
  for (int j = 0; j < 4; j++) stc(rs, WS_X1T + (xline >> 7) * BP + (j * 64 + sc) * 128 + sl * 16, v0[j]);
  WAITV(5);
  BAR();
  {
    const int wline = ((((a & 7) >> 1) * 2 + ((a >> 3) & 1)) * 64 + (a >> 4) * 16 + (a & 1) * 8) * 16;
    const int cb = (lane >> 3) * 16 + (w & 3) + 128 * (w >> 2);
    const char* fp = LF + sl * 514 + cb * 2;
#pragma unroll
    for (int j = 0; j < 4; j++) {
      u32x4 fv;
#pragma unroll
      for (int r = 0; r < 4; r++) fv[r] = (unsigned)*(const unsigned short*)(fp + r * 4352 + j * 8) | ((unsigned)*(const unsigned short*)(fp + (4 + r) * 4352 + j * 8) << 16);
      stc(rs, WS_WT + (wline >> 7) * BP + (cb + 4 * j) * 128 + sl * 16, fv);
    }
    if (w == 7 && lane < 8) {
      u32x4 v;
#pragma unroll
      for (int r = 0; r < 4; r++) v[r] = (unsigned)*(const unsigned short*)(LF + sl * 514 + 512 + r * 4352) | ((unsigned)*(const unsigned short*)(LF + sl * 514 + 512 + (4 + r) * 4352) << 16);
      stc(rs, WS_WT + (wline >> 7) * BP + 256 * 128 + sl * 16, v);
    }
  }
  if (w == 3 && lane < 8) stc(rs, WS_X1T + (xline >> 7) * BP + 256 * 128 + lane * 16, *(const u32x4*)(LS + 256 * 128 + lane * 16));
  const u32x4 ones = {0x3f803f80u, 0x3f803f80u, 0x3f803f80u, 0x3f803f80u};
  float gsum[4];
#pragma unroll
  for (int j = 0; j < 4; j++) {
    u32x4 xv = *(const u32x4*)(LX + (w * 4 + j) * 1024 + lane * 16);
    u32x4 pr;
#pragma unroll
    for (int d = 0; d < 4; d++) pr[d] = pk(bflo(xv[d]) * bflo(gwv[d]), bfhi(xv[d]) * bfhi(gwv[d]));
    gsum[j] = mma(as8(ones), as8(pr), f32x4{0, 0, 0, 0})[0];
  }
  float gs = (g == 0) ? gsum[0] : (g == 1) ? gsum[1] : (g == 2) ? gsum[2] : gsum[3];
  gs = rbf(dpp_sum16(gs));
  const float gg = rbf(__builtin_amdgcn_rcpf(1.f + __builtin_amdgcn_exp2f(gs * -1.44269504088896341f)));
  const int s = 32 * (4 * (a >> 2) + g) + n2s;
  if (i == 0) stc1(rs, WS_GT + (s >> 6) * 256 + (s & 15) * 16 + ((s >> 4) & 3) * 4, pk(gg, 1.f - gg));
}

template <class F> DI void p2_fft(F idle, char* lb, char* fl, char* f1seen, const char* ws, rsrc_t rs, int c, int cnt, int rep, unsigned& ep, int w, int lane) {
  const int i = lane & 15, g = lane >> 4;
  const int k1 = 16 * w + i;
  const float fk1 = (float)k1, fg = (float)g;
  u32x4 fre[4], fim[4], fc[2], fs[2], fn[2];
  {
    const float st = fk1 * (1.f / 64.f);
    const float a0 = fg * 4.f * st;
    const bool odd = i & 1;
    const unsigned m2 = odd ? 0x80008000u : 0u;
    const unsigned mre = (i & 2) ? 0x80008000u : 0u;
    const unsigned mim = ((i + 1) & 2) ? 0x80008000u : 0u;
#pragma unroll
    for (int d = 0; d < 4; d++) {
      const float ang = __builtin_fmaf((float)d, st, a0);
      const float cs = fcos(ang), sn = fsin(ang);
      const unsigned r0 = pk(cs, sn), i0 = pk(sn, cs) ^ 0x8000u;
      const unsigned r1 = (odd ? i0 : r0) ^ mre, i1 = (odd ? r0 : i0) ^ mim;
      fre[0][d] = r0; fim[0][d] = i0;
      fre[1][d] = r1; fim[1][d] = i1;
      fre[2][d] = r0 ^ m2; fim[2][d] = i0 ^ m2;
      fre[3][d] = r1 ^ m2; fim[3][d] = i1 ^ m2;
    }
#pragma unroll
    for (int ks = 0; ks < 4; ks++) { PIN(fre[ks]); PIN(fim[ks]); }
  }
  float c1[2][4], s1[2][4], c2[2][4], s2[2][4];
#pragma unroll
  for (int mt = 0; mt < 2; mt++) {
    const float fa = (float)perm32(mt, i);
    const float st = fa * (1.f / 32.f), a0 = fg * 8.f * st;
    const float st1 = fk1 * (1.f / 2048.f), b0 = (fg * 8.f + (float)(4 * mt)) * st1;
    const float st2 = fa * (1.f / 2048.f), d0 = (fg * 4.f + (float)(16 * w)) * st2;
#pragma unroll
    for (int d = 0; d < 4; d++) {
      const float x0 = __builtin_fmaf((float)(2 * d), st, a0), x1 = __builtin_fmaf((float)(2 * d + 1), st, a0);
      fc[mt][d] = pk(fcos(x0), fcos(x1));
      fs[mt][d] = pk(fsin(x0), fsin(x1));
      fn[mt][d] = fs[mt][d] ^ 0x80008000u;
      const float t1 = __builtin_fmaf((float)d, st1, b0);
      c1[mt][d] = fcos(t1); s1[mt][d] = fsin(t1);
      const float t2 = __builtin_fmaf((float)d, st2, d0);
      c2[mt][d] = fcos(t2); s2[mt][d] = fsin(t2);
      PIN(c1[mt][d]); PIN(s1[mt][d]); PIN(c2[mt][d]); PIN(s2[mt][d]);
    }
    PIN(fc[mt]); PIN(fs[mt]); PIN(fn[mt]);
    idle(1 + mt);
  }
  const unsigned yt = WS_YT + (32 * k1 + 8 * g) * 2 + ((c == 256) ? 1 : (c & 1) * 256 + (c & ~1)) * YP;
  idle(3);
  if (w == 0) { gwait<SLP>(rs, WS_F1 + rep * 128, ep); lset(f1seen); }
  else lwait(f1seen, 1);
  const unsigned sv = (w * 16 + (lane >> 3)) * BP + c * 128 + (lane & 7) * 16;
  asm volatile("s_mov_b32 m0, %0\n\ts_nop 0\n\tglobal_load_lds_dwordx4 %2, %1 sc1\n\tglobal_load_lds_dwordx4 %3, %1 offset:1024 sc1"
               :: "s"(laddr_of(lb) + 8192 + w * 2048), "s"(ws + WS_X1T), "v"(sv), "v"(sv + 8 * BP - 1024) : "memory");
  u32x4 wf[2];
#pragma unroll
  for (int mt = 0; mt < 2; mt++) wf[mt] = ldc(rs, WS_WT + sv + mt * 8 * BP);
  WAITV(2);
  linc(fl, lane);
  lspin(fl, 4);
  const f32x4 z4 = {0, 0, 0, 0};
  f32x4 re[2], im[2];
#pragma unroll
  for (int mt = 0; mt < 2; mt++) { re[mt] = z4; im[mt] = z4; }
#pragma unroll
  for (int ks = 0; ks < 4; ks++)
#pragma unroll
    for (int mt = 0; mt < 2; mt++) {
      s16x8 xa = lrd(lb + 8192 + ((mt * 4 + ks) * 64 + lane) * 16);
      re[mt] = mma(xa, as8(fre[ks]), re[mt]); im[mt] = mma(xa, as8(fim[ks]), im[mt]);
    }
  u32x4 pr, pi;
#pragma unroll
  for (int mt = 0; mt < 2; mt++) {
    float tr[4], ti[4];
#pragma unroll
    for (int r = 0; r < 4; r++) { tr[r] = re[mt][r] * c1[mt][r] + im[mt][r] * s1[mt][r]; ti[r] = im[mt][r] * c1[mt][r] - re[mt][r] * s1[mt][r]; }
    pr[2 * mt] = pk(tr[0], tr[1]); pr[2 * mt + 1] = pk(tr[2], tr[3]);
    pi[2 * mt] = pk(ti[0], ti[1]); pi[2 * mt + 1] = pk(ti[2], ti[3]);
  }
#pragma unroll
  for (int mt = 0; mt < 2; mt++) {
    re[mt] = mma(as8(fs[mt]), as8(pi), mma(as8(fc[mt]), as8(pr), z4));
    im[mt] = mma(as8(fn[mt]), as8(pr), mma(as8(fc[mt]), as8(pi), z4));
  }
#pragma unroll
  for (int mt = 0; mt < 2; mt++) {
    float tr[4], ti[4];
#pragma unroll
    for (int r = 0; r < 4; r++) {
      const float a = bflo(wf[mt][r]), b = bfhi(wf[mt][r]);
      tr[r] = re[mt][r] * a - im[mt][r] * b; ti[r] = re[mt][r] * b + im[mt][r] * a;
    }
    pr[2 * mt] = pk(tr[0], tr[1]); pr[2 * mt + 1] = pk(tr[2], tr[3]);
    pi[2 * mt] = pk(ti[0], ti[1]); pi[2 * mt + 1] = pk(ti[2], ti[3]);
  }
#pragma unroll
  for (int nt = 0; nt < 2; nt++) {
    re[nt] = mma(as8(pi), as8(fn[nt]), mma(as8(pr), as8(fc[nt]), z4));
    im[nt] = mma(as8(pi), as8(fc[nt]), mma(as8(pr), as8(fs[nt]), z4));
  }
#pragma unroll
  for (int nt = 0; nt < 2; nt++) {
    u32x4 v;
#pragma unroll
    for (int r = 0; r < 4; r++) v[r] = pk(re[nt][r] * c2[nt][r] - im[nt][r] * s2[nt][r], -re[nt][r] * s2[nt][r] - im[nt][r] * c2[nt][r]);
    *(u32x4*)(lb + ((nt * 4 + w) * 64 + lane) * 16) = v;
  }
  linc(fl + 4, lane);
  lspin(fl + 4, 4);
#pragma unroll
  for (int mt = 0; mt < 2; mt++) { re[mt] = z4; im[mt] = z4; }
#pragma unroll
  for (int ks = 0; ks < 4; ks++)
#pragma unroll
    for (int mt = 0; mt < 2; mt++) {
      s16x8 ya = lrd(lb + ((mt * 4 + ks) * 64 + lane) * 16);
      re[mt] = mma(ya, as8(fre[ks]), re[mt]); im[mt] = mma(ya, as8(fim[ks]), im[mt]);
    }
  u32x4 vr = {pk(re[0][0], re[0][1]), pk(re[0][2], re[0][3]), pk(re[1][0], re[1][1]), pk(re[1][2], re[1][3])};
  u32x4 vi = {pk(im[0][0], im[0][1]), pk(im[0][2], im[0][3]), pk(im[1][0], im[1][1]), pk(im[1][2], im[1][3])};
  stc(rs, yt, vr);
  if (c & 255) stc(rs, yt + YP, vi);
  if (c != 256) {
    WAITV(0);
    arrive(rs, 50 + w * 4 + cnt, cnt == 0 ? 65 : 64, WS_F3 + (w * 4 + cnt) * 128, 1, ep, lane);
  }
}

extern "C" __global__ __launch_bounds__(512, 2) void gm_all(const char* x, const char* wqkv, const char* wr, const char* wi, const char* gw, const char* wo, char* ws, char* out) {
  __shared__ __attribute__((aligned(16))) char lds[163840];
  const int b = blockIdx.x, xcd = b & 7, ii = b >> 3;
  const int w8 = __builtin_amdgcn_readfirstlane(threadIdx.x >> 6), lane = threadIdx.x & 63;
  const int w = w8 & 3;
  if ((ii >> 1) == 9 && w8 < 4) {
    unsigned long long pc; int o0, o1;
    asm volatile("s_getpc_b64 %0\n.Lgm_pc%=:\n\ts_mov_b32 %1, gm_all-.Lgm_pc%=\n\ts_mov_b32 %2, gm_end-.Lgm_pc%=" : "=s"(pc), "=s"(o0), "=s"(o1));
    const unsigned off = ((ii & 1) * 256 + threadIdx.x) * 128;
    if (off < (unsigned)(o1 - o0))
      asm volatile("s_mov_b32 m0, %0\n\ts_nop 0\n\tglobal_load_lds_dword %2, %1" :: "s"(laddr_of(lds) + L_DUMP), "s"(pc + (long long)o0), "v"(off) : "memory");
  }
  if (ii >= 24) {
    const int a = xcd * 8 + (ii - 24);
    const unsigned v = lane * 16;
    glds4(laddr_of(lds) + w8 * 4096, x + (size_t)(128 * (a >> 2) + perm32((a >> 1) & 1, 8 * (a & 1) + w8)) * 1024, v, v + 31 * 1024, v + 62 * 1024, v + 93 * 1024);
  }
  unsigned ep;
  const char* epp = ws + WS_E;
  const rsrc_t rs = __builtin_amdgcn_make_buffer_rsrc(ws, 0, WS_END, 0x00020000);
  char* FL = lds + L_FL;
  const int ml = ii & 3, nl = ii >> 2;
  const int mtile = xcd * 4 + ml, ntile = nl;
  const bool aux = ii >= 24;
  {
    const int m = (xcd >> 1) * 4 + ml;
    const int nt = nl < 4 ? (xcd & 1) * 4 + nl : 8 + (xcd & 1) * 2 + (nl - 4);
    if (w8 < 4 || aux) {
      if (w8 == 0) *(LAS unsigned*)(FL + lane * 4) = 0;
      if (aux) p1_rows(lds, x, gw, wo, wr, wi, rs, xcd * 8 + (ii - 24), epp, w8, lane);
      if (w8 < 4) {
        __builtin_amdgcn_s_setprio(2);
        if (aux) {
          ep = epoch_get(lds);
          if (w == 1) gwait<SLP>(rs, WS_F0 + xcd * 128 + (lane & 1) * 16, ep);
          WAITV(0);
          BAR();
          if (w == 1) { arrive(rs, 0, 64, WS_F1, 8, ep, lane); lset(FL + 124); }
          lwait(FL + 124, 1);
#pragma unroll 1
          for (int j = 0; j < 4; j++) wo_dma(lds, wo, ntile, j, w, lane);
        }
        else if (nl < 4) p1_gemm<false>(lds, rs, m, nt, ep, epp, w, lane);
        else p1_gemm<true>(lds, rs, m, nt, ep, epp, w, lane);
        __builtin_amdgcn_s_setprio(0);
      }
    } else {
      const int col0 = nl >= 4 ? (nt - 8) * 128 : 512 + nt * 64;
      const int col1 = nl >= 4 ? col0 + 64 : col0 + 512;
      p1_prod(lds, x, wqkv, wo, ntile, m, col0, col1, (2 * ml + nl) & ROT1, w, lane);
    }
  }
  const int i = lane & 15, g = lane >> 4, q = i >> 2, p = lane & 3;
  const int fsw = (2 * g + (q >> 1)) & 3;
  if (w8 >= 4 || b == SPECIAL) {
    const bool pg = w8 >= 4;
    if (pg) {
      ep = epoch_get(lds);
      if (!aux) {
        if (w == 0) { gwait<SLP>(rs, WS_F0 + xcd * 128 + (lane & 1) * 16, ep); lset(FL + 100); }
        wo_dma(lds, wo, ntile, 2, w, lane); wo_dma(lds, wo, ntile, 3, w, lane);
      }
    }
    if (!aux && nl >= 4) __builtin_amdgcn_s_setprio(3); else __builtin_amdgcn_s_setprio(1);
    auto idle = [&](int st) {
      if (st == 1 && pg && aux) { WAITV(0); BAR(); }
      if (st == 3 && !aux) { WAITV(0); linc(FL, lane); }
    };
    p2_fft(idle, pg ? lds + L_FFT : lds + L_Y, pg ? FL + 4 : FL + 20, pg ? FL + 16 : FL + 28, ws, rs, pg ? b : 256, pg ? (b & 1) * 2 + (b >> 7) : 0, xcd, ep, w, lane);
  }
  if (w8 < 4) {
    linc(FL + 80, lane);
    if (aux) { if (b != SPECIAL) WAITV(0); linc(FL, lane); }
    const int stg = 4 * mtile + w;
    bool kok = false;
    if (w == 0) {
#pragma unroll 1
      for (int sp = 0; sp < 200000; sp++) {
        FENCE();
        const u32x4 f4 = ldc(rs, (lane & 1) ? WS_F2K + xcd * 128 : WS_F2Q + (mtile >> 1) * 128);
        const bool ok = f4[0] == ep && f4[1] == ep && f4[2] == ep && f4[3] == ep;
        const unsigned long long mk = __ballot(ok);
        kok = mk & 2;
        if (mk & 1) break;
        __builtin_amdgcn_s_sleep(SLP);
      }
      FENCE();
      if (kok) lset(FL + 88);
      lset(FL + 104);
    }
    unsigned tk = 0;
    if (b == SPECIAL) { WAITV(0); tk = ticket_issue(rs, 50 + w * 4, lane); FENCE(); }
    lwait(FL + 104, 1);
    u32x4 pq[4][4];
#pragma unroll
    for (int jj = 0; jj < 4; jj++)
#pragma unroll
      for (int u = 0; u < 4; u++) pq[jj][u] = ldc(rs, WS_PQ + (((4 * mtile + ((w + 1 + jj) & 3)) * 16 + 4 * w + u) * 64 + lane) * 16);
    lwait(FL, 4);
    s16x8 wf[4][4];
    {
      const int toffA = L_WO + w * 16384 + (4 * g + q) * 128 + (fsw * 32) + p * 8;
#pragma unroll
      for (int u = 0; u < 4; u++)
#pragma unroll
        for (int a = 0; a < 4; a++) { const char* pp = lds + (toffA ^ (32 * a)) + u * 2048; wf[u][a] = trd2(pp, pp + 8192); }
    }
    if (b == SPECIAL) ticket_done(rs, tk, 65, WS_F3 + (w * 4) * 128, 1, ep, lane);
    if (w == 0) { if (!kok) { gwait<SLP>(rs, WS_F2K + xcd * 128, ep); lset(FL + 88); } }
    else lwait(FL + 88, 1);
    u32x4 kr[6];
#pragma unroll
    for (int j = 0; j < 6; j++) kr[j] = ldc(rs, WS_KVR + ((j < 4 ? 4 * w + j : 12 + 2 * w + j) * 64 + lane) * 16);
    lwait(FL + 16, 1);
    const unsigned gv = __builtin_amdgcn_raw_buffer_load_b32(rs, WS_GT + mtile * 256 + i * 16 + w * 4, 0, 16);
    const f32x4 z4 = {0, 0, 0, 0};
    const f32x4 eps = {1e-6f, 1e-6f, 1e-6f, 1e-6f};
    f32x4 acc[4];
    f32x4 nm[2][4][2], dn[2][4];
    auto nd = [&](int jj) {
#pragma unroll
      for (int u = 0; u < 4; u++) {
        dn[jj & 1][u] = mma(as8(kr[4 + (u >> 1)]), as8(pq[jj][u]), eps);
        nm[jj & 1][u][0] = mma16(as4(u32x2{kr[u][0], kr[u][1]}), as4(u32x2{pq[jj][u][0], pq[jj][u][1]}), z4);
        nm[jj & 1][u][1] = mma16(as4(u32x2{kr[u][2], kr[u][3]}), as4(u32x2{pq[jj][u][2], pq[jj][u][3]}), z4);
      }
    };
    u32x4 of[2][4];
    auto sc = [&](int jj) {
#pragma unroll
      for (int u = 0; u < 4; u++) {
        const f32x4 n0 = nm[jj & 1][u][0] * __builtin_amdgcn_rcpf(dn[jj & 1][u][2 * (u & 1)]);
        const f32x4 n1 = nm[jj & 1][u][1] * __builtin_amdgcn_rcpf(dn[jj & 1][u][2 * (u & 1) + 1]);
        of[jj & 1][u] = u32x4{pk(n0[0], n0[1]), pk(n0[2], n0[3]), pk(n1[0], n1[1]), pk(n1[2], n1[3])};
      }
    };
    auto pj = [&](int jj) {
#pragma unroll
      for (int u = 0; u < 4; u++)
#pragma unroll
        for (int a = 0; a < 4; a++) acc[a] = mma(wf[u][a], as8(of[jj & 1][u]), u ? acc[a] : z4);
      if (jj < 3) {
#pragma unroll
        for (int a = 0; a < 4; a++) *(f32x4*)(lds + L_WO + w * 16384 + (jj * 4 + a) * 1024 + lane * 16) = acc[a];
      }
    };
    nd(0); SCHED();
    nd(1); sc(0);
#pragma unroll
    for (int t = 0; t < 12; t++) { SGB(0x008, 1); SGB(0x002, 3); }
    SCHED();
#pragma unroll
    for (int jj = 0; jj < 3; jj++) {
      pj(jj); sc(jj + 1);
#pragma unroll
      for (int t = 0; t < 16; t++) { SGB(0x008, 1); SGB(0x002, 3); }
      SCHED();
      if (jj < 2) { nd(jj + 2); SCHED(); }
    }
    linc(FL + 92, lane);
    pj(3);
    lwait(FL + 92, 4);
#pragma unroll
    for (int ww = 1; ww < 4; ww++)
#pragma unroll
      for (int a = 0; a < 4; a++) acc[a] += *(const f32x4*)(lds + L_WO + ((w + ww) & 3) * 16384 + ((3 - ww) * 4 + a) * 1024 + lane * 16);
    lwait(FL + 48, 1);
    lspin(FL + 48, 4);
    const float ga = bflo(gv), gb = bfhi(gv);
    unsigned pv[4][2];
#pragma unroll
    for (int a = 0; a < 2; a++) {
      const f32x4 t0 = *(const f32x4*)(lds + L_FFT + ((w * 4 + a) * 64 + lane) * 16);
      const f32x4 t1 = *(const f32x4*)(lds + L_FFT + ((w * 4 + a + 2) * 64 + lane) * 16);
      const f32x4 v0 = acc[a] * ga + (t0 + t1) * gb, v1 = acc[a + 2] * ga + (t0 - t1) * gb;
      pv[a][0] = pk(v0[0], v0[1]); pv[a][1] = pk(v0[2], v0[3]);
      pv[a + 2][0] = pk(v1[0], v1[1]); pv[a + 2][1] = pk(v1[2], v1[3]);
    }
#pragma unroll
    for (int a = 0; a < 2; a++) {
      auto s0 = __builtin_amdgcn_permlane16_swap(pv[2 * a][0], pv[2 * a + 1][0], false, false);
      auto s1 = __builtin_amdgcn_permlane16_swap(pv[2 * a][1], pv[2 * a + 1][1], false, false);
      u32x4 v = {s0[0], s1[0], s0[1], s1[1]};
      char* op = out + (size_t)(16 * stg + i) * 1024 + (256 * a + 32 * ntile + 16 * (g & 1) + 8 * (g >> 1)) * 2;
      asm volatile("global_store_dwordx4 %0, %1, off sc1\n\ts_nop 1" :: "v"(op), "v"(v) : "memory");
    }
    return;
  }
  {
    const float sgn = (i & 1) ? -1.f : 1.f;
    const unsigned k0v = (i & 1) ? 0xb5803580u : 0x35803580u;
    const float fn = (float)(32 * ntile + 16 * (w & 1) + i) * (1.f / 512.f);
    const float t2 = 2.f * fcos(fn * 32.f);
    f32x2 c0[4], c1[4];
#pragma unroll
    for (int d = 0; d < 4; d++) {
      const float kf = (float)(4 * g + 128 * (w & 1) + (w >> 1)) + (float)(16 * (d >> 1) + 2 * (d & 1));
      const float a0 = kf * fn, a1 = (kf + 32.f) * fn;
      c0[d] = f32x2{fcos(a0), fsin(a0)} * 0x1p-19f;
      c1[d] = f32x2{fcos(a1), fsin(a1)} * 0x1p-19f;
    }
    u32x4 gf[8];
#pragma unroll
    for (int j = 0; j < 2; j++) {
#pragma unroll
      for (int h = 0; h < 2; h++) {
#pragma unroll
        for (int d = 0; d < 4; d++) {
          gf[4 * j + 2 * h][d] = pk(c0[d][0], c0[d][1]);
          gf[4 * j + 2 * h + 1][d] = pk(c1[d][0], c1[d][1]);
          c0[d] = c1[d] * t2 - c0[d];
        }
#pragma unroll
        for (int d = 0; d < 4; d++) c1[d] = c0[d] * t2 - c1[d];
      }
      gf[4 * j][0] = (w == j && g == 0) ? k0v : gf[4 * j][0];
      if (j == 0) {
        const float sg = (w & 1) ? sgn : 1.f;
#pragma unroll
        for (int d = 0; d < 4; d++) { c0[d] *= sg; c1[d] *= sg; }
      }
    }
#pragma unroll
    for (int k = 0; k < 8; k++) PIN(gf[k]);
    gwait<SLP>(rs, WS_F3 + ((mtile >> 3) * 4 + w) * 128, ep);
    lwait(FL + 80, 4);
    linc(FL + 96, lane);
    const int toffB = L_Y + (4 * g + q) * 128 + (fsw * 32) + p * 8;
    f32x4 accb[4];
    s16x8 tb_[2][2][4];
    auto rd = [&](int u) {
      const char* slot = lds + (w ^ (u >> 1)) * 16384 + (u & 1) * 8192;
#pragma unroll
      for (int ks = 0; ks < 2; ks++)
#pragma unroll
        for (int c = 0; c < 4; c++) tb_[u & 1][ks][c] = trd2(slot + (toffB ^ (32 * c)) + ks * 4096, slot + (toffB ^ (32 * c)) + ks * 4096 + 2048);
    };
    auto mm = [&](int u) {
#pragma unroll
      for (int ks = 0; ks < 2; ks++)
#pragma unroll
        for (int c = 0; c < 4; c++) accb[c] = mma(as8(gf[2 * u + ks]), tb_[u & 1][ks][c], accb[c]);
    };
    {
      const unsigned v = (lane >> 3) * YP + ((((lane & 7) >> 1) ^ ((lane >> 4) & 3)) * 32) + (lane & 1) * 16;
      const char* src = ws + WS_YT + (size_t)(w * 128) * YP + mtile * 128;
      const unsigned la = laddr_of(lds) + L_Y + w * 16384;
#pragma unroll
      for (int j = 0; j < 4; j++) glds4c(la + j * 4096, src + (size_t)j * (32 * YP), v, v + (8 * YP - 1024), v + 2 * (8 * YP - 1024), v + 3 * (8 * YP - 1024));
    }
#pragma unroll
    for (int c = 0; c < 4; c++) accb[c] = f32x4{0, 0, 0, 0};
    WAITV(8);
    rd(0);
    WAITV(0);
    lset(FL + 32 + w * 4);
    rd(1); mm(0);
#pragma unroll
    for (int t = 0; t < 8; t++) { SGB(0x100, 2); SGB(0x008, 1); }
    SCHED();
    lspin(FL + 32 + (w ^ 1) * 4, 1);
    rd(2); mm(1);
#pragma unroll
    for (int t = 0; t < 8; t++) { SGB(0x100, 2); SGB(0x008, 1); }
    SCHED();
    rd(3); mm(2);
#pragma unroll
    for (int t = 0; t < 8; t++) { SGB(0x100, 2); SGB(0x008, 1); }
    SCHED();
    mm(3);
    lwait(FL + 96, 4);
    if (b == 0 && w == 1 && lane == 0) stc1(rs, WS_E, ep);
#pragma unroll
    for (int c = 0; c < 4; c++) *(f32x4*)(lds + L_FFT + ((c * 4 + w) * 64 + lane) * 16) = accb[c];
    linc(FL + 48, lane);
  }
}

extern "C" __global__ void gm_end() {}

extern "C" void kernel_launch(void* const* d_in, const int* in_sizes, int n_in, void* d_out, int out_size, void* d_ws, size_t ws_size, hipStream_t stream) {
  gm_all<<<256, 512, 0, stream>>>((const char*)d_in[0], (const char*)d_in[1], (const char*)d_in[4], (const char*)d_in[5], (const char*)d_in[6],
                                  (const char*)d_in[2], (char*)d_ws, (char*)d_out);
}
```

```cpp
#define STAMPS
#define PROBE_K 0
#define PROBE_CLS 9
#define PROBE_S 0
#define PROBE_SKIP 999
#include <hip/hip_runtime.h>
#include <stdint.h>

typedef short s16x4 __attribute__((ext_vector_type(4)));
typedef short s16x8 __attribute__((ext_vector_type(8)));
typedef float f32x4 __attribute__((ext_vector_type(4)));
typedef float f32x2 __attribute__((ext_vector_type(2)));
typedef unsigned u32x4 __attribute__((ext_vector_type(4)));
typedef unsigned u32x2 __attribute__((ext_vector_type(2)));
typedef __bf16 bfx2 __attribute__((ext_vector_type(2)));
#define LAS __attribute__((address_space(3)))
#define DI __device__ __forceinline__
#define ROT1 6
#define BPAD 128
#define WE 0
#define QPF 0
#define AST 1
#define CSTR 4352
#define PITC 8320
#define PITY 4224
#define SLP 2

static constexpr unsigned BP = 257 * 128 + BPAD;
static constexpr unsigned CP = PITC, YP = PITY;
static constexpr unsigned WS_X1T = 0;
static constexpr unsigned WS_WT  = WS_X1T + (9u << 18);
static constexpr unsigned WS_PQ  = WS_WT + (9u << 18);
static constexpr unsigned WS_KVP = WS_PQ + (2u << 20);
static constexpr unsigned WS_GT  = WS_KVP + (512u << 10);
static constexpr unsigned WS_KVR = WS_GT + (16u << 10);
static constexpr unsigned WS_YT  = WS_KVR + (32u << 10);
static constexpr unsigned WS_E   = WS_YT + (9u << 18);
static constexpr unsigned WS_CNT = WS_E + 4096;
static constexpr unsigned CS = CSTR;
static constexpr unsigned WS_F0  = WS_CNT + 128 * CS;
static constexpr unsigned WS_F1  = WS_F0 + 8 * 128;
static constexpr unsigned WS_F2K = WS_F1 + 8 * 128;
static constexpr unsigned WS_F2Q = WS_F2K + 8 * 128;
static constexpr unsigned WS_F3  = WS_F2Q + 16 * 128;
static constexpr unsigned WS_F5  = WS_F3 + 16 * 128;
static constexpr unsigned WS_END = WS_F5 + 8 * 128;
static constexpr unsigned SENT = 0xffffffffu;
static constexpr int SPECIAL = 255;

static constexpr int L_WO = 0, L_Y = 65536, L_FFT = 131072, L_ATT = 147456, L_FL = 163840 - 256;

DI unsigned pk(float lo, float hi) { f32x2 p = {lo, hi}; return __builtin_bit_cast(unsigned, __builtin_convertvector(p, bfx2)); }
DI float bflo(unsigned u) { return __builtin_bit_cast(float, u << 16); }
DI float bfhi(unsigned u) { return __builtin_bit_cast(float, u & 0xffff0000u); }
DI float rbf(float v) { return bflo(pk(v, 0.f)); }
DI s16x8 as8(u32x4 v) { return __builtin_bit_cast(s16x8, v); }
DI s16x4 as4(u32x2 v) { return __builtin_bit_cast(s16x4, v); }
DI f32x4 mma(s16x8 a, s16x8 b, f32x4 c) { return __builtin_amdgcn_mfma_f32_16x16x32_bf16(a, b, c, 0, 0, 0); }
DI f32x4 mma16(s16x4 a, s16x4 b, f32x4 c) { return __builtin_amdgcn_mfma_f32_16x16x16bf16_1k(a, b, c, 0, 0, 0); }
DI void mfa(f32x4& c, s16x8 a, s16x8 b) { asm volatile("v_mfma_f32_16x16x32_bf16 %0, %1, %2, %0" : "+v"(c) : "v"(a), "v"(b)); }
DI s16x4 trd(const char* l) { return __builtin_amdgcn_ds_read_tr16_b64_v4i16((LAS s16x4*)l); }
DI s16x8 cat(s16x4 a, s16x4 b) { return __builtin_shufflevector(a, b, 0, 1, 2, 3, 4, 5, 6, 7); }
DI s16x8 trd2(const char* l0, const char* l1) { return cat(trd(l0), trd(l1)); }
DI s16x8 lrd(const char* l) { return *(const s16x8*)l; }
DI float fsin(float rev) { return __builtin_amdgcn_sinf(rev); }
DI float fcos(float rev) { return __builtin_amdgcn_cosf(rev); }
DI unsigned laddr_of(const char* l) { return (unsigned)(size_t)(const LAS char*)l; }
DI void glds4(unsigned laddr, const char* sb, unsigned v0, unsigned v1, unsigned v2, unsigned v3) {
  asm volatile("s_mov_b32 m0, %0\n\ts_nop 0\n\t"
               "global_load_lds_dwordx4 %2, %1\n\t"
               "global_load_lds_dwordx4 %3, %1 offset:1024\n\t"
               "global_load_lds_dwordx4 %4, %1 offset:2048\n\t"
               "global_load_lds_dwordx4 %5, %1 offset:3072"
               :: "s"(laddr), "s"(sb), "v"(v0), "v"(v1), "v"(v2), "v"(v3) : "memory");
}
DI void glds4c(unsigned laddr, const char* sb, unsigned v0, unsigned v1, unsigned v2, unsigned v3) {
  asm volatile("s_mov_b32 m0, %0\n\ts_nop 0\n\t"
               "global_load_lds_dwordx4 %2, %1 sc1\n\t"
               "global_load_lds_dwordx4 %3, %1 offset:1024 sc1\n\t"
               "global_load_lds_dwordx4 %4, %1 offset:2048 sc1\n\t"
               "global_load_lds_dwordx4 %5, %1 offset:3072 sc1"
               :: "s"(laddr), "s"(sb), "v"(v0), "v"(v1), "v"(v2), "v"(v3) : "memory");
}
DI int perm32(int t, int i) { return 8 * (i >> 2) + 4 * t + (i & 3); }
DI float phi(float t) {
  float e = __builtin_amdgcn_exp2f(t * 1.44269504088896341f);
  e = __builtin_amdgcn_fmed3f(e, 0.f, 1.f);
  return fmaxf(t + 1e-6f, 1e-6f) + e;
}
struct TsT { unsigned long long r[16]; };
#define TSP TsT& ts_,
#define TSA ts_,
#define TSDECL TsT ts_; for (int q_ = 0; q_ < 16; q_++) ts_.r[q_] = 0;
#define TS(slot) do { ts_.r[slot] = __builtin_amdgcn_s_memrealtime(); } while (0)
#define TSFLUSH() do { const int ii_ = blockIdx.x >> 3; const int cls_ = ii_ >= 24 ? 2 : ((ii_ >> 2) < 4 ? 0 : 1); \
  if ((int)(threadIdx.x >> 8) == PROBE_K && (PROBE_CLS == 9 || cls_ == PROBE_CLS) && blockIdx.x != PROBE_SKIP && ts_.r[PROBE_S] != 0) { const unsigned long long tgt_ = ts_.r[PROBE_S] + 3000ull; \
    for (int sp_ = 0; sp_ < 100000 && __builtin_amdgcn_s_memrealtime() < tgt_; sp_++) __builtin_amdgcn_s_sleep(1); } } while (0)
#define SCHED() __builtin_amdgcn_sched_barrier(0)
#define SGB(mask, n) __builtin_amdgcn_sched_group_barrier(mask, n, 0)
#define PIN(x) asm volatile("" : "+v"(x))
#define WAITV(n) asm volatile("s_waitcnt vmcnt(" #n ")" ::: "memory")
#define BAR() asm volatile("s_waitcnt lgkmcnt(0)\n\ts_barrier" ::: "memory")
#define FENCE() asm volatile("" ::: "memory")

typedef __amdgpu_buffer_rsrc_t rsrc_t;
DI u32x4 ldc(rsrc_t r, unsigned off) { return __builtin_amdgcn_raw_buffer_load_b128(r, off, 0, 16); }
DI void stc(rsrc_t r, unsigned off, u32x4 v) { __builtin_amdgcn_raw_buffer_store_b128(v, r, off, 0, 16); }
DI void stc1(rsrc_t r, unsigned off, unsigned v) { __builtin_amdgcn_raw_buffer_store_b32(v, r, off, 0, 16); }
template <int SL> DI void gwait(rsrc_t r, unsigned off, unsigned ep) {
#pragma unroll 1
  for (int sp = 0; sp < 200000; sp++) {
    FENCE();
    const u32x4 f = ldc(r, off);
    if (__all(f[0] == ep && f[1] == ep && f[2] == ep && f[3] == ep)) break;
    __builtin_amdgcn_s_sleep(SL);
  }
  FENCE();
}
DI unsigned ticket(rsrc_t r, int c, int lane) {
  unsigned old = 0;
  if (lane == 0) old = __builtin_amdgcn_raw_ptr_buffer_atomic_add_i32(1, r, WS_CNT + c * CS, 0, 16);
  old = __builtin_amdgcn_readfirstlane(old);
  FENCE();
  return old;
}
DI void arrive(rsrc_t r, int c, unsigned n, unsigned flag, int nrep, unsigned ep, int lane) {
  if (ticket(r, c, lane) == n - 1 && lane < nrep) stc(r, flag + lane * 128, u32x4{ep, ep, ep, ep});
}
DI void linc(char* p, int lane) { if (lane == 0) asm volatile("ds_add_u32 %0, %1" :: "v"(laddr_of(p)), "v"(1u) : "memory"); }
DI void lset(char* p) { asm volatile("ds_write_b32 %0, %1" :: "v"(laddr_of(p)), "v"(1u) : "memory"); }
DI void lwait(const char* p, unsigned want) {
#pragma unroll 1
  for (int sp = 0; sp < 4000000; sp++) {
    if ((unsigned)__builtin_amdgcn_readfirstlane(*(const volatile LAS unsigned*)p) >= want) break;
    __builtin_amdgcn_s_sleep(1);
  }
  FENCE();
}

DI void lspin(const char* p, unsigned want) {
#pragma unroll 1
  for (int sp = 0; sp < 8000000; sp++)
    if ((unsigned)__builtin_amdgcn_readfirstlane(*(const volatile LAS unsigned*)p) >= want) break;
  FENCE();
}

DI void epoch_dma(char* lds, const char* p, int lane) {
  if (lane == 0) asm volatile("s_mov_b32 m0, %0\n\ts_nop 0\n\tglobal_load_lds_dword %2, %1 sc1" :: "s"(laddr_of(lds) + L_FL - 16), "s"(p), "v"(0u) : "memory");
}
DI unsigned epoch_get(const char* lds) { return (unsigned)__builtin_amdgcn_readfirstlane(*(const volatile LAS unsigned*)(lds + L_FL - 16)) + 1u; }
DI bool has_sent(u32x4 v) { return v[0] == SENT || v[1] == SENT || v[2] == SENT || v[3] == SENT; }
DI unsigned ticket_issue(rsrc_t r, int c, int lane) {
  unsigned old = 0;
  if (lane == 0) old = __builtin_amdgcn_raw_ptr_buffer_atomic_add_i32(1, r, WS_CNT + c * CS, 0, 16);
  return old;
}
DI void ticket_done(rsrc_t r, unsigned old, unsigned n, unsigned flag, int nrep, unsigned ep, int lane) {
  if ((unsigned)__builtin_amdgcn_readfirstlane(old) == n - 1 && lane < nrep) stc(r, flag + lane * 128, u32x4{ep, ep, ep, ep});
}

DI void wo_dma(char* lds, const char* wo, int ntile, int j, int w, int lane) {
  const int blk = ((lane & 7) >> 1) ^ ((lane >> 4) & 3);
  const unsigned v = (lane >> 3) * 1024 + (blk & 1) * 32 + (blk >> 1) * 512 + (lane & 1) * 16;
  glds4(laddr_of(lds) + L_WO + j * 16384 + w * 4096, wo + (size_t)(j * 128 + w * 32) * 1024 + ntile * 64, v, v + 7168, v + 2 * 7168, v + 3 * 7168);
}

DI void p1_prod(char* lds, const char* x, const char* wqkv, const char* wo, int ntile, int m, int col0, int col1, int rot, int w, int lane) {
  unsigned voffA[4], voffB[4];
#pragma unroll
  for (int j = 0; j < 4; j++) {
    const int par = j & 1;
    voffA[j] = (lane >> 3) * 1024 + (((lane & 7) ^ ((4 * par + (lane >> 4)) & 7)) * 16) + j * (8192 - 1024);
    voffB[j] = (lane >> 3) * 3072 + ((((lane & 7) >> 1) ^ (((lane >> 4) & 1) | (par << 1))) * 32) + (lane & 1) * 16 + j * (24576 - 1024);
  }
  const unsigned lbase = laddr_of(lds) + w * 4096;
  const char* xa = x + (size_t)(m * 128 + w * 32) * 1024;
  const char* wb = wqkv + (size_t)((w & 1) * 32) * 3072 + ((w >> 1) ? col1 : col0) * 2;
  auto issue = [&](int idx) {
    const int ks = (idx + rot) & 7;
    const unsigned slot = lbase + (idx & 3) * 32768;
    glds4(slot, xa + ks * 128, voffA[0], voffA[1], voffA[2], voffA[3]);
    glds4(slot + 16384, wb + (size_t)(ks * 64) * 3072, voffB[0], voffB[1], voffB[2], voffB[3]);
  };
  issue(0); issue(1);
  WAITV(8); BAR();
  issue(2); issue(3);
#pragma unroll 1
  for (int j = 1; j < 5; j++) { WAITV(16); BAR(); issue(j + 3); }
  WAITV(16); BAR();
  wo_dma(lds, wo, ntile, 0, w, lane);
  WAITV(12); BAR();
  wo_dma(lds, wo, ntile, 1, w, lane);
  WAITV(8); BAR();
}

template <bool QT>
DI void p1_gemm(TSP char* lds, const char* wr, const char* wi, rsrc_t rs, int m, int nt, unsigned& ep, const char* epp, int w, int lane) {
  const int i = lane & 15, g = lane >> 4, q = i >> 2, p = lane & 3;
  f32x4 acc[8][2];
#pragma unroll
  for (int a = 0; a < 8; a++) { acc[a][0] = f32x4{0, 0, 0, 0}; acc[a][1] = f32x4{0, 0, 0, 0}; }
  const int aoff = i * 128 + ((g ^ (i >> 1)) * 16);
  const int boff = 16384 + (8 * g + q) * 128 + ((w ^ (((q >> 1) & 1) | ((g & 1) << 1))) * 32) + p * 8;
  s16x8 A[2][2][8]; s16x4 B[2][2][2][2];
  auto rd1 = [&](int buf, const char* slot, int r) {
    const int ks = r / 12, rr = r % 12;
    const int t = rr == 0 ? 0 : rr == 1 ? 1 : rr == 3 ? 2 : rr == 4 ? 3 : -1;
    const int a = rr == 2 ? 0 : rr - 4;
    if (t >= 0) B[buf][ks][t >> 1][t & 1] = trd(slot + boff + (t >> 1) * 8192 + ks * 4096 + (t & 1) * 512);
    else A[buf][ks][a] = lrd(slot + (aoff ^ (ks * 64)) + a * 2048);
  };
  auto mf1 = [&](int buf, int ks, int mt, int hh) {
    const s16x8 bf = cat(B[buf][ks][hh][0], B[buf][ks][hh][1]);
    if (QT) mfa(acc[mt][hh], bf, A[buf][ks][mt]); else mfa(acc[mt][hh], A[buf][ks][mt], bf);
  };
  auto region = [&](int cur, const char* slot) {
#pragma unroll
    for (int mi = 0; mi < 32; mi++) {
      mf1(cur, mi >> 4, (mi & 15) >> 1, mi & 1);
      SCHED();
      if (mi < 24) { rd1(cur ^ 1, slot, mi); SCHED(); }
    }
  };
  if (QT && QPF) {
    const unsigned ln = (((blockIdx.x >> 3) & 7) * 2 + (w & 1)) * 64 + lane;
    asm volatile("s_mov_b32 m0, %0\n\ts_nop 0\n\tglobal_load_lds_dword %2, %1" :: "s"(laddr_of(lds) + L_ATT + 8192), "s"(((w >> 1) ? wi : wr) + (size_t)(blockIdx.x & 7) * 131584), "v"(ln * 128u) : "memory");
  }
  if (w == 0) epoch_dma(lds, epp, lane);
  const bool zw = QT && blockIdx.x >= 128 && blockIdx.x < 136;
  if (zw && w == 3 && lane < 9) stc1(rs, WS_CNT + ((blockIdx.x & 7) * 9 + lane) * CS, 0u);
  __builtin_amdgcn_s_waitcnt(0xC07F);
  BAR();
  TS(1);
#pragma unroll
  for (int r = 0; r < 24; r++) rd1(0, lds, r);
  SCHED();
#pragma unroll 1
  for (int it = 0; it < 3; it++) {
    BAR(); SCHED();
    region(0, lds + ((2 * it + 1) & 3) * 32768);
    if (it == (zw ? 0 : 1)) WAITV(0);
    BAR(); SCHED();
    if (zw && it == 0 && w == 3) { const unsigned e = epoch_get(lds); if (lane < 8) stc1(rs, WS_F0 + lane * 128 + (blockIdx.x & 7) * 4, e); }
    region(1, lds + ((2 * it + 2) & 3) * 32768);
  }
  ep = epoch_get(lds);
  BAR(); SCHED();
  region(0, lds + 3 * 32768);
  asm volatile("s_nop 7");
  SCHED();
  auto mmh = [&](int hh) {
#pragma unroll
    for (int mt = 0; mt < 8; mt++)
#pragma unroll
      for (int ks = 0; ks < 2; ks++) {
        const s16x8 bf = cat(B[1][ks][hh][0], B[1][ks][hh][1]);
        acc[mt][hh] = QT ? mma(bf, A[1][ks][mt], acc[mt][hh]) : mma(A[1][ks][mt], bf, acc[mt][hh]);
      }
  };
  mmh(0);
  SCHED();
  TS(2);
  if (QT) {
    mmh(1);
#pragma unroll
    for (int mt = 0; mt < 8; mt++) {
      u32x4 v = {pk(phi(acc[mt][0][0]), phi(acc[mt][0][1])), pk(phi(acc[mt][0][2]), phi(acc[mt][0][3])),
                 pk(phi(acc[mt][1][0]), phi(acc[mt][1][1])), pk(phi(acc[mt][1][2]), phi(acc[mt][1][3]))};
      stc(rs, WS_PQ + (((m * 8 + mt) * 16 + (nt - 8) * 4 + w) * 64 + lane) * 16, v);
    }
    lwait(lds + L_FL + 100, 1);
    WAITV(0);
    arrive(rs, 34 + m, 16, WS_F2Q + m * 128, 1, ep, lane);
  } else {
    mmh(1);
    u32x4 pa[4];
#pragma unroll
    for (int u = 0; u < 4; u++)
      pa[u] = u32x4{pk(phi(acc[2 * u][0][0]), phi(acc[2 * u][0][1])), pk(phi(acc[2 * u][0][2]), phi(acc[2 * u][0][3])),
                    pk(phi(acc[2 * u + 1][0][0]), phi(acc[2 * u + 1][0][1])), pk(phi(acc[2 * u + 1][0][2]), phi(acc[2 * u + 1][0][3]))};
#pragma unroll
    for (int t = 0; t < 16; t++) { SGB(0x008, 1); SGB(0x002, 2); }
    SCHED();
    f32x4 kv = {0, 0, 0, 0}, z = {0, 0, 0, 0};
    const u32x4 ones = {0x3f803f80u, 0x3f803f80u, 0x3f803f80u, 0x3f803f80u};
#pragma unroll
    for (int u = 0; u < 4; u++) {
      u32x4 b = {pk(acc[2 * u][1][0], acc[2 * u][1][1]), pk(acc[2 * u][1][2], acc[2 * u][1][3]),
                 pk(acc[2 * u + 1][1][0], acc[2 * u + 1][1][1]), pk(acc[2 * u + 1][1][2], acc[2 * u + 1][1][3])};
      z = mma(as8(pa[u]), as8(ones), z);
      kv = mma(as8(pa[u]), as8(b), kv);
    }
    const int head = nt * 4 + w;
    u32x4 v = {pk(kv[0], kv[1]), pk(kv[2], kv[3]), pk(z[0], z[1]), pk(z[2], z[3])};
    stc(rs, WS_KVP + ((m * 32 + head) * 64 + lane) * 16, v);
    lwait(lds + L_FL + 100, 1);
    WAITV(0);
    if (ticket(rs, 1 + head, lane) == 15) {
      TS(4);
      u32x4 kl[16];
#pragma unroll
      for (int pp = 0; pp < 16; pp++) kl[pp] = ldc(rs, WS_KVP + ((pp * 32 + head) * 64 + lane) * 16);
      u32x4 r;
#pragma unroll
      for (int d = 0; d < 4; d++) {
        float lo[4], hi[4];
#pragma unroll
        for (int j = 0; j < 4; j++) {
          lo[j] = (bflo(kl[4 * j][d]) + bflo(kl[4 * j + 1][d])) + (bflo(kl[4 * j + 2][d]) + bflo(kl[4 * j + 3][d]));
          hi[j] = (bfhi(kl[4 * j][d]) + bfhi(kl[4 * j + 1][d])) + (bfhi(kl[4 * j + 2][d]) + bfhi(kl[4 * j + 3][d]));
        }
        r[d] = pk((lo[0] + lo[1]) + (lo[2] + lo[3]), (hi[0] + hi[1]) + (hi[2] + hi[3]));
      }
      const int u = (head >> 3) * 4 + (head & 3), hh = (head >> 2) & 1;
      const bool zr = (i & 3) == hh + 2 * (u & 1);
      __builtin_amdgcn_raw_buffer_store_b64(u32x2{r[0], r[1]}, rs, WS_KVR + (u * 64 + lane) * 16 + hh * 8, 0, 16);
      if (zr) stc(rs, WS_KVR + 16384 + ((u >> 1) * 64 + lane) * 16, hh ? u32x4{0u, 0u, r[2], r[3]} : u32x4{r[2], r[3], 0u, 0u});
      WAITV(0);
      arrive(rs, 33, 32, WS_F2K, 8, ep, lane);
      TS(5);
    }
  }
}

DI float dpp_sum16(float v) {
  v += __builtin_bit_cast(float, __builtin_amdgcn_update_dpp(0, __builtin_bit_cast(int, v), 0x128, 0xf, 0xf, false));
  v += __builtin_bit_cast(float, __builtin_amdgcn_update_dpp(0, __builtin_bit_cast(int, v), 0x124, 0xf, 0xf, false));
  v += __builtin_bit_cast(float, __builtin_amdgcn_update_dpp(0, __builtin_bit_cast(int, v), 0x122, 0xf, 0xf, false));
  v += __builtin_bit_cast(float, __builtin_amdgcn_update_dpp(0, __builtin_bit_cast(int, v), 0x121, 0xf, 0xf, false));
  return v;
}

DI void p1_rows(TSP char* lds, const char* x, const char* gw, const char* wo, const char* wr, const char* wi, rsrc_t rs, int a, const char* epp, int w, int lane) {
  const int i = lane & 15, g = lane >> 4;
  char* LX = lds;
  char* LS = lds + 32768;
  char* LC = lds + 106496;
  const int amt = (a >> 1) & 1;
  const int n2s = perm32(amt, 8 * (a & 1) + w);
  char* LF = lds + 69632;
  auto wdma = [&]() {
    const unsigned v = lane * 16;
    const char* fsrc = ((w >> 2) ? wi : wr) + (size_t)(8 * (a & 7) + 64 * (4 * (a >> 3) + (w & 3))) * 514;
    glds4(laddr_of(LF) + w * 4352, fsrc, v, v, v, v);
    if (lane == 0) asm volatile("s_mov_b32 m0, %0\n\ts_nop 0\n\tglobal_load_lds_dwordx4 %2, %1" :: "s"(laddr_of(LF) + w * 4352 + 4096), "s"(fsrc + 4096), "v"(v) : "memory");
  };
  if (WE) wdma();
  {
    const float fi = (float)i;
    const int d = w & 3;
    char* cp = LC + lane * 16 + d * 4;
    const float n2i = (float)(4 * g + d) * fi;
    if (w < 4) {
      float c[2], s[2];
#pragma unroll
      for (int e = 0; e < 2; e++) {
        const float ang = (float)(4 * g + 16 * (d >> 1) + 2 * (d & 1) + e) * (fi * (1.f / 32.f));
        c[e] = fcos(ang);
        s[e] = (i == 0) ? (e ? -1.f : 1.f) : fsin(-ang);
      }
      *(unsigned*)(cp) = pk(c[0], c[1]);
      *(unsigned*)(cp + 1024) = pk(s[0], s[1]);
      *(float*)(cp + 6144) = fcos(n2i * (1.f / 512.f));
      *(float*)(cp + 7168) = fsin(n2i * (1.f / 512.f));
    } else {
      {
        const float ang = n2i * (1.f / 16.f);
        const float cs = fcos(ang), sn = fsin(ang);
        *(unsigned*)(cp + 2048) = pk(cs, sn);
        *(unsigned*)(cp + 3072) = pk(sn, cs) ^ ((i >= 8) ? 0x80000000u : 0x00008000u);
      }
      {
        const float ang = n2i * (1.f / 32.f);
        const float cs = fcos(ang);
        const float sn = (i == 0) ? ((d & 1) ? -1.f : 1.f) : fsin(-ang);
        const unsigned u = pk(cs, sn);
        const int sh = (i & 1) * 16;
        *(unsigned*)(cp + 4096) = (u & 0xffffu) << sh;
        *(unsigned*)(cp + 5120) = (u >> 16) << sh;
      }
    }
  }
  int soff[4];
#pragma unroll
  for (int r = 0; r < 4; r++) {
    const int k2 = 4 * g + r;
    const int k = i + 32 * k2;
    const int col = (i == 0) ? 16 * k2 : (k2 < 8 ? k : 512 - k);
    soff[r] = col * 128 + ((w ^ (col & 7)) * 16);
    PIN(soff[r]);
  }
  TS(12);
  BAR();
  if (!WE) wdma();
  epoch_dma(lds, epp, lane);
  const u32x4 gwv = *(const u32x4*)(gw + lane * 16);
  asm volatile("s_mov_b32 m0, %0\n\ts_nop 0\n\tglobal_load_lds_dword %2, %1" :: "s"(laddr_of(lds) + L_ATT + 8192), "s"(wo), "v"((unsigned)((a * 8 + w) * 8 + (lane & 7)) * 128u) : "memory");
  const u32x4 b1re = *(const u32x4*)(LC + lane * 16), b1im = *(const u32x4*)(LC + 1024 + lane * 16);
  const u32x4 a3re = *(const u32x4*)(LC + 2048 + lane * 16), a3im = *(const u32x4*)(LC + 3072 + lane * 16);
  const u32x4 a4re = *(const u32x4*)(LC + 4096 + lane * 16), a4im = *(const u32x4*)(LC + 5120 + lane * 16);
  const f32x4 twc = *(const f32x4*)(LC + 6144 + lane * 16), tws = *(const f32x4*)(LC + 7168 + lane * 16);
  WAITV(8);
  TS(15);
  {
    unsigned o[4][4];
#pragma unroll
    for (int a4 = 0; a4 < 4; a4++) {
      const char* row = LX + (w * 4 + a4) * 1024;
      s16x8 xf = trd2(row + lane * 8, row + 512 + lane * 8);
      const f32x4 z4 = {0, 0, 0, 0};
      f32x4 are = mma(xf, as8(b1re), z4), aim = mma(xf, as8(b1im), z4);
      f32x4 tr = are * twc + aim * tws, ti = aim * twc - are * tws;
      asm("" : "+v"(tr), "+v"(ti));
      u32x4 bb;
#pragma unroll
      for (int r = 0; r < 4; r++) bb[r] = pk(tr[r], ti[r]);
      f32x4 xre = mma(as8(a3re), as8(bb), z4), xim = mma(as8(a3im), as8(bb), z4);
      f32x4 sre = mma(as8(a4re), as8(bb), z4), sim = mma(as8(a4im), as8(bb), z4);
#pragma unroll
      for (int r = 0; r < 4; r++) o[r][a4] = pk((i == 0) ? sre[r] : xre[r], (i == 0) ? sim[r] : xim[r]);
    }
    if (lane == 0) {
      u32x4 v = {o[0][0] >> 16, o[0][1] >> 16, o[0][2] >> 16, o[0][3] >> 16};
      *(u32x4*)(LS + 256 * 128 + w * 16) = v;
#pragma unroll
      for (int a4 = 0; a4 < 4; a4++) o[0][a4] &= 0xffffu;
    }
#pragma unroll
    for (int r = 0; r < 4; r++) {
      u32x4 v = {o[r][0], o[r][1], o[r][2], o[r][3]};
      *(u32x4*)(LS + soff[r]) = v;
    }
  }
  TS(14);
  BAR();
  TS(2);
  const int sl = lane & 7;
  const int sc = w * 8 + (lane >> 3);
  const int xline = (amt * 4 + (a >> 4)) * 1024 + ((a >> 2) & 3) * 256 + (a & 1) * 128;
  u32x4 v0[4];
#pragma unroll
  for (int j = 0; j < 4; j++) v0[j] = *(const u32x4*)(LS + (j * 64 + sc) * 128 + ((sl ^ (sc & 7)) * 16));
#pragma unroll
  for (int j = 0; j < 4; j++) stc(rs, WS_X1T + (xline >> 7) * BP + (j * 64 + sc) * 128 + sl * 16, v0[j]);
  TS(12);
  WAITV(7);
  TS(14);
  WAITV(5);
  TS(1);
  BAR();
  TS(13);
  {
    const int wline = ((((a & 7) >> 1) * 2 + ((a >> 3) & 1)) * 64 + (a >> 4) * 16 + (a & 1) * 8) * 16;
    const int cb = (lane >> 3) * 16 + (w & 3) + 128 * (w >> 2);
    const char* fp = LF + sl * 514 + cb * 2;
#pragma unroll
    for (int j = 0; j < 4; j++) {
      u32x4 fv;
#pragma unroll
      for (int r = 0; r < 4; r++) fv[r] = (unsigned)*(const unsigned short*)(fp + r * 4352 + j * 8) | ((unsigned)*(const unsigned short*)(fp + (4 + r) * 4352 + j * 8) << 16);
      stc(rs, WS_WT + (wline >> 7) * BP + (cb + 4 * j) * 128 + sl * 16, fv);
    }
    if (w == 7 && lane < 8) {
      u32x4 v;
#pragma unroll
      for (int r = 0; r < 4; r++) v[r] = (unsigned)*(const unsigned short*)(LF + sl * 514 + 512 + r * 4352) | ((unsigned)*(const unsigned short*)(LF + sl * 514 + 512 + (4 + r) * 4352) << 16);
      stc(rs, WS_WT + (wline >> 7) * BP + 256 * 128 + sl * 16, v);
    }
  }
  if (w == 3 && lane < 8) stc(rs, WS_X1T + (xline >> 7) * BP + 256 * 128 + lane * 16, *(const u32x4*)(LS + 256 * 128 + lane * 16));
  const u32x4 ones = {0x3f803f80u, 0x3f803f80u, 0x3f803f80u, 0x3f803f80u};
  float gsum[4];
#pragma unroll
  for (int j = 0; j < 4; j++) {
    u32x4 xv = *(const u32x4*)(LX + (w * 4 + j) * 1024 + lane * 16);
    u32x4 pr;
#pragma unroll
    for (int d = 0; d < 4; d++) pr[d] = pk(bflo(xv[d]) * bflo(gwv[d]), bfhi(xv[d]) * bfhi(gwv[d]));
    gsum[j] = mma(as8(ones), as8(pr), f32x4{0, 0, 0, 0})[0];
  }
  float gs = (g == 0) ? gsum[0] : (g == 1) ? gsum[1] : (g == 2) ? gsum[2] : gsum[3];
  gs = rbf(dpp_sum16(gs));
  const float gg = rbf(__builtin_amdgcn_rcpf(1.f + __builtin_amdgcn_exp2f(gs * -1.44269504088896341f)));
  const int s = 32 * (4 * (a >> 2) + g) + n2s;
  if (i == 0) stc1(rs, WS_GT + (s >> 6) * 256 + (s & 15) * 16 + ((s >> 4) & 3) * 4, pk(gg, 1.f - gg));
}

template <class F> DI void p2_fft(TSP F idle, char* lb, char* fl, char* f1seen, const char* ws, rsrc_t rs, int c, int cnt, int rep, unsigned& ep, int w, int lane) {
  const int i = lane & 15, g = lane >> 4;
  const int k1 = 16 * w + i;
  const float fk1 = (float)k1, fg = (float)g;
  u32x4 fre[4], fim[4], fc[2], fs[2], fn[2];
  {
    const float st = fk1 * (1.f / 64.f);
    const float a0 = fg * 4.f * st;
    const bool odd = i & 1;
    const unsigned m2 = odd ? 0x80008000u : 0u;
    const unsigned mre = (i & 2) ? 0x80008000u : 0u;
    const unsigned mim = ((i + 1) & 2) ? 0x80008000u : 0u;
#pragma unroll
    for (int d = 0; d < 4; d++) {
      const float ang = __builtin_fmaf((float)d, st, a0);
      const float cs = fcos(ang), sn = fsin(ang);
      const unsigned r0 = pk(cs, sn), i0 = pk(sn, cs) ^ 0x8000u;
      const unsigned r1 = (odd ? i0 : r0) ^ mre, i1 = (odd ? r0 : i0) ^ mim;
      fre[0][d] = r0; fim[0][d] = i0;
      fre[1][d] = r1; fim[1][d] = i1;
      fre[2][d] = r0 ^ m2; fim[2][d] = i0 ^ m2;
      fre[3][d] = r1 ^ m2; fim[3][d] = i1 ^ m2;
    }
#pragma unroll
    for (int ks = 0; ks < 4; ks++) { PIN(fre[ks]); PIN(fim[ks]); }
  }
  idle(0);
  float c1[2][4], s1[2][4], c2[2][4], s2[2][4];
#pragma unroll
  for (int mt = 0; mt < 2; mt++) {
    const float fa = (float)perm32(mt, i);
    const float st = fa * (1.f / 32.f), a0 = fg * 8.f * st;
    const float st1 = fk1 * (1.f / 2048.f), b0 = (fg * 8.f + (float)(4 * mt)) * st1;
    const float st2 = fa * (1.f / 2048.f), d0 = (fg * 4.f + (float)(16 * w)) * st2;
#pragma unroll
    for (int d = 0; d < 4; d++) {
      const float x0 = __builtin_fmaf((float)(2 * d), st, a0), x1 = __builtin_fmaf((float)(2 * d + 1), st, a0);
      fc[mt][d] = pk(fcos(x0), fcos(x1));
      fs[mt][d] = pk(fsin(x0), fsin(x1));
      fn[mt][d] = fs[mt][d] ^ 0x80008000u;
      const float t1 = __builtin_fmaf((float)d, st1, b0);
      c1[mt][d] = fcos(t1); s1[mt][d] = fsin(t1);
      const float t2 = __builtin_fmaf((float)d, st2, d0);
      c2[mt][d] = fcos(t2); s2[mt][d] = fsin(t2);
      PIN(c1[mt][d]); PIN(s1[mt][d]); PIN(c2[mt][d]); PIN(s2[mt][d]);
    }
    PIN(fc[mt]); PIN(fs[mt]); PIN(fn[mt]);
    idle(1 + mt);
  }
  const unsigned yt = WS_YT + (32 * k1 + 8 * g) * 2 + ((c == 256) ? 1 : (c & 1) * 256 + (c & ~1)) * YP;
  idle(3);
  TS(4);
  if (w == 0) { gwait<SLP>(rs, WS_F1 + rep * 128, ep); lset(f1seen); }
  else lwait(f1seen, 1);
  TS(5);
  const unsigned sv = (w * 16 + (lane >> 3)) * BP + c * 128 + (lane & 7) * 16;
  asm volatile("s_mov_b32 m0, %0\n\ts_nop 0\n\tglobal_load_lds_dwordx4 %2, %1 sc1\n\tglobal_load_lds_dwordx4 %3, %1 offset:1024 sc1"
               :: "s"(laddr_of(lb) + 8192 + w * 2048), "s"(ws + WS_X1T), "v"(sv), "v"(sv + 8 * BP - 1024) : "memory");
  u32x4 wf[2];
#pragma unroll
  for (int mt = 0; mt < 2; mt++) wf[mt] = ldc(rs, WS_WT + sv + mt * 8 * BP);
  WAITV(2);
  linc(fl, lane);
  lspin(fl, 4);
  TS(6);
  const f32x4 z4 = {0, 0, 0, 0};
  f32x4 re[2], im[2];
#pragma unroll
  for (int mt = 0; mt < 2; mt++) { re[mt] = z4; im[mt] = z4; }
#pragma unroll
  for (int ks = 0; ks < 4; ks++)
#pragma unroll
    for (int mt = 0; mt < 2; mt++) {
      s16x8 xa = lrd(lb + 8192 + ((mt * 4 + ks) * 64 + lane) * 16);
      re[mt] = mma(xa, as8(fre[ks]), re[mt]); im[mt] = mma(xa, as8(fim[ks]), im[mt]);
    }
  u32x4 pr, pi;
#pragma unroll
  for (int mt = 0; mt < 2; mt++) {
    float tr[4], ti[4];
#pragma unroll
    for (int r = 0; r < 4; r++) { tr[r] = re[mt][r] * c1[mt][r] + im[mt][r] * s1[mt][r]; ti[r] = im[mt][r] * c1[mt][r] - re[mt][r] * s1[mt][r]; }
    pr[2 * mt] = pk(tr[0], tr[1]); pr[2 * mt + 1] = pk(tr[2], tr[3]);
    pi[2 * mt] = pk(ti[0], ti[1]); pi[2 * mt + 1] = pk(ti[2], ti[3]);
  }
#pragma unroll
  for (int mt = 0; mt < 2; mt++) {
    re[mt] = mma(as8(fs[mt]), as8(pi), mma(as8(fc[mt]), as8(pr), z4));
    im[mt] = mma(as8(fn[mt]), as8(pr), mma(as8(fc[mt]), as8(pi), z4));
  }
#pragma unroll
  for (int mt = 0; mt < 2; mt++) {
    float tr[4], ti[4];
#pragma unroll
    for (int r = 0; r < 4; r++) {
      const float a = bflo(wf[mt][r]), b = bfhi(wf[mt][r]);
      tr[r] = re[mt][r] * a - im[mt][r] * b; ti[r] = re[mt][r] * b + im[mt][r] * a;
    }
    pr[2 * mt] = pk(tr[0], tr[1]); pr[2 * mt + 1] = pk(tr[2], tr[3]);
    pi[2 * mt] = pk(ti[0], ti[1]); pi[2 * mt + 1] = pk(ti[2], ti[3]);
  }
#pragma unroll
  for (int nt = 0; nt < 2; nt++) {
    re[nt] = mma(as8(pi), as8(fn[nt]), mma(as8(pr), as8(fc[nt]), z4));
    im[nt] = mma(as8(pi), as8(fc[nt]), mma(as8(pr), as8(fs[nt]), z4));
  }
#pragma unroll
  for (int nt = 0; nt < 2; nt++) {
    u32x4 v;
#pragma unroll
    for (int r = 0; r < 4; r++) v[r] = pk(re[nt][r] * c2[nt][r] - im[nt][r] * s2[nt][r], -re[nt][r] * s2[nt][r] - im[nt][r] * c2[nt][r]);
    *(u32x4*)(lb + ((nt * 4 + w) * 64 + lane) * 16) = v;
  }
  linc(fl + 4, lane);
  lspin(fl + 4, 4);
#pragma unroll
  for (int mt = 0; mt < 2; mt++) { re[mt] = z4; im[mt] = z4; }
#pragma unroll
  for (int ks = 0; ks < 4; ks++)
#pragma unroll
    for (int mt = 0; mt < 2; mt++) {
      s16x8 ya = lrd(lb + ((mt * 4 + ks) * 64 + lane) * 16);
      re[mt] = mma(ya, as8(fre[ks]), re[mt]); im[mt] = mma(ya, as8(fim[ks]), im[mt]);
    }
  u32x4 vr = {pk(re[0][0], re[0][1]), pk(re[0][2], re[0][3]), pk(re[1][0], re[1][1]), pk(re[1][2], re[1][3])};
  u32x4 vi = {pk(im[0][0], im[0][1]), pk(im[0][2], im[0][3]), pk(im[1][0], im[1][1]), pk(im[1][2], im[1][3])};
  stc(rs, yt, vr);
  if (c & 255) stc(rs, yt + YP, vi);
  if (c != 256) {
    WAITV(0);
    arrive(rs, 50 + w * 4 + cnt, cnt == 0 ? 65 : 64, WS_F3 + (w * 4 + cnt) * 128, 1, ep, lane);
  }
  TS(7);
}

extern "C" __global__ __launch_bounds__(512, 2) void gm_all(const char* x, const char* wqkv, const char* wr, const char* wi, const char* gw, const char* wo, char* ws, char* out) {
  __shared__ __attribute__((aligned(16))) char lds[163840];
  const int b = blockIdx.x, xcd = b & 7, ii = b >> 3;
  const int w8 = __builtin_amdgcn_readfirstlane(threadIdx.x >> 6), lane = threadIdx.x & 63;
  const int w = w8 & 3;
  TSDECL
  TS(0);
  if ((ii >> 1) == 9 && w8 < 4) {
    unsigned long long pc; int o0, o1;
    asm volatile("s_getpc_b64 %0\n.Lgm_pc%=:\n\ts_mov_b32 %1, gm_all-.Lgm_pc%=\n\ts_mov_b32 %2, gm_end-.Lgm_pc%=" : "=s"(pc), "=s"(o0), "=s"(o1));
    const unsigned off = ((ii & 1) * 256 + threadIdx.x) * 128;
    if (off < (unsigned)(o1 - o0))
      asm volatile("s_mov_b32 m0, %0\n\ts_nop 0\n\tglobal_load_lds_dword %2, %1" :: "s"(laddr_of(lds) + L_ATT + 8192), "s"(pc + (long long)o0), "v"(off) : "memory");
  }
  if (ii >= 24) {
    const int a = xcd * 8 + (ii - 24);
    const unsigned v = lane * 16;
    glds4(laddr_of(lds) + w8 * 4096, x + (size_t)(128 * (a >> 2) + perm32((a >> 1) & 1, 8 * (a & 1) + w8)) * 1024, v, v + 31 * 1024, v + 62 * 1024, v + 93 * 1024);
  }
  unsigned ep;
  const char* epp = ws + WS_E;
  const rsrc_t rs = __builtin_amdgcn_make_buffer_rsrc(ws, 0, WS_END, 0x00020000);
  char* FL = lds + L_FL;
  const int ml = ii & 3, nl = ii >> 2;
  const int mtile = xcd * 4 + ml, ntile = nl;
  const bool aux = ii >= 24;
  {
    const int m = (xcd >> 1) * 4 + ml;
    const int nt = nl < 4 ? (xcd & 1) * 4 + nl : 8 + (xcd & 1) * 2 + (nl - 4);
    if (w8 < 4 || aux) {
      if (w8 == 0) *(LAS unsigned*)(FL + lane * 4) = 0;
      if (aux) p1_rows(TSA lds, x, gw, wo, wr, wi, rs, xcd * 8 + (ii - 24), epp, w8, lane);
      if (w8 < 4) {
        __builtin_amdgcn_s_setprio(2);
        if (aux) {
          ep = epoch_get(lds);
          if (w == 1) gwait<SLP>(rs, WS_F0 + xcd * 128 + (lane & 1) * 16, ep);
          TS(4);
          WAITV(0);
          TS(5);
          BAR();
          TS(6);
          if (w == 1) { arrive(rs, 0, 64, WS_F1, 8, ep, lane); lset(FL + 124); }
          lwait(FL + 124, 1);
#pragma unroll 1
          for (int j = 0; j < 4; j++) wo_dma(lds, wo, ntile, j, w, lane);
        }
        else if (nl < 4) p1_gemm<false>(TSA lds, wr, wi, rs, m, nt, ep, epp, w, lane);
        else p1_gemm<true>(TSA lds, wr, wi, rs, m, nt, ep, epp, w, lane);
        __builtin_amdgcn_s_setprio(0);
      }
    } else {
      const int col0 = nl >= 4 ? (nt - 8) * 128 : 512 + nt * 64;
      const int col1 = nl >= 4 ? col0 + 64 : col0 + 512;
      p1_prod(lds, x, wqkv, wo, ntile, m, col0, col1, (2 * ml + nl) & ROT1, w, lane);
    }
  }
  TS(3);
  const int i = lane & 15, g = lane >> 4, q = i >> 2, p = lane & 3;
  const int fsw = (2 * g + (q >> 1)) & 3;
  if (w8 >= 4 || b == SPECIAL) {
    const bool pg = w8 >= 4;
    if (pg) {
      ep = epoch_get(lds);
      if (!aux) {
        if (w == 0) { gwait<SLP>(rs, WS_F0 + xcd * 128 + (lane & 1) * 16, ep); lset(FL + 100); }
        wo_dma(lds, wo, ntile, 2, w, lane); wo_dma(lds, wo, ntile, 3, w, lane);
      }
    }
    if (!aux && nl >= 4) __builtin_amdgcn_s_setprio(3); else __builtin_amdgcn_s_setprio(1);
    auto idle = [&](int st) {
      if (st == AST && pg && aux) { WAITV(0); BAR(); }
      if (st == 3 && !aux) { WAITV(0); linc(FL, lane); }
    };
    p2_fft(TSA idle, pg ? lds + L_FFT : lds + L_Y, pg ? FL + 4 : FL + 20, pg ? FL + 16 : FL + 28, ws, rs, pg ? b : 256, pg ? (b & 1) * 2 + (b >> 7) : 0, xcd, ep, w, lane);
  }
  if (w8 < 4) {
    linc(FL + 80, lane);
    if (aux) { if (b != SPECIAL) WAITV(0); linc(FL, lane); }
    TS(7);
    const int stg = 4 * mtile + w;
    bool kok = false;
    if (w == 0) {
#pragma unroll 1
      for (int sp = 0; sp < 200000; sp++) {
        FENCE();
        const bool ok = ldc(rs, (lane & 1) ? WS_F2K + xcd * 128 : WS_F2Q + (mtile >> 1) * 128)[0] == ep;
        const unsigned long long mk = __ballot(ok);
        kok = mk & 2;
        if (mk & 1) break;
        __builtin_amdgcn_s_sleep(SLP);
      }
      FENCE();
      if (kok) lset(FL + 88);
      lset(FL + 104);
    }
    unsigned tk = 0;
    if (b == SPECIAL) { WAITV(0); tk = ticket_issue(rs, 50 + w * 4, lane); FENCE(); }
    lwait(FL + 104, 1);
    u32x4 pq[4][4];
#pragma unroll
    for (int jj = 0; jj < 4; jj++)
#pragma unroll
      for (int u = 0; u < 4; u++) pq[jj][u] = ldc(rs, WS_PQ + (((4 * mtile + ((w + 1 + jj) & 3)) * 16 + 4 * w + u) * 64 + lane) * 16);
    lwait(FL, 4);
    s16x8 wf[4][4];
    {
      const int toffA = L_WO + w * 16384 + (4 * g + q) * 128 + (fsw * 32) + p * 8;
#pragma unroll
      for (int u = 0; u < 4; u++)
#pragma unroll
        for (int a = 0; a < 4; a++) { const char* pp = lds + (toffA ^ (32 * a)) + u * 2048; wf[u][a] = trd2(pp, pp + 8192); }
    }
    if (b == SPECIAL) ticket_done(rs, tk, 65, WS_F3 + (w * 4) * 128, 1, ep, lane);
    if (w == 0) { if (!kok) { gwait<SLP>(rs, WS_F2K + xcd * 128, ep); lset(FL + 88); } }
    else lwait(FL + 88, 1);
    TS(8);
    u32x4 kr[6];
#pragma unroll
    for (int j = 0; j < 6; j++) kr[j] = ldc(rs, WS_KVR + ((j < 4 ? 4 * w + j : 12 + 2 * w + j) * 64 + lane) * 16);
    lwait(FL + 16, 1);
    const unsigned gv = __builtin_amdgcn_raw_buffer_load_b32(rs, WS_GT + mtile * 256 + i * 16 + w * 4, 0, 16);
    TS(13);
    const f32x4 z4 = {0, 0, 0, 0};
    const f32x4 eps = {1e-6f, 1e-6f, 1e-6f, 1e-6f};
    f32x4 acc[4];
    f32x4 nm[2][4][2], dn[2][4];
    auto nd = [&](int jj) {
#pragma unroll
      for (int u = 0; u < 4; u++) {
        dn[jj & 1][u] = mma(as8(kr[4 + (u >> 1)]), as8(pq[jj][u]), eps);
        nm[jj & 1][u][0] = mma16(as4(u32x2{kr[u][0], kr[u][1]}), as4(u32x2{pq[jj][u][0], pq[jj][u][1]}), z4);
        nm[jj & 1][u][1] = mma16(as4(u32x2{kr[u][2], kr[u][3]}), as4(u32x2{pq[jj][u][2], pq[jj][u][3]}), z4);
      }
    };
    u32x4 of[2][4];
    auto sc = [&](int jj) {
#pragma unroll
      for (int u = 0; u < 4; u++) {
        const f32x4 n0 = nm[jj & 1][u][0] * __builtin_amdgcn_rcpf(dn[jj & 1][u][2 * (u & 1)]);
        const f32x4 n1 = nm[jj & 1][u][1] * __builtin_amdgcn_rcpf(dn[jj & 1][u][2 * (u & 1) + 1]);
        of[jj & 1][u] = u32x4{pk(n0[0], n0[1]), pk(n0[2], n0[3]), pk(n1[0], n1[1]), pk(n1[2], n1[3])};
      }
    };
    auto pj = [&](int jj) {
#pragma unroll
      for (int u = 0; u < 4; u++)
#pragma unroll
        for (int a = 0; a < 4; a++) acc[a] = mma(wf[u][a], as8(of[jj & 1][u]), u ? acc[a] : z4);
      if (jj < 3) {
#pragma unroll
        for (int a = 0; a < 4; a++) *(f32x4*)(lds + L_WO + w * 16384 + (jj * 4 + a) * 1024 + lane * 16) = acc[a];
      }
    };
    nd(0); SCHED();
    nd(1); sc(0);
#pragma unroll
    for (int t = 0; t < 12; t++) { SGB(0x008, 1); SGB(0x002, 3); }
    SCHED();
#pragma unroll
    for (int jj = 0; jj < 3; jj++) {
      pj(jj); sc(jj + 1);
#pragma unroll
      for (int t = 0; t < 16; t++) { SGB(0x008, 1); SGB(0x002, 3); }
      SCHED();
      if (jj < 2) { nd(jj + 2); SCHED(); }
    }
    linc(FL + 92, lane);
    pj(3);
    TS(9);
    lwait(FL + 92, 4);
#pragma unroll
    for (int ww = 1; ww < 4; ww++)
#pragma unroll
      for (int a = 0; a < 4; a++) acc[a] += *(const f32x4*)(lds + L_WO + ((w + ww) & 3) * 16384 + ((3 - ww) * 4 + a) * 1024 + lane * 16);
    lwait(FL + 48, 1);
    lspin(FL + 48, 4);
    TS(10);
    const float ga = bflo(gv), gb = bfhi(gv);
    unsigned pv[4][2];
#pragma unroll
    for (int a = 0; a < 2; a++) {
      const f32x4 t0 = *(const f32x4*)(lds + L_FFT + ((w * 4 + a) * 64 + lane) * 16);
      const f32x4 t1 = *(const f32x4*)(lds + L_FFT + ((w * 4 + a + 2) * 64 + lane) * 16);
      const f32x4 v0 = acc[a] * ga + (t0 + t1) * gb, v1 = acc[a + 2] * ga + (t0 - t1) * gb;
      pv[a][0] = pk(v0[0], v0[1]); pv[a][1] = pk(v0[2], v0[3]);
      pv[a + 2][0] = pk(v1[0], v1[1]); pv[a + 2][1] = pk(v1[2], v1[3]);
    }
#pragma unroll
    for (int a = 0; a < 2; a++) {
      auto s0 = __builtin_amdgcn_permlane16_swap(pv[2 * a][0], pv[2 * a + 1][0], false, false);
      auto s1 = __builtin_amdgcn_permlane16_swap(pv[2 * a][1], pv[2 * a + 1][1], false, false);
      u32x4 v = {s0[0], s1[0], s0[1], s1[1]};
      char* op = out + (size_t)(16 * stg + i) * 1024 + (256 * a + 32 * ntile + 16 * (g & 1) + 8 * (g >> 1)) * 2;
      asm volatile("global_store_dwordx4 %0, %1, off sc1\n\ts_nop 1" :: "v"(op), "v"(v) : "memory");
    }
    TS(11);
    TSFLUSH();
    return;
  }
  {
    const float sgn = (i & 1) ? -1.f : 1.f;
    const unsigned k0v = (i & 1) ? 0xb5803580u : 0x35803580u;
    const float fn = (float)(32 * ntile + 16 * (w & 1) + i) * (1.f / 512.f);
    const float t2 = 2.f * fcos(fn * 32.f);
    f32x2 c0[4], c1[4];
#pragma unroll
    for (int d = 0; d < 4; d++) {
      const float kf = (float)(4 * g + 128 * (w & 1) + (w >> 1)) + (float)(16 * (d >> 1) + 2 * (d & 1));
      const float a0 = kf * fn, a1 = (kf + 32.f) * fn;
      c0[d] = f32x2{fcos(a0), fsin(a0)} * 0x1p-19f;
      c1[d] = f32x2{fcos(a1), fsin(a1)} * 0x1p-19f;
    }
    u32x4 gf[8];
#pragma unroll
    for (int j = 0; j < 2; j++) {
#pragma unroll
      for (int h = 0; h < 2; h++) {
#pragma unroll
        for (int d = 0; d < 4; d++) {
          gf[4 * j + 2 * h][d] = pk(c0[d][0], c0[d][1]);
          gf[4 * j + 2 * h + 1][d] = pk(c1[d][0], c1[d][1]);
          c0[d] = c1[d] * t2 - c0[d];
        }
#pragma unroll
        for (int d = 0; d < 4; d++) c1[d] = c0[d] * t2 - c1[d];
      }
      gf[4 * j][0] = (w == j && g == 0) ? k0v : gf[4 * j][0];
      if (j == 0) {
        const float sg = (w & 1) ? sgn : 1.f;
#pragma unroll
        for (int d = 0; d < 4; d++) { c0[d] *= sg; c1[d] *= sg; }
      }
    }
#pragma unroll
    for (int k = 0; k < 8; k++) PIN(gf[k]);
    TS(8);
    gwait<SLP>(rs, WS_F3 + ((mtile >> 3) * 4 + w) * 128, ep);
    lwait(FL + 80, 4);
    linc(FL + 96, lane);
    TS(9);
    const int toffB = L_Y + (4 * g + q) * 128 + (fsw * 32) + p * 8;
    f32x4 accb[4];
    s16x8 tb_[2][2][4];
    auto rd = [&](int u) {
      const char* slot = lds + (w ^ (u >> 1)) * 16384 + (u & 1) * 8192;
#pragma unroll
      for (int ks = 0; ks < 2; ks++)
#pragma unroll
        for (int c = 0; c < 4; c++) tb_[u & 1][ks][c] = trd2(slot + (toffB ^ (32 * c)) + ks * 4096, slot + (toffB ^ (32 * c)) + ks * 4096 + 2048);
    };
    auto mm = [&](int u) {
#pragma unroll
      for (int ks = 0; ks < 2; ks++)
#pragma unroll
        for (int c = 0; c < 4; c++) accb[c] = mma(as8(gf[2 * u + ks]), tb_[u & 1][ks][c], accb[c]);
    };
    {
      const unsigned v = (lane >> 3) * YP + ((((lane & 7) >> 1) ^ ((lane >> 4) & 3)) * 32) + (lane & 1) * 16;
      const char* src = ws + WS_YT + (size_t)(w * 128) * YP + mtile * 128;
      const unsigned la = laddr_of(lds) + L_Y + w * 16384;
#pragma unroll
      for (int j = 0; j < 4; j++) glds4c(la + j * 4096, src + (size_t)j * (32 * YP), v, v + (8 * YP - 1024), v + 2 * (8 * YP - 1024), v + 3 * (8 * YP - 1024));
    }
#pragma unroll
    for (int c = 0; c < 4; c++) accb[c] = f32x4{0, 0, 0, 0};
    WAITV(8);
    rd(0);
    WAITV(0);
    lset(FL + 32 + w * 4);
    TS(10);
    rd(1); mm(0);
#pragma unroll
    for (int t = 0; t < 8; t++) { SGB(0x100, 2); SGB(0x008, 1); }
    SCHED();
    lspin(FL + 32 + (w ^ 1) * 4, 1);
    rd(2); mm(1);
#pragma unroll
    for (int t = 0; t < 8; t++) { SGB(0x100, 2); SGB(0x008, 1); }
    SCHED();
    rd(3); mm(2);
#pragma unroll
    for (int t = 0; t < 8; t++) { SGB(0x100, 2); SGB(0x008, 1); }
    SCHED();
    mm(3);
    lwait(FL + 96, 4);
    if (b == 0 && w == 1 && lane == 0) stc1(rs, WS_E, ep);
#pragma unroll
    for (int c = 0; c < 4; c++) *(f32x4*)(lds + L_FFT + ((c * 4 + w) * 64 + lane) * 16) = accb[c];
    linc(FL + 48, lane);
    TS(11);
    TSFLUSH();
  }
}

extern "C" __global__ void gm_end() {}

extern "C" void kernel_launch(void* const* d_in, const int* in_sizes, int n_in, void* d_out, int out_size, void* d_ws, size_t ws_size, hipStream_t stream) {
  gm_all<<<256, 512, 0, stream>>>((const char*)d_in[0], (const char*)d_in[1], (const char*)d_in[4], (const char*)d_in[5], (const char*)d_in[6],
                                  (const char*)d_in[2], (char*)d_ws, (char*)d_out);
}
```

```cpp
#include <hip/hip_runtime.h>
#include <stdint.h>

typedef short s16x4 __attribute__((ext_vector_type(4)));
typedef short s16x8 __attribute__((ext_vector_type(8)));
typedef float f32x4 __attribute__((ext_vector_type(4)));
typedef float f32x2 __attribute__((ext_vector_type(2)));
typedef unsigned u32x4 __attribute__((ext_vector_type(4)));
typedef unsigned u32x2 __attribute__((ext_vector_type(2)));
typedef __bf16 bfx2 __attribute__((ext_vector_type(2)));
#define LAS __attribute__((address_space(3)))
#define DI __device__ __forceinline__
static constexpr int ROT1 = 6;
static constexpr int SLP = 2;

static constexpr unsigned BP = 258 * 128;
static constexpr unsigned YP = 4224;
static constexpr unsigned CS = 4352;
static constexpr unsigned WS_X1T = 0;
static constexpr unsigned WS_WT  = WS_X1T + (9u << 18);
static constexpr unsigned WS_PQ  = WS_WT + (9u << 18);
static constexpr unsigned WS_KVP = WS_PQ + (2u << 20);
static constexpr unsigned WS_GT  = WS_KVP + (512u << 10);
static constexpr unsigned WS_KVR = WS_GT + (16u << 10);
static constexpr unsigned WS_YT  = WS_KVR + (32u << 10);
static constexpr unsigned WS_E   = WS_YT + (9u << 18);
static constexpr unsigned WS_CNT = WS_E + 4096;
static constexpr unsigned WS_F0  = WS_CNT + 72 * CS;
static constexpr unsigned WS_F1  = WS_F0 + 8 * 128;
static constexpr unsigned WS_F2K = WS_F1 + 8 * 128;
static constexpr unsigned WS_F2Q = WS_F2K + 8 * 128;
static constexpr unsigned WS_F3  = WS_F2Q + 16 * 128;
static constexpr unsigned WS_END = WS_F3 + 16 * 128;
static constexpr int SPECIAL = 255;

static constexpr int L_WO = 0, L_Y = 65536, L_FFT = 131072, L_DUMP = 155648, L_FL = 163840 - 256;

DI unsigned pk(float lo, float hi) { f32x2 p = {lo, hi}; return __builtin_bit_cast(unsigned, __builtin_convertvector(p, bfx2)); }
DI float bflo(unsigned u) { return __builtin_bit_cast(float, u << 16); }
DI float bfhi(unsigned u) { return __builtin_bit_cast(float, u & 0xffff0000u); }
DI float rbf(float v) { return bflo(pk(v, 0.f)); }
DI s16x8 as8(u32x4 v) { return __builtin_bit_cast(s16x8, v); }
DI s16x4 as4(u32x2 v) { return __builtin_bit_cast(s16x4, v); }
DI f32x4 mma(s16x8 a, s16x8 b, f32x4 c) { return __builtin_amdgcn_mfma_f32_16x16x32_bf16(a, b, c, 0, 0, 0); }
DI f32x4 mma16(s16x4 a, s16x4 b, f32x4 c) { return __builtin_amdgcn_mfma_f32_16x16x16bf16_1k(a, b, c, 0, 0, 0); }
DI void mfa(f32x4& c, s16x8 a, s16x8 b) { asm volatile("v_mfma_f32_16x16x32_bf16 %0, %1, %2, %0" : "+v"(c) : "v"(a), "v"(b)); }
DI s16x4 trd(const char* l) { return __builtin_amdgcn_ds_read_tr16_b64_v4i16((LAS s16x4*)l); }
DI s16x8 cat(s16x4 a, s16x4 b) { return __builtin_shufflevector(a, b, 0, 1, 2, 3, 4, 5, 6, 7); }
DI s16x8 trd2(const char* l0, const char* l1) { return cat(trd(l0), trd(l1)); }
DI s16x8 lrd(const char* l) { return *(const s16x8*)l; }
DI float fsin(float rev) { return __builtin_amdgcn_sinf(rev); }
DI float fcos(float rev) { return __builtin_amdgcn_cosf(rev); }
DI unsigned laddr_of(const char* l) { return (unsigned)(size_t)(const LAS char*)l; }
DI void glds4(unsigned laddr, const char* sb, unsigned v0, unsigned v1, unsigned v2, unsigned v3) {
  asm volatile("s_mov_b32 m0, %0\n\ts_nop 0\n\t"
               "global_load_lds_dwordx4 %2, %1\n\t"
               "global_load_lds_dwordx4 %3, %1 offset:1024\n\t"
               "global_load_lds_dwordx4 %4, %1 offset:2048\n\t"
               "global_load_lds_dwordx4 %5, %1 offset:3072"
               :: "s"(laddr), "s"(sb), "v"(v0), "v"(v1), "v"(v2), "v"(v3) : "memory");
}
DI void glds4c(unsigned laddr, const char* sb, unsigned v0, unsigned v1, unsigned v2, unsigned v3) {
  asm volatile("s_mov_b32 m0, %0\n\ts_nop 0\n\t"
               "global_load_lds_dwordx4 %2, %1 sc1\n\t"
               "global_load_lds_dwordx4 %3, %1 offset:1024 sc1\n\t"
               "global_load_lds_dwordx4 %4, %1 offset:2048 sc1\n\t"
               "global_load_lds_dwordx4 %5, %1 offset:3072 sc1"
               :: "s"(laddr), "s"(sb), "v"(v0), "v"(v1), "v"(v2), "v"(v3) : "memory");
}
DI int perm32(int t, int i) { return 8 * (i >> 2) + 4 * t + (i & 3); }
DI float phi(float t) {
  float e = __builtin_amdgcn_exp2f(t * 1.44269504088896341f);
  e = __builtin_amdgcn_fmed3f(e, 0.f, 1.f);
  return fmaxf(t + 1e-6f, 1e-6f) + e;
}
#define SCHED() __builtin_amdgcn_sched_barrier(0)
#define SGB(mask, n) __builtin_amdgcn_sched_group_barrier(mask, n, 0)
#define PIN(x) asm volatile("" : "+v"(x))
#define WAITV(n) asm volatile("s_waitcnt vmcnt(" #n ")" ::: "memory")
#define BAR() asm volatile("s_waitcnt lgkmcnt(0)\n\ts_barrier" ::: "memory")
#define FENCE() asm volatile("" ::: "memory")

typedef __amdgpu_buffer_rsrc_t rsrc_t;
DI u32x4 ldc(rsrc_t r, unsigned off) { return __builtin_amdgcn_raw_buffer_load_b128(r, off, 0, 16); }
DI void stc(rsrc_t r, unsigned off, u32x4 v) { __builtin_amdgcn_raw_buffer_store_b128(v, r, off, 0, 16); }
DI void stc1(rsrc_t r, unsigned off, unsigned v) { __builtin_amdgcn_raw_buffer_store_b32(v, r, off, 0, 16); }
template <int SL> DI void gwait(rsrc_t r, unsigned off, unsigned ep) {
#pragma unroll 1
  for (int sp = 0; sp < 200000; sp++) {
    FENCE();
    const u32x4 f = ldc(r, off);
    if (__all(f[0] == ep && f[1] == ep && f[2] == ep && f[3] == ep)) break;
    __builtin_amdgcn_s_sleep(SL);
  }
  FENCE();
}
DI unsigned ticket(rsrc_t r, int c, int lane) {
  unsigned old = 0;
  if (lane == 0) old = __builtin_amdgcn_raw_ptr_buffer_atomic_add_i32(1, r, WS_CNT + c * CS, 0, 16);
  old = __builtin_amdgcn_readfirstlane(old);
  FENCE();
  return old;
}
DI void arrive(rsrc_t r, int c, unsigned n, unsigned flag, int nrep, unsigned ep, int lane) {
  if (ticket(r, c, lane) == n - 1 && lane < nrep) stc(r, flag + lane * 128, u32x4{ep, ep, ep, ep});
}
DI void linc(char* p, int lane) { if (lane == 0) asm volatile("ds_add_u32 %0, %1" :: "v"(laddr_of(p)), "v"(1u) : "memory"); }
DI void lset(char* p) { asm volatile("ds_write_b32 %0, %1" :: "v"(laddr_of(p)), "v"(1u) : "memory"); }
DI void lwait(const char* p, unsigned want) {
#pragma unroll 1
  for (int sp = 0; sp < 4000000; sp++) {
    if ((unsigned)__builtin_amdgcn_readfirstlane(*(const volatile LAS unsigned*)p) >= want) break;
    __builtin_amdgcn_s_sleep(1);
  }
  FENCE();
}

DI void lspin(const char* p, unsigned want) {
#pragma unroll 1
  for (int sp = 0; sp < 8000000; sp++)
    if ((unsigned)__builtin_amdgcn_readfirstlane(*(const volatile LAS unsigned*)p) >= want) break;
  FENCE();
}

DI void epoch_dma(char* lds, const char* p, int lane) {
  if (lane == 0) asm volatile("s_mov_b32 m0, %0\n\ts_nop 0\n\tglobal_load_lds_dword %2, %1 sc1" :: "s"(laddr_of(lds) + L_FL - 16), "s"(p), "v"(0u) : "memory");
}
DI unsigned epoch_get(const char* lds) { return (unsigned)__builtin_amdgcn_readfirstlane(*(const volatile LAS unsigned*)(lds + L_FL - 16)) * 1664525u + 1013904223u; }
DI unsigned ticket_issue(rsrc_t r, int c, int lane) {
  unsigned old = 0;
  if (lane == 0) old = __builtin_amdgcn_raw_ptr_buffer_atomic_add_i32(1, r, WS_CNT + c * CS, 0, 16);
  return old;
}
DI void ticket_done(rsrc_t r, unsigned old, unsigned n, unsigned flag, int nrep, unsigned ep, int lane) {
  if ((unsigned)__builtin_amdgcn_readfirstlane(old) == n - 1 && lane < nrep) stc(r, flag + lane * 128, u32x4{ep, ep, ep, ep});
}

DI void wo_dma(char* lds, const char* wo, int ntile, int j, int w, int lane) {
  const int blk = ((lane & 7) >> 1) ^ ((lane >> 4) & 3);
  const unsigned v = (lane >> 3) * 1024 + (blk & 1) * 32 + (blk >> 1) * 512 + (lane & 1) * 16;
  glds4(laddr_of(lds) + L_WO + j * 16384 + w * 4096, wo + (size_t)(j * 128 + w * 32) * 1024 + ntile * 64, v, v + 7168, v + 2 * 7168, v + 3 * 7168);
}

DI void p1_prod(char* lds, const char* x, const char* wqkv, const char* wo, int ntile, int m, int col0, int col1, int rot, int w, int lane) {
  unsigned voffA[4], voffB[4];
#pragma unroll
  for (int j = 0; j < 4; j++) {
    const int par = j & 1;
    voffA[j] = (lane >> 3) * 1024 + (((lane & 7) ^ ((4 * par + (lane >> 4)) & 7)) * 16) + j * (8192 - 1024);
    voffB[j] = (lane >> 3) * 3072 + ((((lane & 7) >> 1) ^ (((lane >> 4) & 1) | (par << 1))) * 32) + (lane & 1) * 16 + j * (24576 - 1024);
  }
  const unsigned lbase = laddr_of(lds) + w * 4096;
  const char* xa = x + (size_t)(m * 128 + w * 32) * 1024;
  const char* wb = wqkv + (size_t)((w & 1) * 32) * 3072 + ((w >> 1) ? col1 : col0) * 2;
  auto issue = [&](int idx) {
    const int ks = (idx + rot) & 7;
    const unsigned slot = lbase + (idx & 3) * 32768;
    glds4(slot, xa + ks * 128, voffA[0], voffA[1], voffA[2], voffA[3]);
    glds4(slot + 16384, wb + (size_t)(ks * 64) * 3072, voffB[0], voffB[1], voffB[2], voffB[3]);
  };
  issue(0); issue(1);
  WAITV(8); BAR();
  issue(2); issue(3);
#pragma unroll 1
  for (int j = 1; j < 5; j++) { WAITV(16); BAR(); issue(j + 3); }
  WAITV(16); BAR();
  wo_dma(lds, wo, ntile, 0, w, lane);
  WAITV(12); BAR();
  wo_dma(lds, wo, ntile, 1, w, lane);
  WAITV(8); BAR();
}

template <bool QT>
DI void p1_gemm(char* lds, rsrc_t rs, int m, int nt, unsigned& ep, const char* epp, int w, int lane) {
  const int i = lane & 15, g = lane >> 4, q = i >> 2, p = lane & 3;
  f32x4 acc[8][2];
#pragma unroll
  for (int a = 0; a < 8; a++) { acc[a][0] = f32x4{0, 0, 0, 0}; acc[a][1] = f32x4{0, 0, 0, 0}; }
  const int aoff = i * 128 + ((g ^ (i >> 1)) * 16);
  const int boff = 16384 + (8 * g + q) * 128 + ((w ^ (((q >> 1) & 1) | ((g & 1) << 1))) * 32) + p * 8;
  s16x8 A[2][2][8]; s16x4 B[2][2][2][2];
  auto rd1 = [&](int buf, const char* slot, int r) {
    const int ks = r / 12, rr = r % 12;
    const int t = rr == 0 ? 0 : rr == 1 ? 1 : rr == 3 ? 2 : rr == 4 ? 3 : -1;
    const int a = rr == 2 ? 0 : rr - 4;
    if (t >= 0) B[buf][ks][t >> 1][t & 1] = trd(slot + boff + (t >> 1) * 8192 + ks * 4096 + (t & 1) * 512);
    else A[buf][ks][a] = lrd(slot + (aoff ^ (ks * 64)) + a * 2048);
  };
  auto mf1 = [&](int buf, int ks, int mt, int hh) {
    const s16x8 bf = cat(B[buf][ks][hh][0], B[buf][ks][hh][1]);
    if (QT) mfa(acc[mt][hh], bf, A[buf][ks][mt]); else mfa(acc[mt][hh], A[buf][ks][mt], bf);
  };
  auto region = [&](int cur, const char* slot) {
#pragma unroll
    for (int mi = 0; mi < 32; mi++) {
      mf1(cur, mi >> 4, (mi & 15) >> 1, mi & 1);
      SCHED();
      if (mi < 24) { rd1(cur ^ 1, slot, mi); SCHED(); }
    }
  };
  if (w == 0) epoch_dma(lds, epp, lane);
  const bool zw = QT && blockIdx.x >= 128 && blockIdx.x < 136;
  if (zw && w == 3 && lane < 9) stc1(rs, WS_CNT + ((blockIdx.x & 7) * 9 + lane) * CS, 0u);
  __builtin_amdgcn_s_waitcnt(0xC07F);
  BAR();
#pragma unroll
  for (int r = 0; r < 24; r++) rd1(0, lds, r);
  SCHED();
#pragma unroll 1
  for (int it = 0; it < 3; it++) {
    BAR(); SCHED();
    region(0, lds + ((2 * it + 1) & 3) * 32768);
    if (it == (zw ? 0 : 1)) WAITV(0);
    BAR(); SCHED();
    if (zw && it == 0 && w == 3) { const unsigned e = epoch_get(lds); if (lane < 8) stc1(rs, WS_F0 + lane * 128 + (blockIdx.x & 7) * 4, e); }
    region(1, lds + ((2 * it + 2) & 3) * 32768);
  }
  ep = epoch_get(lds);
  BAR(); SCHED();
  region(0, lds + 3 * 32768);
  asm volatile("s_nop 7");
  SCHED();
  auto mmh = [&](int hh) {
#pragma unroll
    for (int mt = 0; mt < 8; mt++)
#pragma unroll
      for (int ks = 0; ks < 2; ks++) {
        const s16x8 bf = cat(B[1][ks][hh][0], B[1][ks][hh][1]);
        acc[mt][hh] = QT ? mma(bf, A[1][ks][mt], acc[mt][hh]) : mma(A[1][ks][mt], bf, acc[mt][hh]);
      }
  };
  mmh(0);
  SCHED();
  if (QT) {
    mmh(1);
#pragma unroll
    for (int mt = 0; mt < 8; mt++) {
      u32x4 v = {pk(phi(acc[mt][0][0]), phi(acc[mt][0][1])), pk(phi(acc[mt][0][2]), phi(acc[mt][0][3])),
                 pk(phi(acc[mt][1][0]), phi(acc[mt][1][1])), pk(phi(acc[mt][1][2]), phi(acc[mt][1][3]))};
      stc(rs, WS_PQ + (((m * 8 + mt) * 16 + (nt - 8) * 4 + w) * 64 + lane) * 16, v);
    }
    lwait(lds + L_FL + 100, 1);
    WAITV(0);
    arrive(rs, 34 + m, 16, WS_F2Q + m * 128, 1, ep, lane);
  } else {
    mmh(1);
    u32x4 pa[4];
#pragma unroll
    for (int u = 0; u < 4; u++)
      pa[u] = u32x4{pk(phi(acc[2 * u][0][0]), phi(acc[2 * u][0][1])), pk(phi(acc[2 * u][0][2]), phi(acc[2 * u][0][3])),
                    pk(phi(acc[2 * u + 1][0][0]), phi(acc[2 * u + 1][0][1])), pk(phi(acc[2 * u + 1][0][2]), phi(acc[2 * u + 1][0][3]))};
#pragma unroll
    for (int t = 0; t < 16; t++) { SGB(0x008, 1); SGB(0x002, 2); }
    SCHED();
    f32x4 kv = {0, 0, 0, 0}, z = {0, 0, 0, 0};
    const u32x4 ones = {0x3f803f80u, 0x3f803f80u, 0x3f803f80u, 0x3f803f80u};
#pragma unroll
    for (int u = 0; u < 4; u++) {
      u32x4 b = {pk(acc[2 * u][1][0], acc[2 * u][1][1]), pk(acc[2 * u][1][2], acc[2 * u][1][3]),
                 pk(acc[2 * u + 1][1][0], acc[2 * u + 1][1][1]), pk(acc[2 * u + 1][1][2], acc[2 * u + 1][1][3])};
      z = mma(as8(pa[u]), as8(ones), z);
      kv = mma(as8(pa[u]), as8(b), kv);
    }
    const int head = nt * 4 + w;
    u32x4 v = {pk(kv[0], kv[1]), pk(kv[2], kv[3]), pk(z[0], z[1]), pk(z[2], z[3])};
    stc(rs, WS_KVP + ((m * 32 + head) * 64 + lane) * 16, v);
    lwait(lds + L_FL + 100, 1);
    WAITV(0);
    if (ticket(rs, 1 + head, lane) == 15) {
      u32x4 kl[16];
#pragma unroll
      for (int pp = 0; pp < 16; pp++) kl[pp] = ldc(rs, WS_KVP + ((pp * 32 + head) * 64 + lane) * 16);
      u32x4 r;
#pragma unroll
      for (int d = 0; d < 4; d++) {
        float lo[4], hi[4];
#pragma unroll
        for (int j = 0; j < 4; j++) {
          lo[j] = (bflo(kl[4 * j][d]) + bflo(kl[4 * j + 1][d])) + (bflo(kl[4 * j + 2][d]) + bflo(kl[4 * j + 3][d]));
          hi[j] = (bfhi(kl[4 * j][d]) + bfhi(kl[4 * j + 1][d])) + (bfhi(kl[4 * j + 2][d]) + bfhi(kl[4 * j + 3][d]));
        }
        r[d] = pk((lo[0] + lo[1]) + (lo[2] + lo[3]), (hi[0] + hi[1]) + (hi[2] + hi[3]));
      }
      const int u = (head >> 3) * 4 + (head & 3), hh = (head >> 2) & 1;
      const bool zr = (i & 3) == hh + 2 * (u & 1);
      __builtin_amdgcn_raw_buffer_store_b64(u32x2{r[0], r[1]}, rs, WS_KVR + (u * 64 + lane) * 16 + hh * 8, 0, 16);
      if (zr) stc(rs, WS_KVR + 16384 + ((u >> 1) * 64 + lane) * 16, hh ? u32x4{0u, 0u, r[2], r[3]} : u32x4{r[2], r[3], 0u, 0u});
      WAITV(0);
      arrive(rs, 33, 32, WS_F2K, 8, ep, lane);
    }
  }
}

DI float dpp_sum16(float v) {
  v += __builtin_bit_cast(float, __builtin_amdgcn_update_dpp(0, __builtin_bit_cast(int, v), 0x128, 0xf, 0xf, false));
  v += __builtin_bit_cast(float, __builtin_amdgcn_update_dpp(0, __builtin_bit_cast(int, v), 0x124, 0xf, 0xf, false));
  v += __builtin_bit_cast(float, __builtin_amdgcn_update_dpp(0, __builtin_bit_cast(int, v), 0x122, 0xf, 0xf, false));
  v += __builtin_bit_cast(float, __builtin_amdgcn_update_dpp(0, __builtin_bit_cast(int, v), 0x121, 0xf, 0xf, false));
  return v;
}

DI void p1_rows(char* lds, const char* x, const char* gw, const char* wo, const char* wr, const char* wi, rsrc_t rs, int a, const char* epp, int w, int lane) {
  const int i = lane & 15, g = lane >> 4;
  char* LX = lds;
  char* LS = lds + 32768;
  char* LC = lds + 106496;
  const int amt = (a >> 1) & 1;
  const int n2s = perm32(amt, 8 * (a & 1) + w);
  char* LF = lds + 69632;
  auto wdma = [&]() {
    const unsigned v = lane * 16;
    const char* fsrc = ((w >> 2) ? wi : wr) + (size_t)(8 * (a & 7) + 64 * (4 * (a >> 3) + (w & 3))) * 514;
    glds4(laddr_of(LF) + w * 4352, fsrc, v, v, v, v);
    if (lane == 0) asm volatile("s_mov_b32 m0, %0\n\ts_nop 0\n\tglobal_load_lds_dwordx4 %2, %1" :: "s"(laddr_of(LF) + w * 4352 + 4096), "s"(fsrc + 4096), "v"(v) : "memory");
  };
  {
    const float fi = (float)i;
    const int d = w & 3;
    char* cp = LC + lane * 16 + d * 4;
    const float n2i = (float)(4 * g + d) * fi;
    if (w < 4) {
      float c[2], s[2];
#pragma unroll
      for (int e = 0; e < 2; e++) {
        const float ang = (float)(4 * g + 16 * (d >> 1) + 2 * (d & 1) + e) * (fi * (1.f / 32.f));
        c[e] = fcos(ang);
        s[e] = (i == 0) ? (e ? -1.f : 1.f) : fsin(-ang);
      }
      *(unsigned*)(cp) = pk(c[0], c[1]);
      *(unsigned*)(cp + 1024) = pk(s[0], s[1]);
      *(float*)(cp + 6144) = fcos(n2i * (1.f / 512.f));
      *(float*)(cp + 7168) = fsin(n2i * (1.f / 512.f));
    } else {
      {
        const float ang = n2i * (1.f / 16.f);
        const float cs = fcos(ang), sn = fsin(ang);
        *(unsigned*)(cp + 2048) = pk(cs, sn);
        *(unsigned*)(cp + 3072) = pk(sn, cs) ^ ((i >= 8) ? 0x80000000u : 0x00008000u);
      }
      {
        const float ang = n2i * (1.f / 32.f);
        const float cs = fcos(ang);
        const float sn = (i == 0) ? ((d & 1) ? -1.f : 1.f) : fsin(-ang);
        const unsigned u = pk(cs, sn);
        const int sh = (i & 1) * 16;
        *(unsigned*)(cp + 4096) = (u & 0xffffu) << sh;
        *(unsigned*)(cp + 5120) = (u >> 16) << sh;
      }
    }
  }
  int soff[4];
#pragma unroll
  for (int r = 0; r < 4; r++) {
    const int k2 = 4 * g + r;
    const int k = i + 32 * k2;
    const int col = (i == 0) ? 16 * k2 : (k2 < 8 ? k : 512 - k);
    soff[r] = col * 128 + ((w ^ (col & 7)) * 16);
    PIN(soff[r]);
  }
  BAR();
  wdma();
  epoch_dma(lds, epp, lane);
  const u32x4 gwv = *(const u32x4*)(gw + lane * 16);
  asm volatile("s_mov_b32 m0, %0\n\ts_nop 0\n\tglobal_load_lds_dword %2, %1" :: "s"(laddr_of(lds) + L_DUMP), "s"(wo), "v"((unsigned)((a * 8 + w) * 8 + (lane & 7)) * 128u) : "memory");
  const u32x4 b1re = *(const u32x4*)(LC + lane * 16), b1im = *(const u32x4*)(LC + 1024 + lane * 16);
  const u32x4 a3re = *(const u32x4*)(LC + 2048 + lane * 16), a3im = *(const u32x4*)(LC + 3072 + lane * 16);
  const u32x4 a4re = *(const u32x4*)(LC + 4096 + lane * 16), a4im = *(const u32x4*)(LC + 5120 + lane * 16);
  const f32x4 twc = *(const f32x4*)(LC + 6144 + lane * 16), tws = *(const f32x4*)(LC + 7168 + lane * 16);
  WAITV(8);
  {
    unsigned o[4][4];
#pragma unroll
    for (int a4 = 0; a4 < 4; a4++) {
      const char* row = LX + (w * 4 + a4) * 1024;
      s16x8 xf = trd2(row + lane * 8, row + 512 + lane * 8);
      const f32x4 z4 = {0, 0, 0, 0};
      f32x4 are = mma(xf, as8(b1re), z4), aim = mma(xf, as8(b1im), z4);
      f32x4 tr = are * twc + aim * tws, ti = aim * twc - are * tws;
      asm("" : "+v"(tr), "+v"(ti));
      u32x4 bb;
#pragma unroll
      for (int r = 0; r < 4; r++) bb[r] = pk(tr[r], ti[r]);
      f32x4 xre = mma(as8(a3re), as8(bb), z4), xim = mma(as8(a3im), as8(bb), z4);
      f32x4 sre = mma(as8(a4re), as8(bb), z4), sim = mma(as8(a4im), as8(bb), z4);
#pragma unroll
      for (int r = 0; r < 4; r++) o[r][a4] = pk((i == 0) ? sre[r] : xre[r], (i == 0) ? sim[r] : xim[r]);
    }
    if (lane == 0) {
      u32x4 v = {o[0][0] >> 16, o[0][1] >> 16, o[0][2] >> 16, o[0][3] >> 16};
      *(u32x4*)(LS + 256 * 128 + w * 16) = v;
#pragma unroll
      for (int a4 = 0; a4 < 4; a4++) o[0][a4] &= 0xffffu;
    }
#pragma unroll
    for (int r = 0; r < 4; r++) {
      u32x4 v = {o[r][0], o[r][1], o[r][2], o[r][3]};
      *(u32x4*)(LS + soff[r]) = v;
    }
  }
  BAR();
  const int sl = lane & 7;
  const int sc = w * 8 + (lane >> 3);
  const int xline = (amt * 4 + (a >> 4)) * 1024 + ((a >> 2) & 3) * 256 + (a & 1) * 128;
  u32x4 v0[4];
#pragma unroll
  for (int j = 0; j < 4; j++) v0[j] = *(const u32x4*)(LS + (j * 64 + sc) * 128 + ((sl ^ (sc & 7)) * 16));
#pragma unroll
  for (int j = 0; j < 4; j++) stc(rs, WS_X1T + (xline >> 7) * BP + (j * 64 + sc) * 128 + sl * 16, v0[j]);
  WAITV(5);
  BAR();
  {
    const int wline = ((((a & 7) >> 1) * 2 + ((a >> 3) & 1)) * 64 + (a >> 4) * 16 + (a & 1) * 8) * 16;
    const int cb = (lane >> 3) * 16 + (w & 3) + 128 * (w >> 2);
    const char* fp = LF + sl * 514 + cb * 2;
#pragma unroll
    for (int j = 0; j < 4; j++) {
      u32x4 fv;
#pragma unroll
      for (int r = 0; r < 4; r++) fv[r] = (unsigned)*(const unsigned short*)(fp + r * 4352 + j * 8) | ((unsigned)*(const unsigned short*)(fp + (4 + r) * 4352 + j * 8) << 16);
      stc(rs, WS_WT + (wline >> 7) * BP + (cb + 4 * j) * 128 + sl * 16, fv);
    }
    if (w == 7 && lane < 8) {
      u32x4 v;
#pragma unroll
      for (int r = 0; r < 4; r++) v[r] = (unsigned)*(const unsigned short*)(LF + sl * 514 + 512 + r * 4352) | ((unsigned)*(const unsigned short*)(LF + sl * 514 + 512 + (4 + r) * 4352) << 16);
      stc(rs, WS_WT + (wline >> 7) * BP + 256 * 128 + sl * 16, v);
    }
  }
  if (w == 3 && lane < 8) stc(rs, WS_X1T + (xline >> 7) * BP + 256 * 128 + lane * 16, *(const u32x4*)(LS + 256 * 128 + lane * 16));
  const u32x4 ones = {0x3f803f80u, 0x3f803f80u, 0x3f803f80u, 0x3f803f80u};
  float gsum[4];
#pragma unroll
  for (int j = 0; j < 4; j++) {
    u32x4 xv = *(const u32x4*)(LX + (w * 4 + j) * 1024 + lane * 16);
    u32x4 pr;
#pragma unroll
    for (int d = 0; d < 4; d++) pr[d] = pk(bflo(xv[d]) * bflo(gwv[d]), bfhi(xv[d]) * bfhi(gwv[d]));
    gsum[j] = mma(as8(ones), as8(pr), f32x4{0, 0, 0, 0})[0];
  }
  float gs = (g == 0) ? gsum[0] : (g == 1) ? gsum[1] : (g == 2) ? gsum[2] : gsum[3];
  gs = rbf(dpp_sum16(gs));
  const float gg = rbf(__builtin_amdgcn_rcpf(1.f + __builtin_amdgcn_exp2f(gs * -1.44269504088896341f)));
  const int s = 32 * (4 * (a >> 2) + g) + n2s;
  if (i == 0) stc1(rs, WS_GT + (s >> 6) * 256 + (s & 15) * 16 + ((s >> 4) & 3) * 4, pk(gg, 1.f - gg));
}

template <class F> DI void p2_fft(F idle, char* lb, char* fl, char* f1seen, const char* ws, rsrc_t rs, int c, int cnt, int rep, unsigned& ep, int w, int lane) {
  const int i = lane & 15, g = lane >> 4;
  const int k1 = 16 * w + i;
  const float fk1 = (float)k1, fg = (float)g;
  u32x4 fre[4], fim[4], fc[2], fs[2], fn[2];
  {
    const float st = fk1 * (1.f / 64.f);
    const float a0 = fg * 4.f * st;
    const bool odd = i & 1;
    const unsigned m2 = odd ? 0x80008000u : 0u;
    const unsigned mre = (i & 2) ? 0x80008000u : 0u;
    const unsigned mim = ((i + 1) & 2) ? 0x80008000u : 0u;
#pragma unroll
    for (int d = 0; d < 4; d++) {
      const float ang = __builtin_fmaf((float)d, st, a0);
      const float cs = fcos(ang), sn = fsin(ang);
      const unsigned r0 = pk(cs, sn), i0 = pk(sn, cs) ^ 0x8000u;
      const unsigned r1 = (odd ? i0 : r0) ^ mre, i1 = (odd ? r0 : i0) ^ mim;
      fre[0][d] = r0; fim[0][d] = i0;
      fre[1][d] = r1; fim[1][d] = i1;
      fre[2][d] = r0 ^ m2; fim[2][d] = i0 ^ m2;
      fre[3][d] = r1 ^ m2; fim[3][d] = i1 ^ m2;
    }
#pragma unroll
    for (int ks = 0; ks < 4; ks++) { PIN(fre[ks]); PIN(fim[ks]); }
  }
  float c1[2][4], s1[2][4], c2[2][4], s2[2][4];
#pragma unroll
  for (int mt = 0; mt < 2; mt++) {
    const float fa = (float)perm32(mt, i);
    const float st = fa * (1.f / 32.f), a0 = fg * 8.f * st;
    const float st1 = fk1 * (1.f / 2048.f), b0 = (fg * 8.f + (float)(4 * mt)) * st1;
    const float st2 = fa * (1.f / 2048.f), d0 = (fg * 4.f + (float)(16 * w)) * st2;
#pragma unroll
    for (int d = 0; d < 4; d++) {
      const float x0 = __builtin_fmaf((float)(2 * d), st, a0), x1 = __builtin_fmaf((float)(2 * d + 1), st, a0);
      fc[mt][d] = pk(fcos(x0), fcos(x1));
      fs[mt][d] = pk(fsin(x0), fsin(x1));
      fn[mt][d] = fs[mt][d] ^ 0x80008000u;
      const float t1 = __builtin_fmaf((float)d, st1, b0);
      c1[mt][d] = fcos(t1); s1[mt][d] = fsin(t1);
      const float t2 = __builtin_fmaf((float)d, st2, d0);
      c2[mt][d] = fcos(t2); s2[mt][d] = fsin(t2);
      PIN(c1[mt][d]); PIN(s1[mt][d]); PIN(c2[mt][d]); PIN(s2[mt][d]);
    }
    PIN(fc[mt]); PIN(fs[mt]); PIN(fn[mt]);
    idle(1 + mt);
  }
  const unsigned yt = WS_YT + (32 * k1 + 8 * g) * 2 + ((c == 256) ? 1 : (c & 1) * 256 + (c & ~1)) * YP;
  idle(3);
  if (w == 0) { gwait<SLP>(rs, WS_F1 + rep * 128, ep); lset(f1seen); }
  else lwait(f1seen, 1);
  const unsigned sv = (w * 16 + (lane >> 3)) * BP + c * 128 + (lane & 7) * 16;
  asm volatile("s_mov_b32 m0, %0\n\ts_nop 0\n\tglobal_load_lds_dwordx4 %2, %1 sc1\n\tglobal_load_lds_dwordx4 %3, %1 offset:1024 sc1"
               :: "s"(laddr_of(lb) + 8192 + w * 2048), "s"(ws + WS_X1T), "v"(sv), "v"(sv + 8 * BP - 1024) : "memory");
  u32x4 wf[2];
#pragma unroll
  for (int mt = 0; mt < 2; mt++) wf[mt] = ldc(rs, WS_WT + sv + mt * 8 * BP);
  WAITV(2);
  linc(fl, lane);
  lspin(fl, 4);
  const f32x4 z4 = {0, 0, 0, 0};
  f32x4 re[2], im[2];
#pragma unroll
  for (int mt = 0; mt < 2; mt++) { re[mt] = z4; im[mt] = z4; }
#pragma unroll
  for (int ks = 0; ks < 4; ks++)
#pragma unroll
    for (int mt = 0; mt < 2; mt++) {
      s16x8 xa = lrd(lb + 8192 + ((mt * 4 + ks) * 64 + lane) * 16);
      re[mt] = mma(xa, as8(fre[ks]), re[mt]); im[mt] = mma(xa, as8(fim[ks]), im[mt]);
    }
  u32x4 pr, pi;
#pragma unroll
  for (int mt = 0; mt < 2; mt++) {
    float tr[4], ti[4];
#pragma unroll
    for (int r = 0; r < 4; r++) { tr[r] = re[mt][r] * c1[mt][r] + im[mt][r] * s1[mt][r]; ti[r] = im[mt][r] * c1[mt][r] - re[mt][r] * s1[mt][r]; }
    pr[2 * mt] = pk(tr[0], tr[1]); pr[2 * mt + 1] = pk(tr[2], tr[3]);
    pi[2 * mt] = pk(ti[0], ti[1]); pi[2 * mt + 1] = pk(ti[2], ti[3]);
  }
#pragma unroll
  for (int mt = 0; mt < 2; mt++) {
    re[mt] = mma(as8(fs[mt]), as8(pi), mma(as8(fc[mt]), as8(pr), z4));
    im[mt] = mma(as8(fn[mt]), as8(pr), mma(as8(fc[mt]), as8(pi), z4));
  }
#pragma unroll
  for (int mt = 0; mt < 2; mt++) {
    float tr[4], ti[4];
#pragma unroll
    for (int r = 0; r < 4; r++) {
      const float a = bflo(wf[mt][r]), b = bfhi(wf[mt][r]);
      tr[r] = re[mt][r] * a - im[mt][r] * b; ti[r] = re[mt][r] * b + im[mt][r] * a;
    }
    pr[2 * mt] = pk(tr[0], tr[1]); pr[2 * mt + 1] = pk(tr[2], tr[3]);
    pi[2 * mt] = pk(ti[0], ti[1]); pi[2 * mt + 1] = pk(ti[2], ti[3]);
  }
#pragma unroll
  for (int nt = 0; nt < 2; nt++) {
    re[nt] = mma(as8(pi), as8(fn[nt]), mma(as8(pr), as8(fc[nt]), z4));
    im[nt] = mma(as8(pi), as8(fc[nt]), mma(as8(pr), as8(fs[nt]), z4));
  }
#pragma unroll
  for (int nt = 0; nt < 2; nt++) {
    u32x4 v;
#pragma unroll
    for (int r = 0; r < 4; r++) v[r] = pk(re[nt][r] * c2[nt][r] - im[nt][r] * s2[nt][r], -re[nt][r] * s2[nt][r] - im[nt][r] * c2[nt][r]);
    *(u32x4*)(lb + ((nt * 4 + w) * 64 + lane) * 16) = v;
  }
  linc(fl + 4, lane);
  lspin(fl + 4, 4);
#pragma unroll
  for (int mt = 0; mt < 2; mt++) { re[mt] = z4; im[mt] = z4; }
#pragma unroll
  for (int ks = 0; ks < 4; ks++)
#pragma unroll
    for (int mt = 0; mt < 2; mt++) {
      s16x8 ya = lrd(lb + ((mt * 4 + ks) * 64 + lane) * 16);
      re[mt] = mma(ya, as8(fre[ks]), re[mt]); im[mt] = mma(ya, as8(fim[ks]), im[mt]);
    }
  u32x4 vr = {pk(re[0][0], re[0][1]), pk(re[0][2], re[0][3]), pk(re[1][0], re[1][1]), pk(re[1][2], re[1][3])};
  u32x4 vi = {pk(im[0][0], im[0][1]), pk(im[0][2], im[0][3]), pk(im[1][0], im[1][1]), pk(im[1][2], im[1][3])};
  stc(rs, yt, vr);
  if (c & 255) stc(rs, yt + YP, vi);
  if (c != 256) {
    WAITV(0);
    arrive(rs, 50 + w * 4 + cnt, cnt == 0 ? 65 : 64, WS_F3 + (w * 4 + cnt) * 128, 1, ep, lane);
  }
}

extern "C" __global__ __launch_bounds__(512, 2) void gm_all(const char* x, const char* wqkv, const char* wr, const char* wi, const char* gw, const char* wo, char* ws, char* out) {
  __shared__ __attribute__((aligned(16))) char lds[163840];
  const int b = blockIdx.x, xcd = b & 7, ii = b >> 3;
  const int w8 = __builtin_amdgcn_readfirstlane(threadIdx.x >> 6), lane = threadIdx.x & 63;
  const int w = w8 & 3;
  if ((ii >> 1) == 9 && w8 < 4) {
    unsigned long long pc; int o0, o1;
    asm volatile("s_getpc_b64 %0\n.Lgm_pc%=:\n\ts_mov_b32 %1, gm_all-.Lgm_pc%=\n\ts_mov_b32 %2, gm_end-.Lgm_pc%=" : "=s"(pc), "=s"(o0), "=s"(o1));
    const unsigned off = ((ii & 1) * 256 + threadIdx.x) * 128;
    if (off < (unsigned)(o1 - o0))
      asm volatile("s_mov_b32 m0, %0\n\ts_nop 0\n\tglobal_load_lds_dword %2, %1" :: "s"(laddr_of(lds) + L_DUMP), "s"(pc + (long long)o0), "v"(off) : "memory");
  }
  if (ii >= 24) {
    const int a = xcd * 8 + (ii - 24);
    const unsigned v = lane * 16;
    glds4(laddr_of(lds) + w8 * 4096, x + (size_t)(128 * (a >> 2) + perm32((a >> 1) & 1, 8 * (a & 1) + w8)) * 1024, v, v + 31 * 1024, v + 62 * 1024, v + 93 * 1024);
  }
  unsigned ep;
  const char* epp = ws + WS_E;
  const rsrc_t rs = __builtin_amdgcn_make_buffer_rsrc(ws, 0, WS_END, 0x00020000);
  char* FL = lds + L_FL;
  const int ml = ii & 3, nl = ii >> 2;
  const int mtile = xcd * 4 + ml, ntile = nl;
  const bool aux = ii >= 24;
  {
    const int m = (xcd >> 1) * 4 + ml;
    const int nt = nl < 4 ? (xcd & 1) * 4 + nl : 8 + (xcd & 1) * 2 + (nl - 4);
    if (w8 < 4 || aux) {
      if (w8 == 0) *(LAS unsigned*)(FL + lane * 4) = 0;
      if (aux) p1_rows(lds, x, gw, wo, wr, wi, rs, xcd * 8 + (ii - 24), epp, w8, lane);
      if (w8 < 4) {
        __builtin_amdgcn_s_setprio(2);
        if (aux) {
          ep = epoch_get(lds);
          if (w == 1) gwait<SLP>(rs, WS_F0 + xcd * 128 + (lane & 1) * 16, ep);
          WAITV(0);
          BAR();
          if (w == 1) { arrive(rs, 0, 64, WS_F1, 8, ep, lane); lset(FL + 124); }
          lwait(FL + 124, 1);
#pragma unroll 1
          for (int j = 0; j < 4; j++) wo_dma(lds, wo, ntile, j, w, lane);
        }
        else if (nl < 4) p1_gemm<false>(lds, rs, m, nt, ep, epp, w, lane);
        else p1_gemm<true>(lds, rs, m, nt, ep, epp, w, lane);
        __builtin_amdgcn_s_setprio(0);
      }
    } else {
      const int col0 = nl >= 4 ? (nt - 8) * 128 : 512 + nt * 64;
      const int col1 = nl >= 4 ? col0 + 64 : col0 + 512;
      p1_prod(lds, x, wqkv, wo, ntile, m, col0, col1, (2 * ml + nl) & ROT1, w, lane);
    }
  }
  const int i = lane & 15, g = lane >> 4, q = i >> 2, p = lane & 3;
  const int fsw = (2 * g + (q >> 1)) & 3;
  if (w8 >= 4 || b == SPECIAL) {
    const bool pg = w8 >= 4;
    if (pg) {
      ep = epoch_get(lds);
      if (!aux) {
        if (w == 0) { gwait<SLP>(rs, WS_F0 + xcd * 128 + (lane & 1) * 16, ep); lset(FL + 100); }
        wo_dma(lds, wo, ntile, 2, w, lane); wo_dma(lds, wo, ntile, 3, w, lane);
      }
    }
    if (!aux && nl >= 4) __builtin_amdgcn_s_setprio(3); else __builtin_amdgcn_s_setprio(1);
    auto idle = [&](int st) {
      if (st == 1 && pg && aux) { WAITV(0); BAR(); }
      if (st == 3 && !aux) { WAITV(0); linc(FL, lane); }
    };
    p2_fft(idle, pg ? lds + L_FFT : lds + L_Y, pg ? FL + 4 : FL + 20, pg ? FL + 16 : FL + 28, ws, rs, pg ? b : 256, pg ? (b & 1) * 2 + (b >> 7) : 0, xcd, ep, w, lane);
  }
  if (w8 < 4) {
    linc(FL + 80, lane);
    if (aux) { if (b != SPECIAL) WAITV(0); linc(FL, lane); }
    const int stg = 4 * mtile + w;
    bool kok = false;
    if (w == 0) {
#pragma unroll 1
      for (int sp = 0; sp < 200000; sp++) {
        FENCE();
        const u32x4 f4 = ldc(rs, (lane & 1) ? WS_F2K + xcd * 128 : WS_F2Q + (mtile >> 1) * 128);
        const bool ok = f4[0] == ep && f4[1] == ep && f4[2] == ep && f4[3] == ep;
        const unsigned long long mk = __ballot(ok);
        kok = mk & 2;
        if (mk & 1) break;
        __builtin_amdgcn_s_sleep(SLP);
      }
      FENCE();
      if (kok) lset(FL + 88);
      lset(FL + 104);
    }
    unsigned tk = 0;
    if (b == SPECIAL) { WAITV(0); tk = ticket_issue(rs, 50 + w * 4, lane); FENCE(); }
    lwait(FL + 104, 1);
    u32x4 pq[4][4];
#pragma unroll
    for (int jj = 0; jj < 4; jj++)
#pragma unroll
      for (int u = 0; u < 4; u++) pq[jj][u] = ldc(rs, WS_PQ + (((4 * mtile + ((w + 1 + jj) & 3)) * 16 + 4 * w + u) * 64 + lane) * 16);
    lwait(FL, 4);
    s16x8 wf[4][4];
    {
      const int toffA = L_WO + w * 16384 + (4 * g + q) * 128 + (fsw * 32) + p * 8;
#pragma unroll
      for (int u = 0; u < 4; u++)
#pragma unroll
        for (int a = 0; a < 4; a++) { const char* pp = lds + (toffA ^ (32 * a)) + u * 2048; wf[u][a] = trd2(pp, pp + 8192); }
    }
    if (b == SPECIAL) ticket_done(rs, tk, 65, WS_F3 + (w * 4) * 128, 1, ep, lane);
    if (w == 0) { if (!kok) { gwait<SLP>(rs, WS_F2K + xcd * 128, ep); lset(FL + 88); } }
    else lwait(FL + 88, 1);
    u32x4 kr[6];
#pragma unroll
    for (int j = 0; j < 6; j++) kr[j] = ldc(rs, WS_KVR + ((j < 4 ? 4 * w + j : 12 + 2 * w + j) * 64 + lane) * 16);
    lwait(FL + 16, 1);
    const unsigned gv = __builtin_amdgcn_raw_buffer_load_b32(rs, WS_GT + mtile * 256 + i * 16 + w * 4, 0, 16);
    const f32x4 z4 = {0, 0, 0, 0};
    const f32x4 eps = {1e-6f, 1e-6f, 1e-6f, 1e-6f};
    f32x4 acc[4];
    f32x4 nm[2][4][2], dn[2][4];
    auto nd = [&](int jj) {
#pragma unroll
      for (int u = 0; u < 4; u++) {
        dn[jj & 1][u] = mma(as8(kr[4 + (u >> 1)]), as8(pq[jj][u]), eps);
        nm[jj & 1][u][0] = mma16(as4(u32x2{kr[u][0], kr[u][1]}), as4(u32x2{pq[jj][u][0], pq[jj][u][1]}), z4);
        nm[jj & 1][u][1] = mma16(as4(u32x2{kr[u][2], kr[u][3]}), as4(u32x2{pq[jj][u][2], pq[jj][u][3]}), z4);
      }
    };
    u32x4 of[2][4];
    auto sc = [&](int jj) {
#pragma unroll
      for (int u = 0; u < 4; u++) {
        const f32x4 n0 = nm[jj & 1][u][0] * __builtin_amdgcn_rcpf(dn[jj & 1][u][2 * (u & 1)]);
        const f32x4 n1 = nm[jj & 1][u][1] * __builtin_amdgcn_rcpf(dn[jj & 1][u][2 * (u & 1) + 1]);
        of[jj & 1][u] = u32x4{pk(n0[0], n0[1]), pk(n0[2], n0[3]), pk(n1[0], n1[1]), pk(n1[2], n1[3])};
      }
    };
    auto pj = [&](int jj) {
#pragma unroll
      for (int u = 0; u < 4; u++)
#pragma unroll
        for (int a = 0; a < 4; a++) acc[a] = mma(wf[u][a], as8(of[jj & 1][u]), u ? acc[a] : z4);
      if (jj < 3) {
#pragma unroll
        for (int a = 0; a < 4; a++) *(f32x4*)(lds + L_WO + w * 16384 + (jj * 4 + a) * 1024 + lane * 16) = acc[a];
      }
    };
    __builtin_amdgcn_s_setprio(1);
    nd(0); SCHED();
    nd(1); sc(0);
#pragma unroll
    for (int t = 0; t < 12; t++) { SGB(0x008, 1); SGB(0x002, 3); }
    SCHED();
#pragma unroll
    for (int jj = 0; jj < 3; jj++) {
      pj(jj); sc(jj + 1);
#pragma unroll
      for (int t = 0; t < 16; t++) { SGB(0x008, 1); SGB(0x002, 3); }
      SCHED();
      if (jj < 2) { nd(jj + 2); SCHED(); }
    }
    linc(FL + 92, lane);
    pj(3);
    lwait(FL + 92, 4);
#pragma unroll
    for (int ww = 1; ww < 4; ww++)
#pragma unroll
      for (int a = 0; a < 4; a++) acc[a] += *(const f32x4*)(lds + L_WO + ((w + ww) & 3) * 16384 + ((3 - ww) * 4 + a) * 1024 + lane * 16);
    lwait(FL + 48, 1);
    lspin(FL + 48, 4);
    const float ga = bflo(gv), gb = bfhi(gv);
    unsigned pv[4][2];
#pragma unroll
    for (int a = 0; a < 2; a++) {
      const f32x4 t0 = *(const f32x4*)(lds + L_FFT + ((w * 4 + a) * 64 + lane) * 16);
      const f32x4 t1 = *(const f32x4*)(lds + L_FFT + ((w * 4 + a + 2) * 64 + lane) * 16);
      const f32x4 v0 = acc[a] * ga + (t0 + t1) * gb, v1 = acc[a + 2] * ga + (t0 - t1) * gb;
      pv[a][0] = pk(v0[0], v0[1]); pv[a][1] = pk(v0[2], v0[3]);
      pv[a + 2][0] = pk(v1[0], v1[1]); pv[a + 2][1] = pk(v1[2], v1[3]);
    }
#pragma unroll
    for (int a = 0; a < 2; a++) {
      auto s0 = __builtin_amdgcn_permlane16_swap(pv[2 * a][0], pv[2 * a + 1][0], false, false);
      auto s1 = __builtin_amdgcn_permlane16_swap(pv[2 * a][1], pv[2 * a + 1][1], false, false);
      u32x4 v = {s0[0], s1[0], s0[1], s1[1]};
      char* op = out + (size_t)(16 * stg + i) * 1024 + (256 * a + 32 * ntile + 16 * (g & 1) + 8 * (g >> 1)) * 2;
      asm volatile("global_store_dwordx4 %0, %1, off sc1\n\ts_nop 1" :: "v"(op), "v"(v) : "memory");
    }
    return;
  }
  {
    __builtin_amdgcn_s_setprio(0);
    const float sgn = (i & 1) ? -1.f : 1.f;
    const unsigned k0v = (i & 1) ? 0xb5803580u : 0x35803580u;
    const float fn = (float)(32 * ntile + 16 * (w & 1) + i) * (1.f / 512.f);
    const float t2 = 2.f * fcos(fn * 32.f);
    f32x2 c0[4], c1[4];
#pragma unroll
    for (int d = 0; d < 4; d++) {
      const float kf = (float)(4 * g + 128 * (w & 1) + (w >> 1)) + (float)(16 * (d >> 1) + 2 * (d & 1));
      const float a0 = kf * fn, a1 = (kf + 32.f) * fn;
      c0[d] = f32x2{fcos(a0), fsin(a0)} * 0x1p-19f;
      c1[d] = f32x2{fcos(a1), fsin(a1)} * 0x1p-19f;
    }
    u32x4 gf[8];
#pragma unroll
    for (int j = 0; j < 2; j++) {
#pragma unroll
      for (int h = 0; h < 2; h++) {
#pragma unroll
        for (int d = 0; d < 4; d++) {
          gf[4 * j + 2 * h][d] = pk(c0[d][0], c0[d][1]);
          gf[4 * j + 2 * h + 1][d] = pk(c1[d][0], c1[d][1]);
          c0[d] = c1[d] * t2 - c0[d];
        }
#pragma unroll
        for (int d = 0; d < 4; d++) c1[d] = c0[d] * t2 - c1[d];
      }
      gf[4 * j][0] = (w == j && g == 0) ? k0v : gf[4 * j][0];
      if (j == 0) {
        const float sg = (w & 1) ? sgn : 1.f;
#pragma unroll
        for (int d = 0; d < 4; d++) { c0[d] *= sg; c1[d] *= sg; }
      }
    }
#pragma unroll
    for (int k = 0; k < 8; k++) PIN(gf[k]);
    gwait<SLP>(rs, WS_F3 + ((mtile >> 3) * 4 + w) * 128, ep);
    lwait(FL + 80, 4);
    linc(FL + 96, lane);
    const int toffB = L_Y + (4 * g + q) * 128 + (fsw * 32) + p * 8;
    f32x4 accb[4];
    s16x8 tb_[2][2][4];
    auto rd = [&](int u) {
      const char* slot = lds + (w ^ (u >> 1)) * 16384 + (u & 1) * 8192;
#pragma unroll
      for (int ks = 0; ks < 2; ks++)
#pragma unroll
        for (int c = 0; c < 4; c++) tb_[u & 1][ks][c] = trd2(slot + (toffB ^ (32 * c)) + ks * 4096, slot + (toffB ^ (32 * c)) + ks * 4096 + 2048);
    };
    auto mm = [&](int u) {
#pragma unroll
      for (int ks = 0; ks < 2; ks++)
#pragma unroll
        for (int c = 0; c < 4; c++) accb[c] = mma(as8(gf[2 * u + ks]), tb_[u & 1][ks][c], accb[c]);
    };
    {
      const unsigned v = (lane >> 3) * YP + ((((lane & 7) >> 1) ^ ((lane >> 4) & 3)) * 32) + (lane & 1) * 16;
      const char* src = ws + WS_YT + (size_t)(w * 128) * YP + mtile * 128;
      const unsigned la = laddr_of(lds) + L_Y + w * 16384;
#pragma unroll
      for (int j = 0; j < 4; j++) glds4c(la + j * 4096, src + (size_t)j * (32 * YP), v, v + (8 * YP - 1024), v + 2 * (8 * YP - 1024), v + 3 * (8 * YP - 1024));
    }
#pragma unroll
    for (int c = 0; c < 4; c++) accb[c] = f32x4{0, 0, 0, 0};
    WAITV(8);
    rd(0);
    WAITV(0);
    lset(FL + 32 + w * 4);
    rd(1); mm(0);
#pragma unroll
    for (int t = 0; t < 8; t++) { SGB(0x100, 2); SGB(0x008, 1); }
    SCHED();
    lspin(FL + 32 + (w ^ 1) * 4, 1);
    rd(2); mm(1);
#pragma unroll
    for (int t = 0; t < 8; t++) { SGB(0x100, 2); SGB(0x008, 1); }
    SCHED();
    rd(3); mm(2);
#pragma unroll
    for (int t = 0; t < 8; t++) { SGB(0x100, 2); SGB(0x008, 1); }
    SCHED();
    mm(3);
    lwait(FL + 96, 4);
    if (b == 0 && w == 1 && lane == 0) stc1(rs, WS_E, ep);
#pragma unroll
    for (int c = 0; c < 4; c++) *(f32x4*)(lds + L_FFT + ((c * 4 + w) * 64 + lane) * 16) = accb[c];
    linc(FL + 48, lane);
  }
}

extern "C" __global__ void gm_end() {}

extern "C" void kernel_launch(void* const* d_in, const int* in_sizes, int n_in, void* d_out, int out_size, void* d_ws, size_t ws_size, hipStream_t stream) {
  gm_all<<<256, 512, 0, stream>>>((const char*)d_in[0], (const char*)d_in[1], (const char*)d_in[4], (const char*)d_in[5], (const char*)d_in[6],
                                  (const char*)d_in[2], (char*)d_ws, (char*)d_out);
}
```
